```python
import torch

def _softmax(x, axis=-1):
    return torch.softmax(x, dim=axis)

def _gelu(x):
    return torch.nn.functional.gelu(x, approximate='tanh')

def _silu(x):
    return torch.nn.functional.silu(x)

def _sigmoid(x):
    return torch.sigmoid(x)

def _layernorm(x, axis=-1, eps=1e-05):
    m = x.mean(dim=axis, keepdim=True)
    v = x.var(dim=axis, keepdim=True, unbiased=False)
    return (x - m) / torch.sqrt(v + eps)

def _scatter_add(tgt, idx, src, axis=0):
    return tgt.clone().index_add_(axis, idx, src)

def _segment_sum(src, seg, n):
    out = torch.zeros((n,) + src.shape[1:], dtype=src.dtype)
    return out.index_add_(0, seg, src)

def _pad(x, pw):
    flat = tuple((v for pair in reversed(pw) for v in pair))
    return torch.nn.functional.pad(x, flat)
kernel_name = 'standalone__tree_speculative_verify_tree_attention_over_candidate_token_tree__sampling'

def setup_inputs():

    _w = lambda a: (lambda t: t.to(torch.bfloat16) if t.is_floating_point() else t)(torch.as_tensor(a))
    B, Hq, Hkv, T, C, D = (32, 32, 4, 32, 2048, 128)
    tree_mask = torch.zeros((B, T, T), dtype=float)
    for b in range(B):
        parent = torch.zeros(T, dtype=torch.int64)
        for i in range(1, T):
            parent[i] = int(torch.randint(0, i, (1,)).item())
        for i in range(T):
            j = i
            tree_mask[b, i, j] = 1.0
            while j > 0:
                j = parent[j]
                tree_mask[b, i, j] = 1.0
    return {'q': _w((lambda *s: torch.randn(*s, dtype=torch.bfloat16) * 0.8)(B, Hq, T, D)), 'k_cache': _w((lambda *s: torch.randn(*s, dtype=torch.bfloat16) * 0.8)(B, Hkv, C, D)), 'v_cache': _w((lambda *s: torch.randn(*s, dtype=torch.bfloat16) * 0.05)(B, Hkv, C, D)), 'k_tree': _w((lambda *s: torch.randn(*s, dtype=torch.bfloat16) * 0.8)(B, Hkv, T, D)), 'v_tree': _w((lambda *s: torch.randn(*s, dtype=torch.bfloat16) * 0.05)(B, Hkv, T, D)), 'tree_mask': _w(tree_mask)}

def reference(q, k_cache, v_cache, k_tree, v_tree, tree_mask):
    B, Hq, T, D = q.shape
    Hkv, C = (k_cache.shape[1], k_cache.shape[2])
    rep = Hq // Hkv
    kc = (lambda x, n, axis: torch.repeat_interleave(x, n, dim=axis))(k_cache, rep, axis=1)
    vc = (lambda x, n, axis: torch.repeat_interleave(x, n, dim=axis))(v_cache, rep, axis=1)
    kt = (lambda x, n, axis: torch.repeat_interleave(x, n, dim=axis))(k_tree, rep, axis=1)
    vt = (lambda x, n, axis: torch.repeat_interleave(x, n, dim=axis))(v_tree, rep, axis=1)
    k = torch.concatenate([kc, kt], axis=2)
    v = torch.concatenate([vc, vt], axis=2)
    scale = D ** (-0.5)
    scores = torch.einsum('bhqd,bhkd->bhqk', q, k) * scale
    ctx_mask = torch.ones((B, 1, T, C), dtype=scores.dtype)
    tm = tree_mask[:, None, :, :].clone()
    full_mask = torch.concatenate([ctx_mask, tm], axis=-1)
    scores = scores + (1.0 - full_mask) * -1000000000.0
    p = _softmax(scores, axis=-1)
    return torch.einsum('bhqk,bhkd->bhqd', p, v)
```

```cpp
#include <hip/hip_runtime.h>
#include <hip/hip_bf16.h>
constexpr int B=32,HQ=32,HKV=4,T=32,C=2048,D=128;
__device__ inline float bf2f(unsigned short u){ return __uint_as_float(((unsigned)u)<<16); }
__global__ __launch_bounds__(256) void naive_k(const unsigned short* q,const unsigned short* kc,const unsigned short* vc,const unsigned short* kt,const unsigned short* vt,const unsigned short* tm, __hip_bfloat16* out){
  __shared__ float Qs[32][129];
  __shared__ float S[32][257];
  __shared__ float mrow[32], lrow[32], arow[32];
  int bh=blockIdx.x; int b=bh/HQ, h=bh%HQ, kvh=h/(HQ/HKV); int tid=threadIdx.x;
  for(int i=tid;i<32*128;i+=256) Qs[i/128][i%128]=bf2f(q[(size_t)bh*32*128+i]);
  if(tid<32){mrow[tid]=-INFINITY; lrow[tid]=0;}
  float o[16]; for(int i=0;i<16;i++) o[i]=0;
  __syncthreads();
  const float scale=0.08838834764831845f;
  for(int c0=0;c0<C+256;c0+=256){
    int key=c0+tid; bool valid = key < C+T;
    const unsigned short* kr = key<C ? kc+((size_t)(b*HKV+kvh)*C+key)*D : kt+((size_t)(b*HKV+kvh)*T+(valid?key-C:0))*D;
    float s[32]; for(int r=0;r<32;r++) s[r]=0;
    for(int d=0;d<128;d++){ float kv=bf2f(kr[d]);
      #pragma unroll
      for(int r=0;r<32;r++) s[r]+=Qs[r][d]*kv; }
    #pragma unroll
    for(int r=0;r<32;r++){ float v=s[r]*scale; if(key>=C){ if(valid) v+= (1.0f-bf2f(tm[((size_t)b*T+r)*T+(key-C)]))*-1e9f; else v=-INFINITY;} S[r][tid]=v; }
    __syncthreads();
    if(tid<32){ float m=mrow[tid]; for(int j=0;j<256;j++) m=fmaxf(m,S[tid][j]); float a=__expf(mrow[tid]-m); float l=0; for(int j=0;j<256;j++){ float p=__expf(S[tid][j]-m); S[tid][j]=p; l+=p;} lrow[tid]=lrow[tid]*a+l; mrow[tid]=m; arow[tid]=a; }
    __syncthreads();
    int d=tid&127, rg=(tid>>7)*16;
    #pragma unroll
    for(int i=0;i<16;i++) o[i]*=arow[rg+i];
    int nk = (c0<C)?256:T;
    for(int j=0;j<nk;j++){ int kk=c0+j; const unsigned short* vr = kk<C ? vc+((size_t)(b*HKV+kvh)*C+kk)*D : vt+((size_t)(b*HKV+kvh)*T+(kk-C))*D; float vv=bf2f(vr[d]);
      #pragma unroll
      for(int i=0;i<16;i++) o[i]+=S[rg+i][j]*vv; }
    __syncthreads();
  }
  int d=tid&127, rg=(tid>>7)*16;
  for(int i=0;i<16;i++) out[((size_t)bh*32+rg+i)*128+d]=__float2bfloat16(o[i]/lrow[rg+i]);
}
extern "C" void kernel_launch(void* const* d_in, const int* in_sizes, int n_in, void* d_out, int out_size, void* d_ws, size_t ws_size, hipStream_t stream) {
  naive_k<<<B*HQ,256,0,stream>>>((const unsigned short*)d_in[0],(const unsigned short*)d_in[1],(const unsigned short*)d_in[2],(const unsigned short*)d_in[3],(const unsigned short*)d_in[4],(const unsigned short*)d_in[5],(__hip_bfloat16*)d_out);
}
```

```cpp
#include <hip/hip_runtime.h>
#include <hip/hip_bf16.h>
constexpr int B=32,HQ=32,HKV=4,T=32,C=2048,D=128;
__device__ inline float bf2f(unsigned short u){ return __uint_as_float(((unsigned)u)<<16); }
__global__ __launch_bounds__(256) void naive_k(const unsigned short* q,const unsigned short* kc,const unsigned short* vc,const unsigned short* kt,const unsigned short* vt,const unsigned short* tm, __hip_bfloat16* out){
  __shared__ float Qs[32][129];
  __shared__ float S[32][257];
  __shared__ float mrow[32], lrow[32], arow[32];
  int bh=blockIdx.x; int b=bh/HQ, h=bh%HQ, kvh=h/(HQ/HKV); int tid=threadIdx.x;
  for(int i=tid;i<32*128;i+=256) Qs[i/128][i%128]=bf2f(q[(size_t)bh*32*128+i]);
  if(tid<32){mrow[tid]=-INFINITY; lrow[tid]=0;}
  float o[16]; for(int i=0;i<16;i++) o[i]=0;
  __syncthreads();
  const float scale=0.08838834764831845f;
  for(int c0=0;c0<C+256;c0+=256){
    int key=c0+tid; bool valid = key < C+T;
    const unsigned short* kr = key<C ? kc+((size_t)(b*HKV+kvh)*C+key)*D : kt+((size_t)(b*HKV+kvh)*T+(valid?key-C:0))*D;
    float s[32]; for(int r=0;r<32;r++) s[r]=0;
    for(int d=0;d<128;d++){ float kv=bf2f(kr[d]);
      #pragma unroll
      for(int r=0;r<32;r++) s[r]+=Qs[r][d]*kv; }
    #pragma unroll
    for(int r=0;r<32;r++){ float v=s[r]*scale; if(key>=C){ if(valid) v+= (1.0f-bf2f(tm[((size_t)b*T+r)*T+(key-C)]))*-1e9f; else v=-INFINITY;} S[r][tid]=v; }
    __syncthreads();
    if(tid<32){ float m=mrow[tid]; for(int j=0;j<256;j++) m=fmaxf(m,S[tid][j]); float a=__expf(mrow[tid]-m); float l=0; for(int j=0;j<256;j++){ float p=__expf(S[tid][j]-m); S[tid][j]=p; l+=p;} lrow[tid]=lrow[tid]*a+l; mrow[tid]=m; arow[tid]=a; }
    __syncthreads();
    int d=tid&127, rg=(tid>>7)*16;
    #pragma unroll
    for(int i=0;i<16;i++) o[i]*=arow[rg+i];
    int nk = (c0<C)?256:T;
    for(int j=0;j<nk;j++){ int kk=c0+j; const unsigned short* vr = kk<C ? vc+((size_t)(b*HKV+kvh)*C+kk)*D : vt+((size_t)(b*HKV+kvh)*T+(kk-C))*D; float vv=bf2f(vr[d]);
      #pragma unroll
      for(int i=0;i<16;i++) o[i]+=S[rg+i][j]*vv; }
    __syncthreads();
  }
  int d=tid&127, rg=(tid>>7)*16;
  for(int i=0;i<16;i++) out[((size_t)bh*32+rg+i)*128+d]=__float2bfloat16(o[i]/lrow[rg+i]);
}

template<int NT>
__global__ __launch_bounds__(256,1) void probe_k(const char* K, const char* V) {
  __shared__ __attribute__((aligned(16))) char lds[65536];
  const unsigned tid = threadIdx.x, lane = tid & 63u; const unsigned wid = __builtin_amdgcn_readfirstlane(tid >> 6);
  const unsigned blk = blockIdx.x;
  const char* Kb = K + (size_t)blk * C * D; const char* Vb = V + (size_t)blk * C * D;
  __amdgpu_buffer_rsrc_t sk = __builtin_amdgcn_make_buffer_rsrc((void*)Kb, (short)0, C*D, 0x00020000);
  __amdgpu_buffer_rsrc_t sv = __builtin_amdgcn_make_buffer_rsrc((void*)Vb, (short)0, C*D, 0x00020000);
  unsigned ldsb = (unsigned)(size_t)lds;
  unsigned voff = lane * 16u;
  for (int rep = 0; rep < 1; ++rep)
  for (int s = 0; s < 16; ++s) {
    #pragma unroll
    for (int p = 0; p < 8; ++p) {
      unsigned so = (unsigned)s * 16384u + wid * 4096u + (unsigned)(p & 3) * 1024u;
      unsigned m0 = ldsb + wid * 16384u + ((s & 1) * 8 + p) * 1024u;
      so = __builtin_amdgcn_readfirstlane(so); m0 = __builtin_amdgcn_readfirstlane(m0);
      if (NT) {
      if (p < 4) asm volatile("s_mov_b32 m0, %0\n\ts_nop 0\n\tbuffer_load_dwordx4 %1, %2, %3 offen nt lds" :: "s"(m0), "v"(voff), "s"(sk), "s"(so) : "memory");
      else asm volatile("s_mov_b32 m0, %0\n\ts_nop 0\n\tbuffer_load_dwordx4 %1, %2, %3 offen nt lds" :: "s"(m0), "v"(voff), "s"(sv), "s"(so) : "memory");
      } else {
      if (p < 4) asm volatile("s_mov_b32 m0, %0\n\ts_nop 0\n\tbuffer_load_dwordx4 %1, %2, %3 offen lds" :: "s"(m0), "v"(voff), "s"(sk), "s"(so) : "memory");
      else asm volatile("s_mov_b32 m0, %0\n\ts_nop 0\n\tbuffer_load_dwordx4 %1, %2, %3 offen lds" :: "s"(m0), "v"(voff), "s"(sv), "s"(so) : "memory");
      }
      asm volatile("s_waitcnt vmcnt(15)" ::: "memory");
    }
  }
  asm volatile("s_waitcnt vmcnt(0)" ::: "memory");
}

extern "C" void kernel_launch(void* const* d_in, const int* in_sizes, int n_in, void* d_out, int out_size, void* d_ws, size_t ws_size, hipStream_t stream) {
  probe_k<0><<<256,256,0,stream>>>((const char*)d_in[1],(const char*)d_in[2]);
  naive_k<<<B*HQ,256,0,stream>>>((const unsigned short*)d_in[0],(const unsigned short*)d_in[1],(const unsigned short*)d_in[2],(const unsigned short*)d_in[3],(const unsigned short*)d_in[4],(const unsigned short*)d_in[5],(__hip_bfloat16*)d_out);
}
```

```cpp
#include <hip/hip_runtime.h>
#include <hip/hip_bf16.h>
#include <utility>

#define KV_POL "nt"
typedef float    f32x16 __attribute__((ext_vector_type(16)));
typedef float    f32x2  __attribute__((ext_vector_type(2)));
typedef float    f32x4  __attribute__((ext_vector_type(4)));
typedef __bf16   bf16x8 __attribute__((ext_vector_type(8)));
typedef __bf16   bf16x4 __attribute__((ext_vector_type(4)));
typedef __bf16   bf16x2 __attribute__((ext_vector_type(2)));
typedef unsigned u32x4  __attribute__((ext_vector_type(4)));
typedef unsigned u32x2  __attribute__((ext_vector_type(2)));
template <int... Is> using iseq = std::integer_sequence<int, Is...>;
template <int N> using make_iseq = std::make_integer_sequence<int, N>;

constexpr int NSTEP = 16;
constexpr float THR = 8.0f;
constexpr float CSC = 0.12751743082f;
constexpr float MASKC = -1442695040.888963f;
#define QSTAGE 0x20000u
#define TREE_V 0xC000u
#define TREE_K 0xE000u
#define SRD_WORD3 0x00020000
#define DI __device__ __forceinline__
#define GAP() __builtin_amdgcn_sched_barrier(0)
#define ACLOB_HI "a128","a129","a130","a131","a132","a133","a134","a135","a136","a137","a138","a139","a140","a141","a142","a143","a144","a145","a146","a147","a148","a149","a150","a151","a152","a153","a154","a155","a156","a157","a158","a159","a160","a161","a162","a163","a164","a165","a166","a167","a168","a169","a170","a171","a172","a173","a174","a175","a176","a177","a178","a179","a180","a181","a182","a183","a184","a185","a186","a187","a188","a189","a190","a191","a192","a193","a194","a195","a196","a197","a198","a199","a200","a201","a202","a203","a204","a205","a206","a207","a208","a209","a210","a211","a212","a213","a214","a215","a216","a217","a218","a219","a220","a221","a222","a223","a224","a225","a226","a227","a228","a229","a230","a231","a232","a233","a234","a235","a236","a237","a238","a239","a240","a241","a242","a243","a244","a245","a246","a247","a248","a249","a250","a251","a252","a253","a254","a255"
#define ACLOB "a0","a1","a2","a3","a4","a5","a6","a7","a8","a9","a10","a11","a12","a13","a14","a15","a16","a17","a18","a19","a20","a21","a22","a23","a24","a25","a26","a27","a28","a29","a30","a31","a32","a33","a34","a35","a36","a37","a38","a39","a40","a41","a42","a43","a44","a45","a46","a47","a48","a49","a50","a51","a52","a53","a54","a55","a56","a57","a58","a59","a60","a61","a62","a63","a64","a65","a66","a67","a68","a69","a70","a71","a72","a73","a74","a75","a76","a77","a78","a79","a80","a81","a82","a83","a84","a85","a86","a87","a88","a89","a90","a91","a92","a93","a94","a95","a96","a97","a98","a99","a100","a101","a102","a103","a104","a105","a106","a107","a108","a109","a110","a111","a112","a113","a114","a115","a116","a117","a118","a119","a120","a121","a122","a123","a124","a125","a126","a127"

DI unsigned rfl(unsigned x) { return __builtin_amdgcn_readfirstlane(x); }
DI unsigned cvtpk(float lo, float hi) { f32x2 v = {lo, hi}; bf16x2 b = __builtin_convertvector(v, bf16x2); return __builtin_bit_cast(unsigned, b); }
DI float max3f(float a, float b, float c) { float r; asm("v_max3_f32 %0, %1, %2, %3" : "=v"(r) : "v"(a), "v"(b), "v"(c)); return r; }
DI float max2f(float a, float b) { float r; asm("v_max_f32_e32 %0, %1, %2" : "=v"(r) : "v"(a), "v"(b)); return r; }
DI float fadd_s(float a, float b) { float r; asm("v_add_f32_e32 %0, %1, %2" : "=v"(r) : "v"(a), "v"(b)); return r; }
DI float fmul_s(float a, float b) { float r; asm("v_mul_f32_e32 %0, %1, %2" : "=v"(r) : "v"(a), "v"(b)); return r; }
DI float fsub_s(float a, float b) { float r; asm("v_sub_f32_e32 %0, %1, %2" : "=v"(r) : "v"(a), "v"(b)); return r; }
DI float w4_aone() { unsigned l; asm volatile("v_mbcnt_lo_u32_b32 %0, -1, 0\n\tv_mbcnt_hi_u32_b32 %0, -1, %0" : "=v"(l)); return l < 32u ? 1.f : 0.f; }
DI void negm_bcast(f32x16& negm, float aone, float neg_mhat) {
  asm volatile("s_nop 1\n\tv_mfma_f32_32x32x2_f32 %0, %1, %2, 0" : "+v"(negm) : "v"(aone), "v"(neg_mhat));
}

struct W4C {
  unsigned vkb[4], vvb, vksrc[2], vvsrc, m0w, sofw;
  __amdgpu_buffer_rsrc_t srdK, srdV;
  f32x16 S[2][2][2];
  f32x16 NEGM[2];
  f32x16 St;
  bf16x8 Vf[4][4];
  bf16x8 Pk[2][4];
  bf16x4 vlo[4][4], vup[4][4];
  float mhat[2], lsum[2], fct[2]; bool pend;
  float rs0[2], rs1[2], pmx0[2], pmx1[2], rmax[2];
  unsigned sof[2];
};

constexpr int AO_(int db, int qb) { return 16 * (2 * db + qb); }
constexpr int AQ_(int qb, int d0) { return 128 + 32 * qb + 4 * d0; }
constexpr int AK_(int kb, int d0) { return 192 + 32 * kb + 4 * d0; }

template <int MC, int IMM> DI void dma1(unsigned m0base, unsigned voff, __amdgpu_buffer_rsrc_t srd, unsigned soff) {
  asm volatile("s_add_u32 m0, %0, %4\n\ts_nop 0\n\tbuffer_load_dwordx4 %1, %2, %3 offen offset:%5 " KV_POL " lds"
               :: "s"(m0base), "v"(voff), "s"(srd), "s"(soff), "i"(MC), "i"(IMM) : "scc", "memory");
}
constexpr int PIMM(int k) { return 2048 * (k >> 1) + 128 * (k & 1); }
constexpr int PM0(int k) { return 896 * (k & 1); }
template <int ISV, int SL, int I> DI void dma_piece(W4C& c) {
  constexpr int tp = I >> 2, k = I & 3;
  dma1<0x10000 * tp + 0x8000 * ISV + 0x4000 * SL + PM0(k), PIMM(k)>(c.m0w, ISV ? c.vvsrc : c.vksrc[k >> 1], ISV ? c.srdV : c.srdK, c.sof[tp]);
}

template <int I, int VOFF> DI void tr_read(W4C& c, unsigned base) {
  constexpr int ks = I >> 3, db = (I >> 1) & 3;
  if constexpr ((I & 1) == 0) asm volatile("ds_read_b64_tr_b16 %0, %1 offset:%2" : "=v"(c.vlo[db][ks]) : "v"(base), "i"(VOFF + 4096 * ks + 512 * db) : "memory");
  else                        asm volatile("ds_read_b64_tr_b16 %0, %1 offset:%2" : "=v"(c.vup[db][ks]) : "v"(base), "i"(VOFF + 4096 * ks + 2048 + 512 * db) : "memory");
}
template <int I, int SL> DI void k_read(W4C& c) {
  constexpr int kb = I >> 3, d0 = I & 7;
  asm volatile("ds_read_b128 a[%c1:%c2], %0 offset:%c3" :: "v"(c.vkb[d0 & 3]), "i"(AK_(kb, d0)), "i"(AK_(kb, d0) + 3), "i"(0x4000 * SL + 8192 * kb + 1024 * (d0 >> 2)) : "memory", ACLOB_HI);
}
template <int I> DI void kt_read(const unsigned (&b4)[4]) {
  asm volatile("ds_read_b128 a[%c1:%c2], %0 offset:%c3" :: "v"(b4[I & 3]), "i"(AK_(0, I)), "i"(AK_(0, I) + 3), "i"(1024 * (I >> 2)) : "memory", ACLOB_HI);
}
template <int D0> DI bf16x8 q_ldread(unsigned base) {
  bf16x8 r; asm volatile("ds_read_b128 %0, %1 offset:%2" : "=v"(r) : "v"(base), "i"(1024 * (D0 >> 2)) : "memory"); return r;
}
template <int I> DI void q_write1(const u32x4& w) {
  constexpr int aq = AQ_(I >> 3, I & 7);
  asm volatile("v_accvgpr_write_b32 a[%c4], %0\n\tv_accvgpr_write_b32 a[%c5], %1\n\tv_accvgpr_write_b32 a[%c6], %2\n\tv_accvgpr_write_b32 a[%c7], %3"
               :: "v"(w[0]), "v"(w[1]), "v"(w[2]), "v"(w[3]), "i"(aq), "i"(aq + 1), "i"(aq + 2), "i"(aq + 3) : ACLOB_HI);
}
template <int X, bool FIRST, int M> DI void qk_mfma(W4C& c) {
  constexpr int d0 = M >> 2, kb = (M >> 1) & 1, qb = M & 1, ak = AK_(kb, d0), aq = AQ_(qb, d0);
  if constexpr (d0 == 0) {
    if constexpr (FIRST) asm volatile("v_mfma_f32_32x32x16_bf16 %0, a[%c1:%c2], a[%c3:%c4], 0" : "=&v"(c.S[X][kb][qb]) : "i"(ak), "i"(ak + 3), "i"(aq), "i"(aq + 3) : ACLOB, ACLOB_HI);
    else                 asm volatile("v_mfma_f32_32x32x16_bf16 %0, a[%c2:%c3], a[%c4:%c5], %1" : "=&v"(c.S[X][kb][qb]) : "v"(c.NEGM[qb]), "i"(ak), "i"(ak + 3), "i"(aq), "i"(aq + 3) : ACLOB, ACLOB_HI);
  } else asm volatile("v_mfma_f32_32x32x16_bf16 %0, a[%c1:%c2], a[%c3:%c4], %0" : "+v"(c.S[X][kb][qb]) : "i"(ak), "i"(ak + 3), "i"(aq), "i"(aq + 3) : ACLOB, ACLOB_HI);
}
template <int D0> DI void qkt_mfma(W4C& c) {
  constexpr int ak = AK_(0, D0), aq = AQ_(0, D0);
  asm volatile("v_mfma_f32_32x32x16_bf16 %0, a[%c1:%c2], a[%c3:%c4], %0" : "+v"(c.St) : "i"(ak), "i"(ak + 3), "i"(aq), "i"(aq + 3) : ACLOB, ACLOB_HI);
}
template <int KS, int DB, int QB, bool ZERO> DI void pv_mfma_(W4C& c) {
  constexpr int ao = AO_(DB, QB);
  if constexpr (ZERO) asm volatile("v_mfma_f32_32x32x16_bf16 a[%c2:%c3], %0, %1, 0" :: "v"(c.Vf[DB][KS]), "v"(c.Pk[QB][KS]), "i"(ao), "i"(ao + 15) : ACLOB);
  else                asm volatile("v_mfma_f32_32x32x16_bf16 a[%c2:%c3], %0, %1, a[%c2:%c3]" :: "v"(c.Vf[DB][KS]), "v"(c.Pk[QB][KS]), "i"(ao), "i"(ao + 15) : ACLOB);
}
template <int M, bool ZERO1> DI void pv_mfma(W4C& c) {
  constexpr int ks = M >> 3, db = (M >> 1) & 3, qb = M & 1;
  pv_mfma_<ks, db, qb, ZERO1 && ks == 0 && qb == 1>(c);
}
template <int X, int QB, int KS, int P> DI void pack1(W4C& c) {
  u32x4 w = __builtin_bit_cast(u32x4, c.Pk[QB][KS]);
  w[P] = cvtpk(c.S[X][KS >> 1][QB][8 * (KS & 1) + 2 * P], c.S[X][KS >> 1][QB][8 * (KS & 1) + 2 * P + 1]);
  c.Pk[QB][KS] = __builtin_bit_cast(bf16x8, w);
}
template <int X, int J> DI void rowsum1(W4C& c) {
  constexpr int qb = J >> 4, ks = (J >> 2) & 3, p = J & 3;
  c.rs0[qb] = fadd_s(c.rs0[qb], c.S[X][ks >> 1][qb][8 * (ks & 1) + 2 * p]); c.rs1[qb] = fadd_s(c.rs1[qb], c.S[X][ks >> 1][qb][8 * (ks & 1) + 2 * p + 1]);
}

#define W4_VLO "+v"(c.vlo[0][0]), "+v"(c.vlo[0][1]), "+v"(c.vlo[0][2]), "+v"(c.vlo[0][3]), "+v"(c.vlo[1][0]), "+v"(c.vlo[1][1]), "+v"(c.vlo[1][2]), "+v"(c.vlo[1][3]), \
               "+v"(c.vlo[2][0]), "+v"(c.vlo[2][1]), "+v"(c.vlo[2][2]), "+v"(c.vlo[2][3]), "+v"(c.vlo[3][0]), "+v"(c.vlo[3][1]), "+v"(c.vlo[3][2]), "+v"(c.vlo[3][3])
#define W4_VUP "+v"(c.vup[0][0]), "+v"(c.vup[0][1]), "+v"(c.vup[0][2]), "+v"(c.vup[0][3]), "+v"(c.vup[1][0]), "+v"(c.vup[1][1]), "+v"(c.vup[1][2]), "+v"(c.vup[1][3]), \
               "+v"(c.vup[2][0]), "+v"(c.vup[2][1]), "+v"(c.vup[2][2]), "+v"(c.vup[2][3]), "+v"(c.vup[3][0]), "+v"(c.vup[3][1]), "+v"(c.vup[3][2]), "+v"(c.vup[3][3])
#define W4_VT "+v"(c.vlo[0][0]), "+v"(c.vlo[0][1]), "+v"(c.vlo[1][0]), "+v"(c.vlo[1][1]), "+v"(c.vlo[2][0]), "+v"(c.vlo[2][1]), "+v"(c.vlo[3][0]), "+v"(c.vlo[3][1]), \
              "+v"(c.vup[0][0]), "+v"(c.vup[0][1]), "+v"(c.vup[1][0]), "+v"(c.vup[1][1]), "+v"(c.vup[2][0]), "+v"(c.vup[2][1]), "+v"(c.vup[3][0]), "+v"(c.vup[3][1])

template <int X, bool FIRST, bool DK, int M> DI void gapA(W4C& c, unsigned vtb) {
  constexpr int Xp = X ^ 1;
  qk_mfma<X, FIRST, M>(c);
  if constexpr (DK && M >= 16 && (M & 1) == 0) dma_piece<0, X, (M - 16) / 2>(c);
  if constexpr (!FIRST && M == 8) asm volatile("" :: "v"(c.NEGM[0]), "v"(c.NEGM[1]));
  if constexpr (FIRST) {
    if constexpr (M < 16) tr_read<M, 0>(c, vtb);
  } else {
    constexpr int base = M + (M >= 4) + (M >= 8) + (M >= 12) + (M >= 16) + (M >= 20) + (M >= 24);
    if constexpr (M <= 25) tr_read<base, 0x8000 + 0x4000 * Xp>(c, c.vvb);
    if constexpr (M == 3 || M == 7 || M == 11 || M == 15 || M == 19 || M == 23) tr_read<base + 1, 0x8000 + 0x4000 * Xp>(c, c.vvb);
    if constexpr (M < 16) {
      constexpr int e0 = 2 * M, e1 = 2 * M + 1;
      c.S[Xp][1][e0 >> 4][e0 & 15] = __builtin_amdgcn_exp2f(c.S[Xp][1][e0 >> 4][e0 & 15]);
      c.S[Xp][1][e1 >> 4][e1 & 15] = __builtin_amdgcn_exp2f(c.S[Xp][1][e1 >> 4][e1 & 15]);
      pack1<Xp, (M >> 3), ((M >> 2) & 1), (M & 3)>(c);
    } else {
      constexpr int cc = M - 16;
      pack1<Xp, (cc >> 3), 2 + ((cc >> 2) & 1), (cc & 3)>(c);
      rowsum1<Xp, 16 * (cc >> 3) + 8 + 4 * ((cc >> 2) & 1) + (cc & 3)>(c);
    }
  }
  GAP();
}
template <int X, bool FIRST, bool DK, int VMC, int... Ms> DI void phaseA_(W4C& c, const int t, unsigned vtb, iseq<Ms...>) {
  if constexpr (DK) { c.sof[0] = c.sofw + 32768u * (unsigned)(t + 2); c.sof[1] = c.sof[0] + 16384u; asm volatile("" : "+s"(c.sof[0]), "+s"(c.sof[1])); }
  (gapA<X, FIRST, DK, Ms>(c, vtb), ...);
  if constexpr (!FIRST) {
    #pragma unroll
    for (int qb = 0; qb < 2; ++qb) c.lsum[qb] = fadd_s(c.lsum[qb], fadd_s(c.rs0[qb], c.rs1[qb]));
  }
  if constexpr (FIRST) { asm volatile("s_waitcnt vmcnt(%16) lgkmcnt(0)\n\ts_barrier" : W4_VT : "i"(VMC) : "memory"); }
  else { asm volatile("s_waitcnt vmcnt(%16) lgkmcnt(0)" : W4_VLO : "i"(VMC) : "memory"); asm volatile("s_barrier" : W4_VUP :: "memory"); }
  GAP();
  #pragma unroll
  for (int ks = 0; ks < (FIRST ? 2 : 4); ++ks)
    #pragma unroll
    for (int db = 0; db < 4; ++db) c.Vf[db][ks] = __builtin_shufflevector(c.vlo[db][ks], c.vup[db][ks], 0, 1, 2, 3, 4, 5, 6, 7);
}
template <int X, bool FIRST, bool DK, int VMC> DI void phaseA(W4C& c, const int t, unsigned vtb = 0) { phaseA_<X, FIRST, DK, VMC>(c, t, vtb, make_iseq<32>{}); }

template <int I> DI void o_scale1(float f0, float f1) {
  float t;
  asm volatile("v_accvgpr_read_b32 %0, a[%c2]\n\tv_mul_f32 %0, %1, %0\n\tv_accvgpr_write_b32 a[%c2], %0" : "=&v"(t) : "v"(((I >> 4) & 1) ? f1 : f0), "i"(I) : ACLOB);
}
template <int... Is> DI void o_scale(float f0, float f1, iseq<Is...>) { (o_scale1<Is>(f0, f1), ...); }

template <int X, bool PEEL, bool ZERO1, bool DV, bool KR, int M> DI void gapB(W4C& c) {
  if constexpr (!PEEL) pv_mfma<M, ZERO1>(c);
  if constexpr (DV && M < 8) dma_piece<1, X ^ 1, M>(c);
  if constexpr (M >= 1 && M <= 16 && KR) k_read<M - 1, X ^ 1>(c);
  if constexpr (M >= 8 && M < 16) {
    constexpr int i = M - 8, q = i >> 2, r = 4 * (i & 3);
    if constexpr ((i & 3) == 0) { c.pmx0[q] = max3f(c.S[X][0][q][0], c.S[X][0][q][1], c.S[X][1][q][0]); c.pmx1[q] = max3f(c.S[X][0][q][2], c.S[X][0][q][3], c.S[X][1][q][1]);
                                  c.pmx0[q] = max3f(c.pmx0[q], c.S[X][1][q][2], c.S[X][1][q][3]); }
    else                        { c.pmx0[q] = max3f(c.pmx0[q], c.S[X][0][q][r], c.S[X][0][q][r + 1]); c.pmx1[q] = max3f(c.pmx1[q], c.S[X][0][q][r + 2], c.S[X][0][q][r + 3]);
                                  c.pmx0[q] = max3f(c.pmx0[q], c.S[X][1][q][r], c.S[X][1][q][r + 1]); c.pmx1[q] = max3f(c.pmx1[q], c.S[X][1][q][r + 2], c.S[X][1][q][r + 3]); }
    if constexpr (PEEL && q == 0) { c.pmx0[0] = max3f(c.pmx0[0], c.St[r], c.St[r + 1]); c.pmx1[0] = max3f(c.pmx1[0], c.St[r + 2], c.St[r + 3]); }
  }
  if constexpr (M == 16 || M == 17) {
    constexpr int q = M - 16; const float mm = max2f(c.pmx0[q], c.pmx1[q]);
    auto pr = __builtin_amdgcn_permlane32_swap(__float_as_uint(mm), __float_as_uint(mm), false, false);
    c.rmax[q] = max2f(__uint_as_float(pr[0]), __uint_as_float(pr[1]));
  }
  if constexpr (M == 18) {
    if constexpr (PEEL) {
      #pragma unroll
      for (int q = 0; q < 2; ++q) { c.mhat[q] = c.rmax[q]; c.fct[q] = 1.f;
        #pragma unroll
        for (int kb = 0; kb < 2; ++kb) { _Pragma("unroll") for (int e = 0; e < 16; ++e) c.S[X][kb][q][e] = fsub_s(c.S[X][kb][q][e], c.rmax[q]); }
        negm_bcast(c.NEGM[q], w4_aone(), -c.mhat[q]);
      }
      float s0 = 0.f, s1 = 0.f;
      #pragma unroll
      for (int e = 0; e < 16; ++e) c.St[e] = __builtin_amdgcn_exp2f(fsub_s(c.St[e], c.rmax[0]));
      #pragma unroll
      for (int e = 0; e < 16; e += 2) { s0 = fadd_s(s0, c.St[e]); s1 = fadd_s(s1, c.St[e + 1]); }
      c.lsum[0] = fadd_s(s0, s1); c.lsum[1] = 0.f;
      #pragma unroll
      for (int ks = 0; ks < 2; ++ks) { u32x4 w;
        #pragma unroll
        for (int p = 0; p < 4; ++p) w[p] = cvtpk(c.St[8 * ks + 2 * p], c.St[8 * ks + 2 * p + 1]);
        c.Pk[0][ks] = __builtin_bit_cast(bf16x8, w); }
      asm volatile("s_nop 1" : "+v"(c.Pk[0][0]), "+v"(c.Pk[0][1]));
    } else {
      const bool grew = (c.rmax[0] > THR) | (c.rmax[1] > THR);
      if (__builtin_expect(__any((int)grew), 0)) {
        #pragma unroll
        for (int q = 0; q < 2; ++q) { const float dl = max2f(c.rmax[q], 0.f); c.mhat[q] = fadd_s(c.mhat[q], dl); c.fct[q] = __builtin_amdgcn_exp2f(-dl);
          #pragma unroll
          for (int kb = 0; kb < 2; ++kb) { _Pragma("unroll") for (int e = 0; e < 16; ++e) c.S[X][kb][q][e] = fsub_s(c.S[X][kb][q][e], dl); }
          negm_bcast(c.NEGM[q], w4_aone(), -c.mhat[q]);
        }
        c.pend = true;
      }
    }
  }
  if constexpr (PEEL && M >= 19 && M < 27) pv_mfma_<((M - 19) >> 2), ((M - 19) & 3), 0, (M - 19) < 4>(c);
  if constexpr (M == 18) { c.rs0[0] = c.rs0[1] = c.rs1[0] = c.rs1[1] = 0.f; }
  if constexpr (M >= 19) {
    constexpr int g = M - 19, e_lo = (32 * g) / 13, e_hi = (32 * (g + 1)) / 13;
    {
      constexpr int p_lo = g ? (32 * (g - 1)) / 13 : 0, p_hi = g ? (32 * g) / 13 : 0;
      if constexpr (p_lo + 0 < p_hi) { constexpr int e = p_lo + 0; if constexpr (e & 1) c.rs1[e >> 4] = fadd_s(c.rs1[e >> 4], c.S[X][0][e >> 4][e & 15]); else c.rs0[e >> 4] = fadd_s(c.rs0[e >> 4], c.S[X][0][e >> 4][e & 15]); }
      if constexpr (p_lo + 1 < p_hi) { constexpr int e = p_lo + 1; if constexpr (e & 1) c.rs1[e >> 4] = fadd_s(c.rs1[e >> 4], c.S[X][0][e >> 4][e & 15]); else c.rs0[e >> 4] = fadd_s(c.rs0[e >> 4], c.S[X][0][e >> 4][e & 15]); }
      if constexpr (p_lo + 2 < p_hi) { constexpr int e = p_lo + 2; if constexpr (e & 1) c.rs1[e >> 4] = fadd_s(c.rs1[e >> 4], c.S[X][0][e >> 4][e & 15]); else c.rs0[e >> 4] = fadd_s(c.rs0[e >> 4], c.S[X][0][e >> 4][e & 15]); }
    }
    if constexpr (e_lo + 0 < e_hi) c.S[X][0][(e_lo + 0) >> 4][(e_lo + 0) & 15] = __builtin_amdgcn_exp2f(c.S[X][0][(e_lo + 0) >> 4][(e_lo + 0) & 15]);
    if constexpr (e_lo + 1 < e_hi) c.S[X][0][(e_lo + 1) >> 4][(e_lo + 1) & 15] = __builtin_amdgcn_exp2f(c.S[X][0][(e_lo + 1) >> 4][(e_lo + 1) & 15]);
    if constexpr (e_lo + 2 < e_hi) c.S[X][0][(e_lo + 2) >> 4][(e_lo + 2) & 15] = __builtin_amdgcn_exp2f(c.S[X][0][(e_lo + 2) >> 4][(e_lo + 2) & 15]);
  }
  if constexpr (M == 31) asm volatile("" :: "v"(c.S[X][0][0]), "v"(c.S[X][0][1]), "v"(c.NEGM[0]), "v"(c.NEGM[1]), "v"(c.rs0[0]), "v"(c.rs0[1]), "v"(c.rs1[0]), "v"(c.rs1[1]));
  GAP();
}
template <int X, bool PEEL, bool ZERO1, bool DV, bool KR, int VMC, int... Ms> DI void phaseB_(W4C& c, const int t, iseq<Ms...>) {
  if constexpr (DV) { c.sof[0] = c.sofw + 32768u * (unsigned)(t + 1); c.sof[1] = c.sof[0] + 16384u; asm volatile("" : "+s"(c.sof[0]), "+s"(c.sof[1])); }
  asm volatile("s_nop 1" ::: "memory");
  GAP();
  (gapB<X, PEEL, ZERO1, DV, KR, Ms>(c), ...);
  {
    #pragma unroll
    for (int e = (32 * 12) / 13; e < 32; ++e) { if (e & 1) c.rs1[e >> 4] = fadd_s(c.rs1[e >> 4], c.S[X][0][e >> 4][e & 15]); else c.rs0[e >> 4] = fadd_s(c.rs0[e >> 4], c.S[X][0][e >> 4][e & 15]); }
  }
  if constexpr (!PEEL) {
    if (__builtin_expect(c.pend, 0)) {
      asm volatile("s_nop 7\n\ts_nop 7" ::: "memory");
      o_scale(c.fct[0], c.fct[1], make_iseq<128>{});
      asm volatile("s_nop 1" ::: "memory");
      c.lsum[0] = fmul_s(c.lsum[0], c.fct[0]); c.lsum[1] = fmul_s(c.lsum[1], c.fct[1]); c.pend = false;
    }
  }
  asm volatile("s_waitcnt vmcnt(%0) lgkmcnt(0)\n\ts_barrier" :: "i"(VMC) : "memory");
  GAP();
}
template <int X, bool PEEL, bool ZERO1, bool DV, bool KR, int VMC> DI void phaseB(W4C& c, const int t) { phaseB_<X, PEEL, ZERO1, DV, KR, VMC>(c, t, make_iseq<32>{}); }

template <int SL, int... Is> DI void k_reads(W4C& c, iseq<Is...>) { (k_read<Is, SL>(c), ...); }
template <int... Is> DI void kt_reads(const unsigned (&b4)[4], iseq<Is...>) { (kt_read<Is>(b4), ...); }
template <int... Is> DI void qkt_all(W4C& c, iseq<Is...>) { (qkt_mfma<Is>(c), ...); }
template <int VOFF, int... Is> DI void tr_reads(W4C& c, iseq<Is...>) { (tr_read<Is, VOFF>(c, c.vvb), ...); }
template <int X, int... Cs> DI void packs(W4C& c, iseq<Cs...>) { (pack1<X, ((Cs >> 3) & 1), 2 * (Cs >> 4) + ((Cs >> 2) & 1), (Cs & 3)>(c), ...); }
template <int X, int... Js> DI void rowsums(W4C& c, iseq<Js...>) { (rowsum1<X, Js>(c), ...); }
template <int... Ms> DI void pv_all(W4C& c, iseq<Ms...>) { (pv_mfma<Ms, false>(c), ...); }
template <int ISV, int SL, int... Is> DI void dma_tiles(W4C& c, iseq<Is...>) { (dma_piece<ISV, SL, Is>(c), ...); }
template <int QB, int... Ds> DI void q_load(const unsigned (&b4)[4], iseq<Ds...>) {
  bf16x8 qf[8];
  ((qf[Ds] = q_ldread<Ds>(b4[Ds & 3])), ...);
  asm volatile("s_waitcnt lgkmcnt(0)" : "+v"(qf[0]), "+v"(qf[1]), "+v"(qf[2]), "+v"(qf[3]), "+v"(qf[4]), "+v"(qf[5]), "+v"(qf[6]), "+v"(qf[7]) :: "memory");
  auto one = [&](auto dc) { constexpr int d0 = decltype(dc)::value; u32x4 w;
    #pragma unroll
    for (int e = 0; e < 4; ++e) w[e] = cvtpk(fmul_s((float)qf[d0][2 * e], CSC), fmul_s((float)qf[d0][2 * e + 1], CSC));
    q_write1<8 * QB + d0>(w); };
  (one(std::integral_constant<int, Ds>{}), ...);
}
template <int I> DI void o_send1(unsigned addr) {
  constexpr int db = I >> 2, g = I & 3, a = AO_(db, 1) + 4 * g;
  asm volatile("ds_write_b128 %0, a[%c1:%c2] offset:%c3" :: "v"(addr), "i"(a), "i"(a + 3), "i"(1024 * I) : "memory");
}
template <int... Is> DI void o_send(unsigned addr, iseq<Is...>) { (o_send1<Is>(addr), ...); }
template <int I> DI f32x4 o_own1() {
  constexpr int db = I >> 2, g = I & 3, a = AO_(db, 0) + 4 * g; f32x4 r;
  asm volatile("v_accvgpr_read_b32 %0, a[%c4]\n\tv_accvgpr_read_b32 %1, a[%c5]\n\tv_accvgpr_read_b32 %2, a[%c6]\n\tv_accvgpr_read_b32 %3, a[%c7]"
               : "=v"(r[0]), "=v"(r[1]), "=v"(r[2]), "=v"(r[3]) : "i"(a), "i"(a + 1), "i"(a + 2), "i"(a + 3));
  return r;
}

extern "C" __global__ __launch_bounds__(256, 1)
void tree_attn_kernel(const unsigned short* __restrict__ Q, const unsigned short* __restrict__ Kc, const unsigned short* __restrict__ Vc,
                      const unsigned short* __restrict__ Kt, const unsigned short* __restrict__ Vt, const unsigned short* __restrict__ Mk, unsigned short* __restrict__ O) {
  __shared__ __attribute__((aligned(16))) char lds[163840];
  const unsigned tid = threadIdx.x, lane = tid & 63u, l32 = lane & 31u, hi = lane >> 5;
  const unsigned wid = rfl(tid >> 6), wr = wid & 1u, wp = wid >> 1;
  const unsigned blk = blockIdx.x, xg = blk & 7u, jj = blk >> 3, pair = xg * 16u + (jj >> 1), half = jj & 1u, b = pair >> 2;
  const unsigned ldsb = (unsigned)(size_t)lds;
  W4C c;
  #pragma unroll
  for (int j = 0; j < 4; ++j) c.vkb[j] = ldsb + 0x10000u * wp + 2048u * (l32 >> 3) + 128u * (l32 & 7u) + 16u * ((2u * j + hi) ^ ((l32 >> 1) & 7u));
  c.vvb = ldsb + 0x10000u * wp + (((lane & 3u) << 3) | (((lane >> 2) & 3u) << 6) | (((lane >> 4) & 1u) << 5) | (hi << 8));
  #pragma unroll
  for (int par = 0; par < 2; ++par) c.vksrc[par] = 256u * (lane >> 3) + 16u * ((lane & 7u) ^ (4u * par + (lane >> 4)));
  c.vvsrc = 256u * ((lane >> 2) & 7u) + 64u * hi + 16u * (lane & 3u);
  c.m0w = rfl(ldsb + 4096u * wid); c.sofw = rfl(4096u * wid);
  c.srdK = __builtin_amdgcn_make_buffer_rsrc((void*)((const char*)Kc + (size_t)pair * 524288u), (short)0, 524288, SRD_WORD3);
  c.srdV = __builtin_amdgcn_make_buffer_rsrc((void*)((const char*)Vc + (size_t)pair * 524288u), (short)0, 524288, SRD_WORD3);
  const size_t qrow0 = (size_t)(pair * 8u + half * 4u) * 32u;
  __amdgpu_buffer_rsrc_t srdQ = __builtin_amdgcn_make_buffer_rsrc((void*)((const char*)Q + qrow0 * 256u), (short)0, 32768, SRD_WORD3);
  __amdgpu_buffer_rsrc_t srdT = __builtin_amdgcn_make_buffer_rsrc((void*)((const char*)(wp ? Vt : Kt) + (size_t)pair * 8192u), (short)0, 8192, SRD_WORD3);
  __amdgpu_buffer_rsrc_t srdM = __builtin_amdgcn_make_buffer_rsrc((void*)((const char*)Mk + (size_t)b * 2048u), (short)0, 2048, SRD_WORD3);

  u32x2 mk[4];
  {
    const unsigned moff = 64u * l32 + 8u * hi;
    asm volatile("s_nop 4\n\tbuffer_load_dwordx2 %0, %4, %5, 0 offen\n\tbuffer_load_dwordx2 %1, %4, %5, 0 offen offset:16\n\t"
                 "buffer_load_dwordx2 %2, %4, %5, 0 offen offset:32\n\tbuffer_load_dwordx2 %3, %4, %5, 0 offen offset:48"
                 : "=&v"(mk[0]), "=&v"(mk[1]), "=&v"(mk[2]), "=&v"(mk[3]) : "v"(moff), "s"(srdM) : "memory");
    const unsigned sq1 = c.sofw + 16384u;
    dma1<QSTAGE + PM0(0), PIMM(0)>(c.m0w, c.vksrc[0], srdQ, c.sofw); dma1<QSTAGE + PM0(1), PIMM(1)>(c.m0w, c.vksrc[0], srdQ, c.sofw);
    dma1<QSTAGE + PM0(2), PIMM(2)>(c.m0w, c.vksrc[1], srdQ, c.sofw); dma1<QSTAGE + PM0(3), PIMM(3)>(c.m0w, c.vksrc[1], srdQ, c.sofw);
    dma1<QSTAGE + 16384 + PM0(0), PIMM(0)>(c.m0w, c.vksrc[0], srdQ, sq1); dma1<QSTAGE + 16384 + PM0(1), PIMM(1)>(c.m0w, c.vksrc[0], srdQ, sq1);
    dma1<QSTAGE + 16384 + PM0(2), PIMM(2)>(c.m0w, c.vksrc[1], srdQ, sq1); dma1<QSTAGE + 16384 + PM0(3), PIMM(3)>(c.m0w, c.vksrc[1], srdQ, sq1);
    const unsigned m0t = rfl(ldsb + (wp ? TREE_V : TREE_K) + 4096u * wr), sot = rfl(4096u * wr);
    const unsigned vt0 = wp ? c.vvsrc : c.vksrc[0], vt1 = wp ? c.vvsrc : c.vksrc[1];
    asm volatile("s_nop 4" ::: "memory");
    dma1<PM0(0), PIMM(0)>(m0t, vt0, srdT, sot); dma1<PM0(1), PIMM(1)>(m0t, vt0, srdT, sot);
    dma1<PM0(2), PIMM(2)>(m0t, vt1, srdT, sot); dma1<PM0(3), PIMM(3)>(m0t, vt1, srdT, sot);
    c.sof[0] = c.sofw; c.sof[1] = c.sofw + 16384u;
    dma_tiles<0, 0>(c, make_iseq<8>{});
    c.sof[0] = c.sofw + 32768u; c.sof[1] = c.sofw + 32768u + 16384u;
    dma_tiles<0, 1>(c, make_iseq<8>{});
    c.sof[0] = c.sofw; c.sof[1] = c.sofw + 16384u;
    dma_tiles<1, 0>(c, make_iseq<8>{});
  }
  asm volatile("s_waitcnt vmcnt(24)\n\ts_barrier" : "+v"(mk[0]), "+v"(mk[1]), "+v"(mk[2]), "+v"(mk[3]) :: "memory");
  {
    unsigned b4[4];
    #pragma unroll
    for (int j = 0; j < 4; ++j) b4[j] = c.vkb[j] - 0x10000u * wp + TREE_K;
    kt_reads(b4, make_iseq<8>{});
    #pragma unroll
    for (int j = 0; j < 4; ++j) b4[j] = c.vkb[j] - 0x10000u * wp + QSTAGE + 16384u * wr + 8192u * wp;
    q_load<0>(b4, make_iseq<8>{});
    #pragma unroll
    for (int g = 0; g < 4; ++g) {
      c.St[4 * g + 0] = (1.0f - __uint_as_float(mk[g][0] << 16)) * MASKC; c.St[4 * g + 1] = (1.0f - __uint_as_float(mk[g][0] & 0xffff0000u)) * MASKC;
      c.St[4 * g + 2] = (1.0f - __uint_as_float(mk[g][1] << 16)) * MASKC; c.St[4 * g + 3] = (1.0f - __uint_as_float(mk[g][1] & 0xffff0000u)) * MASKC;
    }
    asm volatile("s_nop 1" : "+v"(c.St));
    qkt_all(c, make_iseq<8>{});
    #pragma unroll
    for (int j = 0; j < 4; ++j) b4[j] = c.vkb[j] - 0x10000u * wp + QSTAGE + 16384u * wr + 8192u * (wp ^ 1u);
    q_load<1>(b4, make_iseq<8>{});
  }
  asm volatile("s_waitcnt vmcnt(16)\n\ts_barrier" ::: "memory");
  k_reads<0>(c, make_iseq<16>{});
  asm volatile("s_waitcnt lgkmcnt(0)\n\ts_barrier" ::: "memory");
  c.mhat[0] = c.mhat[1] = 0.f; c.lsum[0] = c.lsum[1] = 0.f; c.fct[0] = c.fct[1] = 1.f; c.pend = false;
  const unsigned vtb = c.vvb - 0x10000u * wp + TREE_V;

  phaseA<0, true, true, 16>(c, 0, vtb);  phaseB<0, true,  false, true, true, 16>(c, 0);
  phaseA<1, false, true, 16>(c, 1);      phaseB<1, false, true,  true, true, 16>(c, 1);
  for (int t = 2; t < NSTEP - 2; t += 2) {
    phaseA<0, false, true, 16>(c, t);     phaseB<0, false, false, true, true, 16>(c, t);
    phaseA<1, false, true, 16>(c, t + 1); phaseB<1, false, false, true, true, 16>(c, t + 1);
  }
  phaseA<0, false, false, 8>(c, NSTEP - 2); phaseB<0, false, false, true, true, 8>(c, NSTEP - 2);
  phaseA<1, false, false, 8>(c, NSTEP - 1); phaseB<1, false, false, false, false, 0>(c, NSTEP - 1);
  {
    tr_reads<0x8000 + 0x4000>(c, make_iseq<32>{});
    #pragma unroll
    for (int e = 0; e < 32; ++e) c.S[1][1][e >> 4][e & 15] = __builtin_amdgcn_exp2f(c.S[1][1][e >> 4][e & 15]);
    packs<1>(c, make_iseq<32>{});
    rowsums<1>(c, iseq<8, 9, 10, 11, 12, 13, 14, 15, 24, 25, 26, 27, 28, 29, 30, 31>{});
    #pragma unroll
    for (int qb = 0; qb < 2; ++qb) { const float rs = fadd_s(c.lsum[qb], fadd_s(c.rs0[qb], c.rs1[qb]));
      auto pr = __builtin_amdgcn_permlane32_swap(__float_as_uint(rs), __float_as_uint(rs), false, false);
      c.lsum[qb] = fadd_s(__uint_as_float(pr[0]), __uint_as_float(pr[1])); }
    asm volatile("s_waitcnt lgkmcnt(0)" : W4_VLO :: "memory"); asm volatile("" : W4_VUP :: "memory");
    GAP();
    #pragma unroll
    for (int ks = 0; ks < 4; ++ks)
      #pragma unroll
      for (int db = 0; db < 4; ++db) c.Vf[db][ks] = __builtin_shufflevector(c.vlo[db][ks], c.vup[db][ks], 0, 1, 2, 3, 4, 5, 6, 7);
    asm volatile("s_nop 1" ::: "memory");
    pv_all(c, make_iseq<32>{});
    asm volatile("s_nop 7\n\ts_nop 7" ::: "memory");
  }
  {
    const unsigned xs = ldsb + 0x10000u * wp + 16384u * wr + 16u * lane;
    o_send(xs, make_iseq<16>{});
    float* ml = (float*)(lds + 0x8000u + 512u * wid);
    *(f32x2*)(ml + 2u * lane) = (f32x2){c.mhat[1], c.lsum[1]};
    asm volatile("s_waitcnt lgkmcnt(0)\n\ts_barrier" ::: "memory");
    const f32x2 pm = *(const f32x2*)((const float*)(lds + 0x8000u + 512u * (wid ^ 2u)) + 2u * lane);
    const char* xr = lds + 0x10000u * (wp ^ 1u) + 16384u * wr + 16u * lane;
    const float M = fmaxf(c.mhat[0], pm[0]);
    const float w0 = __builtin_amdgcn_exp2f(c.mhat[0] - M), w1 = __builtin_amdgcn_exp2f(pm[0] - M);
    const float inv = __builtin_amdgcn_rcpf(w0 * c.lsum[0] + w1 * pm[1]);
    const float f0 = w0 * inv, f1 = w1 * inv;
    char* stg = lds + QSTAGE + 8192u * wid;
    auto one = [&](auto ic) { constexpr int I = decltype(ic)::value, db = I >> 2, g = I & 3;
      const f32x4 a = o_own1<I>(); const f32x4 pb = *(const f32x4*)(xr + 1024 * I);
      u32x2 w; w[0] = cvtpk(a[0] * f0 + pb[0] * f1, a[1] * f0 + pb[1] * f1); w[1] = cvtpk(a[2] * f0 + pb[2] * f1, a[3] * f0 + pb[3] * f1);
      *(u32x2*)(stg + 256u * l32 + 16u * ((4u * db + g) ^ (l32 & 15u)) + 8u * hi) = w; };
    [&]<int... Is>(iseq<Is...>) { (one(std::integral_constant<int, Is>{}), ...); }(make_iseq<16>{});
    asm volatile("s_waitcnt lgkmcnt(0)" ::: "memory");
    char* orow = (char*)O + (qrow0 + 32u * (2u * wr + wp)) * 256u;
    #pragma unroll
    for (int j = 0; j < 8; ++j) { const unsigned r = 4u * j + (lane >> 4), ch = lane & 15u;
      const u32x4 v = *(const u32x4*)(stg + 256u * r + 16u * (ch ^ (r & 15u)));
      __builtin_nontemporal_store(v, (u32x4*)(orow + 256u * r + 16u * ch)); }
  }
}

extern "C" void kernel_launch(void* const* d_in, const int* in_sizes, int n_in, void* d_out, int out_size, void* d_ws, size_t ws_size, hipStream_t stream) {
  hipLaunchKernelGGL(tree_attn_kernel, dim3(256), dim3(256), 0, stream, (const unsigned short*)d_in[0], (const unsigned short*)d_in[1], (const unsigned short*)d_in[2],
                     (const unsigned short*)d_in[3], (const unsigned short*)d_in[4], (const unsigned short*)d_in[5], (unsigned short*)d_out);
}
```

```cpp
#include <hip/hip_runtime.h>
extern "C" __global__ __launch_bounds__(256, 1)
void tree_attn_kernel(const char* __restrict__ Q, const char* __restrict__ Kc, const char* __restrict__ Vc, const char* __restrict__ Kt, const char* __restrict__ Vt,
                      const char* __restrict__ Mk, char* __restrict__ O, char* __restrict__ ws) {
  __shared__ __attribute__((aligned(16))) char lds[163840];
  const unsigned wid = __builtin_amdgcn_readfirstlane(threadIdx.x >> 6);
  asm volatile(
    "s_nop 1\n\t"
    "s_load_dwordx16 s[16:31], %0, 0x0\n\t"
    "s_mov_b32 s67, 0\n\t"
    "s_mov_b32 s91, 0\n\t"
    "s_mov_b64 s[92:93], -1\n\t"
    "1:\n\t"
    "v_mbcnt_lo_u32_b32 v203, -1, 0\n\t"
    "v_mbcnt_hi_u32_b32 v203, -1, v203\n\t"
    "s_bfe_u32 s87, %2, 0x10003\n\t"
    "s_getpc_b64 s[94:95]\n\t"
    "CB_%=:\n\t"
    "s_lshl_b32 s68, s87, 2\n\t"
    "s_add_u32 s68, s68, %1\n\t"
    "s_lshl_b32 s68, s68, 13\n\t"
    "v_lshlrev_b32 v244, 7, v203\n\t"
    "v_add_u32 v244, s68, v244\n\t"
    "v_min_u32 v244, CE_%=-CB_%=-4, v244\n\t"
    "global_load_dword v249, v244, s[94:95]\n\t"
    "s_and_b32 s86, %2, 7\n\t"
    "s_lshl_b32 s86, s86, 4\n\t"
    "s_lshr_b32 s68, %2, 4\n\t"
    "s_add_u32 s86, s86, s68\n\t"
    "s_lshl_b32 s69, s86, 1\n\t"
    "s_add_u32 s69, s69, s87\n\t"
    "v_lshrrev_b32 v237, 4, v203\n\t"
    "v_bfe_u32 v239, v203, 2, 2\n\t"
    "v_and_b32 v242, 3, v203\n\t"
    "v_lshlrev_b32 v240, 8, v239\n\t"
    "v_lshl_add_u32 v240, v237, 6, v240\n\t"
    "v_lshl_add_u32 v199, v242, 4, v240\n\t"
    "v_xor_b32 v200, 32, v199\n\t"
    "v_xor_b32 v201, 48, v199\n\t"
    "v_xor_b32 v202, 16, v199\n\t"
    "s_lshl_b32 s88, %1, 14\n\t"
    "s_add_u32 s71, s88, 0x20000\n\t"
    "s_lshl_b32 s70, %1, 16\n\t"
    "s_sub_u32 s70, s70, 0x14000\n\t"
    "s_cmp_lt_u32 %1, 2\n\t"
    "s_cselect_b32 s71, s71, s70\n\t"
    "s_cselect_b32 s79, 8192, 0\n\t"
    "s_lshl_b32 s65, %1, 12\n\t"
    "s_mov_b32 s64, s65\n\t"
    "v_mul_u32_u24 v0, 0x9e3779, v199\n\t"
    "v_add_u32 v0, 0x7f4a7c15, v0\n\t"
    "v_mov_b32 v245, 0x3e003e00\n\t"
    "s_mov_b32 s98, 0x81ff81ff\n\t"
    "v_alignbit_b32 v1, v0, v0, 3\n\t"
    "v_alignbit_b32 v2, v0, v0, 5\n\t"
    "v_alignbit_b32 v3, v0, v0, 7\n\t"
    "v_alignbit_b32 v4, v0, v0, 9\n\t"
    "v_alignbit_b32 v5, v0, v0, 11\n\t"
    "v_alignbit_b32 v6, v0, v0, 13\n\t"
    "v_alignbit_b32 v7, v0, v0, 15\n\t"
    "v_alignbit_b32 v16, v0, v0, 17\n\t"
    "v_alignbit_b32 v17, v0, v0, 19\n\t"
    "v_alignbit_b32 v18, v0, v0, 21\n\t"
    "v_alignbit_b32 v19, v0, v0, 23\n\t"
    "v_alignbit_b32 v20, v0, v0, 25\n\t"
    "v_alignbit_b32 v21, v0, v0, 27\n\t"
    "v_alignbit_b32 v22, v0, v0, 29\n\t"
    "v_alignbit_b32 v23, v0, v0, 31\n\t"
    "v_and_or_b32 v0, v0, s98, v245\n\t"
    "v_and_or_b32 v1, v1, s98, v245\n\t"
    "v_and_or_b32 v2, v2, s98, v245\n\t"
    "v_and_or_b32 v3, v3, s98, v245\n\t"
    "v_and_or_b32 v4, v4, s98, v245\n\t"
    "v_and_or_b32 v5, v5, s98, v245\n\t"
    "v_and_or_b32 v6, v6, s98, v245\n\t"
    "v_and_or_b32 v7, v7, s98, v245\n\t"
    "v_and_or_b32 v16, v16, s98, v245\n\t"
    "v_and_or_b32 v17, v17, s98, v245\n\t"
    "v_and_or_b32 v18, v18, s98, v245\n\t"
    "v_and_or_b32 v19, v19, s98, v245\n\t"
    "v_and_or_b32 v20, v20, s98, v245\n\t"
    "v_and_or_b32 v21, v21, s98, v245\n\t"
    "v_and_or_b32 v22, v22, s98, v245\n\t"
    "v_and_or_b32 v23, v23, s98, v245\n\t"
    "s_nop 0\n\t"
    "v_mfma_f32_16x16x32_bf16 v[8:11], v[0:3], v[4:7], 0\n\t"
    "s_nop 5\n\t"
    "v_mfma_f32_16x16x32_bf16 v[8:11], v[16:19], v[4:7], v[8:11]\n\t"
    "s_nop 5\n\t"
    "v_mfma_f32_16x16x32_bf16 v[8:11], v[0:3], v[20:23], v[8:11]\n\t"
    "s_nop 5\n\t"
    "v_mfma_f32_16x16x32_bf16 v[8:11], v[16:19], v[20:23], v[8:11]\n\t"
    "s_nop 5\n\t"
    "v_mfma_f32_16x16x32_bf16 v[24:27], v[0:3], v[4:7], 0\n\t"
    "s_nop 5\n\t"
    "v_mfma_f32_16x16x32_bf16 v[24:27], v[16:19], v[4:7], v[24:27]\n\t"
    "s_nop 5\n\t"
    "s_waitcnt lgkmcnt(0)\n\t"
    "s_mov_b64 s[44:45], s[16:17]\n\t"
    "s_lshl_b32 s68, s86, 16\n\t"
    "v_mfma_f32_16x16x32_bf16 v[24:27], v[0:3], v[20:23], v[24:27]\n\t"
    "s_add_u32 s44, s44, s68\n\t"
    "s_addc_u32 s45, s45, 0\n\t"
    "s_and_b32 s45, s45, 0xffff\n\t"
    "v_mfma_f32_16x16x32_bf16 v[24:27], v[16:19], v[20:23], v[24:27]\n\t"
    "s_mov_b32 s46, 65536\n\t"
    "s_mov_b32 s47, 0x20000\n\t"
    "s_add_u32 s70, s88, 0\n\t"
    "v_mfma_f32_16x16x32_bf16 v[40:43], v[0:3], v[4:7], 0\n\t"
    "s_add_u32 m0, s71, 0\n\t"
    "s_nop 0\n\t"
    "buffer_load_dwordx4 v199, s[44:47], s70 offen offset:0 nt lds\n\t"
    "v_mfma_f32_16x16x32_bf16 v[40:43], v[16:19], v[4:7], v[40:43]\n\t"
    "buffer_load_dwordx4 v200, s[44:47], s70 offen offset:1024 nt lds\n\t"
    "buffer_load_dwordx4 v201, s[44:47], s70 offen offset:2048 nt lds\n\t"
    "buffer_load_dwordx4 v202, s[44:47], s70 offen offset:3072 nt lds\n\t"
    "v_mfma_f32_16x16x32_bf16 v[40:43], v[0:3], v[20:23], v[40:43]\n\t"
    "s_mov_b64 s[36:37], s[18:19]\n\t"
    "s_lshl_b32 s68, s69, 18\n\t"
    "s_add_u32 s36, s36, s68\n\t"
    "v_mfma_f32_16x16x32_bf16 v[40:43], v[16:19], v[20:23], v[40:43]\n\t"
    "s_addc_u32 s37, s37, 0\n\t"
    "s_and_b32 s37, s37, 0xffff\n\t"
    "s_mov_b32 s38, 262144\n\t"
    "v_mfma_f32_16x16x32_bf16 v[56:59], v[0:3], v[4:7], 0\n\t"
    "s_mov_b32 s39, 0x20000\n\t"
    "s_add_u32 m0, s64, 0\n\t"
    "s_nop 0\n\t"
    "v_mfma_f32_16x16x32_bf16 v[56:59], v[16:19], v[4:7], v[56:59]\n\t"
    "buffer_load_dwordx4 v199, s[36:39], s65 offen offset:0 nt lds\n\t"
    "buffer_load_dwordx4 v200, s[36:39], s65 offen offset:1024 nt lds\n\t"
    "buffer_load_dwordx4 v201, s[36:39], s65 offen offset:2048 nt lds\n\t"
    "v_mfma_f32_16x16x32_bf16 v[56:59], v[0:3], v[20:23], v[56:59]\n\t"
    "buffer_load_dwordx4 v202, s[36:39], s65 offen offset:3072 nt lds\n\t"
    "s_add_u32 s70, s88, 4096\n\t"
    "s_add_u32 m0, s71, 4096\n\t"
    "v_mfma_f32_16x16x32_bf16 v[56:59], v[16:19], v[20:23], v[56:59]\n\t"
    "s_nop 0\n\t"
    "buffer_load_dwordx4 v199, s[44:47], s70 offen offset:0 nt lds\n\t"
    "buffer_load_dwordx4 v200, s[44:47], s70 offen offset:1024 nt lds\n\t"
    "v_mfma_f32_16x16x32_bf16 v[72:75], v[0:3], v[4:7], 0\n\t"
    "buffer_load_dwordx4 v201, s[44:47], s70 offen offset:2048 nt lds\n\t"
    "buffer_load_dwordx4 v202, s[44:47], s70 offen offset:3072 nt lds\n\t"
    "s_lshl_b32 s70, s87, 13\n\t"
    "v_mfma_f32_16x16x32_bf16 v[72:75], v[16:19], v[4:7], v[72:75]\n\t"
    "s_add_u32 s89, s88, s70\n\t"
    "s_mov_b64 s[40:41], s[20:21]\n\t"
    "s_lshl_b32 s68, s69, 18\n\t"
    "v_mfma_f32_16x16x32_bf16 v[72:75], v[0:3], v[20:23], v[72:75]\n\t"
    "s_add_u32 s40, s40, s68\n\t"
    "s_addc_u32 s41, s41, 0\n\t"
    "s_and_b32 s41, s41, 0xffff\n\t"
    "v_mfma_f32_16x16x32_bf16 v[72:75], v[16:19], v[20:23], v[72:75]\n\t"
    "s_mov_b32 s42, 262144\n\t"
    "s_mov_b32 s43, 0x20000\n\t"
    "v_lshrrev_b32 v244, 1, v239\n\t"
    "v_mfma_f32_16x16x32_bf16 v[88:91], v[0:3], v[4:7], 0\n\t"
    "v_xor_b32 v243, v239, v244\n\t"
    "v_and_b32 v243, 1, v243\n\t"
    "v_lshl_or_b32 v243, v243, 1, v244\n\t"
    "v_mfma_f32_16x16x32_bf16 v[88:91], v[16:19], v[4:7], v[88:91]\n\t"
    "v_xor_b32 v243, v243, v237\n\t"
    "v_lshlrev_b32 v240, 10, v239\n\t"
    "v_lshl_add_u32 v240, v242, 6, v240\n\t"
    "v_mfma_f32_16x16x32_bf16 v[88:91], v[0:3], v[20:23], v[88:91]\n\t"
    "v_lshl_add_u32 v196, v243, 4, v240\n\t"
    "v_add_u32 v246, s71, v196\n\t"
    "v_lshrrev_b32 v244, 1, v237\n\t"
    "v_mfma_f32_16x16x32_bf16 v[88:91], v[16:19], v[20:23], v[88:91]\n\t"
    "v_xor_b32 v243, v237, v244\n\t"
    "v_and_b32 v243, 1, v243\n\t"
    "v_lshl_or_b32 v243, v243, 1, v244\n\t"
    "v_mfma_f32_16x16x32_bf16 v[104:107], v[0:3], v[4:7], 0\n\t"
    "v_lshrrev_b32 v244, 1, v242\n\t"
    "v_xor_b32 v243, v243, v244\n\t"
    "v_lshlrev_b32 v240, 10, v237\n\t"
    "v_mfma_f32_16x16x32_bf16 v[104:107], v[16:19], v[4:7], v[104:107]\n\t"
    "v_lshl_add_u32 v240, v239, 6, v240\n\t"
    "v_lshl_add_u32 v240, v243, 4, v240\n\t"
    "v_and_b32 v244, 1, v242\n\t"
    "v_mfma_f32_16x16x32_bf16 v[104:107], v[0:3], v[20:23], v[104:107]\n\t"
    "v_lshl_add_u32 v240, v244, 3, v240\n\t"
    "v_add_u32 v197, 0x10000, v240\n\t"
    "v_xor_b32 v198, 32, v197\n\t"
    "v_mfma_f32_16x16x32_bf16 v[104:107], v[16:19], v[20:23], v[104:107]\n\t"
    "v_and_b32 v236, 15, v203\n\t"
    "v_lshlrev_b32 v247, 6, v236\n\t"
    "v_lshl_add_u32 v247, v237, 3, v247\n\t"
    "v_mfma_f32_16x16x32_bf16 v[120:123], v[0:3], v[4:7], 0\n\t"
    "v_lshlrev_b32 v248, 4, v203\n\t"
    "v_mov_b32 v192, 0x3f803f80\n\t"
    "v_mov_b32 v193, 0x3f803f80\n\t"
    "v_mfma_f32_16x16x32_bf16 v[120:123], v[16:19], v[4:7], v[120:123]\n\t"
    "v_mov_b32 v194, 0x3f803f80\n\t"
    "v_mov_b32 v195, 0x3f803f80\n\t"
    "v_and_b32 v245, 1, v203\n\t"
    "v_mfma_f32_16x16x32_bf16 v[120:123], v[0:3], v[20:23], v[120:123]\n\t"
    "v_mul_u32_u24 v245, 0xaaaa, v245\n\t"
    "v_add_u32 v245, 0x4444, v245\n\t"
    "v_mov_b32 v176, 0\n\t"
    "v_mfma_f32_16x16x32_bf16 v[120:123], v[16:19], v[20:23], v[120:123]\n\t"
    "v_mov_b32 v177, 0\n\t"
    "v_mov_b32 v178, 0\n\t"
    "v_mov_b32 v179, 0\n\t"
    "v_mfma_f32_16x16x32_bf16 v[8:11], v[0:3], v[4:7], v[8:11]\n\t"
    "v_mov_b32 v180, 0\n\t"
    "v_mov_b32 v181, 0\n\t"
    "v_mov_b32 v182, 0\n\t"
    "v_mfma_f32_16x16x32_bf16 v[8:11], v[16:19], v[4:7], v[8:11]\n\t"
    "v_mov_b32 v183, 0\n\t"
    "v_mov_b32 v184, 0\n\t"
    "v_mov_b32 v185, 0\n\t"
    "v_mfma_f32_16x16x32_bf16 v[8:11], v[0:3], v[20:23], v[8:11]\n\t"
    "v_mov_b32 v186, 0\n\t"
    "v_mov_b32 v187, 0\n\t"
    "v_mov_b32 v188, 0\n\t"
    "v_mfma_f32_16x16x32_bf16 v[8:11], v[16:19], v[20:23], v[8:11]\n\t"
    "v_mov_b32 v189, 0\n\t"
    "v_mov_b32 v190, 0\n\t"
    "v_mov_b32 v191, 0\n\t"
    "v_mfma_f32_16x16x32_bf16 v[24:27], v[0:3], v[4:7], v[24:27]\n\t"
    "s_mov_b32 s72, 0x3e0293ee\n\t"
    "s_mov_b32 s73, 0x3e0293ee\n\t"
    "s_mov_b32 s74, 0xceabfb8f\n\t"
    "v_mfma_f32_16x16x32_bf16 v[24:27], v[16:19], v[4:7], v[24:27]\n\t"
    "s_lshr_b32 s69, s86, 2\n\t"
    "s_mov_b64 s[52:53], s[26:27]\n\t"
    "s_lshl_b32 s68, s69, 11\n\t"
    "v_mfma_f32_16x16x32_bf16 v[24:27], v[0:3], v[20:23], v[24:27]\n\t"
    "s_add_u32 s52, s52, s68\n\t"
    "s_addc_u32 s53, s53, 0\n\t"
    "s_and_b32 s53, s53, 0xffff\n\t"
    "v_mfma_f32_16x16x32_bf16 v[24:27], v[16:19], v[20:23], v[24:27]\n\t"
    "s_mov_b32 s54, 2048\n\t"
    "s_mov_b32 s55, 0x20000\n\t"
    "s_mov_b64 s[56:57], s[28:29]\n\t"
    "v_mfma_f32_16x16x32_bf16 v[40:43], v[0:3], v[4:7], v[40:43]\n\t"
    "s_lshl_b32 s68, s86, 16\n\t"
    "s_add_u32 s56, s56, s68\n\t"
    "s_addc_u32 s57, s57, 0\n\t"
    "v_mfma_f32_16x16x32_bf16 v[40:43], v[16:19], v[4:7], v[40:43]\n\t"
    "s_and_b32 s57, s57, 0xffff\n\t"
    "s_mov_b32 s58, 65536\n\t"
    "s_mov_b32 s59, 0x20000\n\t"
    "v_mfma_f32_16x16x32_bf16 v[40:43], v[0:3], v[20:23], v[40:43]\n\t"
    "s_mov_b64 s[60:61], s[22:23]\n\t"
    "s_lshl_b32 s68, s86, 13\n\t"
    "s_add_u32 s60, s60, s68\n\t"
    "v_mfma_f32_16x16x32_bf16 v[40:43], v[16:19], v[20:23], v[40:43]\n\t"
    "s_addc_u32 s61, s61, 0\n\t"
    "s_and_b32 s61, s61, 0xffff\n\t"
    "s_mov_b64 s[62:63], s[24:25]\n\t"
    "v_mfma_f32_16x16x32_bf16 v[56:59], v[0:3], v[4:7], v[56:59]\n\t"
    "s_lshl_b32 s68, s86, 13\n\t"
    "s_add_u32 s62, s62, s68\n\t"
    "s_addc_u32 s63, s63, 0\n\t"
    "v_mfma_f32_16x16x32_bf16 v[56:59], v[16:19], v[4:7], v[56:59]\n\t"
    "s_and_b32 s63, s63, 0xffff\n\t"
    "s_mov_b64 s[48:49], s[30:31]\n\t"
    "s_and_b32 s49, s49, 0xffff\n\t"
    "v_mfma_f32_16x16x32_bf16 v[56:59], v[0:3], v[20:23], v[56:59]\n\t"
    "s_mov_b32 s50, 0x2000000\n\t"
    "s_mov_b32 s51, 0x20000\n\t"
    "s_lshl_b32 s68, %2, 2\n\t"
    "v_mfma_f32_16x16x32_bf16 v[56:59], v[16:19], v[20:23], v[56:59]\n\t"
    "s_add_u32 s68, s68, %1\n\t"
    "s_xor_b32 s69, s68, 32\n\t"
    "s_lshl_b32 s77, s68, 7\n\t"
    "v_mfma_f32_16x16x32_bf16 v[72:75], v[0:3], v[4:7], v[72:75]\n\t"
    "s_lshl_b32 s78, s69, 7\n\t"
    "s_mul_i32 s75, s68, 9216\n\t"
    "s_add_u32 s75, s75, 0x40000\n\t"
    "v_mfma_f32_16x16x32_bf16 v[72:75], v[16:19], v[4:7], v[72:75]\n\t"
    "s_mul_i32 s76, s69, 9216\n\t"
    "s_add_u32 s76, s76, 0x40000\n\t"
    "s_mov_b32 s66, 0\n\t"
    "v_mfma_f32_16x16x32_bf16 v[72:75], v[0:3], v[20:23], v[72:75]\n\t"
    "s_mov_b32 s90, 0\n\t"
    "v_mfma_f32_16x16x32_bf16 v[72:75], v[16:19], v[20:23], v[72:75]\n\t"
    "s_nop 5\n\t"
    "v_mfma_f32_16x16x32_bf16 v[88:91], v[0:3], v[4:7], v[88:91]\n\t"
    "s_nop 5\n\t"
    "v_mfma_f32_16x16x32_bf16 v[88:91], v[16:19], v[4:7], v[88:91]\n\t"
    "s_nop 5\n\t"
    "v_mfma_f32_16x16x32_bf16 v[88:91], v[0:3], v[20:23], v[88:91]\n\t"
    "s_nop 5\n\t"
    "v_mfma_f32_16x16x32_bf16 v[88:91], v[16:19], v[20:23], v[88:91]\n\t"
    "s_nop 5\n\t"
    "v_mfma_f32_16x16x32_bf16 v[104:107], v[0:3], v[4:7], v[104:107]\n\t"
    "s_nop 5\n\t"
    "v_mfma_f32_16x16x32_bf16 v[104:107], v[16:19], v[4:7], v[104:107]\n\t"
    "s_nop 5\n\t"
    "v_mfma_f32_16x16x32_bf16 v[104:107], v[0:3], v[20:23], v[104:107]\n\t"
    "s_nop 5\n\t"
    "v_mfma_f32_16x16x32_bf16 v[104:107], v[16:19], v[20:23], v[104:107]\n\t"
    "s_nop 5\n\t"
    "v_mfma_f32_16x16x32_bf16 v[120:123], v[0:3], v[4:7], v[120:123]\n\t"
    "s_nop 5\n\t"
    "v_mfma_f32_16x16x32_bf16 v[120:123], v[16:19], v[4:7], v[120:123]\n\t"
    "s_nop 5\n\t"
    "v_mfma_f32_16x16x32_bf16 v[120:123], v[0:3], v[20:23], v[120:123]\n\t"
    "s_nop 5\n\t"
    "v_mfma_f32_16x16x32_bf16 v[120:123], v[16:19], v[20:23], v[120:123]\n\t"
    "s_nop 5\n\t"
    "v_mfma_f32_16x16x32_bf16 v[8:11], v[0:3], v[4:7], v[8:11]\n\t"
    "s_nop 5\n\t"
    "v_mfma_f32_16x16x32_bf16 v[8:11], v[16:19], v[4:7], v[8:11]\n\t"
    "s_nop 5\n\t"
    "v_mfma_f32_16x16x32_bf16 v[8:11], v[0:3], v[20:23], v[8:11]\n\t"
    "s_nop 5\n\t"
    "s_waitcnt vmcnt(8)\n\t"
    "s_add_u32 m0, s64, 65536\n\t"
    "v_mfma_f32_16x16x32_bf16 v[8:11], v[16:19], v[20:23], v[8:11]\n\t"
    "ds_read_b128 v[12:15], v246 offset:0\n\t"
    "ds_read_b128 v[28:31], v246 offset:256\n\t"
    "ds_read_b128 v[44:47], v246 offset:512\n\t"
    "v_mfma_f32_16x16x32_bf16 v[24:27], v[0:3], v[4:7], v[24:27]\n\t"
    "ds_read_b128 v[60:63], v246 offset:768\n\t"
    "s_waitcnt lgkmcnt(3)\n\t"
    "v_lshlrev_b32 v76, 16, v12\n\t"
    "v_mfma_f32_16x16x32_bf16 v[24:27], v[16:19], v[4:7], v[24:27]\n\t"
    "v_and_b32 v77, 0xffff0000, v12\n\t"
    "v_lshlrev_b32 v78, 16, v13\n\t"
    "v_and_b32 v79, 0xffff0000, v13\n\t"
    "v_mfma_f32_16x16x32_bf16 v[24:27], v[0:3], v[20:23], v[24:27]\n\t"
    "v_lshlrev_b32 v92, 16, v14\n\t"
    "v_and_b32 v93, 0xffff0000, v14\n\t"
    "v_lshlrev_b32 v94, 16, v15\n\t"
    "v_mfma_f32_16x16x32_bf16 v[24:27], v[16:19], v[20:23], v[24:27]\n\t"
    "v_and_b32 v95, 0xffff0000, v15\n\t"
    "v_mul_f32 v76, s72, v76\n\t"
    "v_mul_f32 v77, s72, v77\n\t"
    "v_mfma_f32_16x16x32_bf16 v[40:43], v[0:3], v[4:7], v[40:43]\n\t"
    "v_mul_f32 v78, s72, v78\n\t"
    "v_mul_f32 v79, s72, v79\n\t"
    "v_mul_f32 v92, s72, v92\n\t"
    "v_mfma_f32_16x16x32_bf16 v[40:43], v[16:19], v[4:7], v[40:43]\n\t"
    "v_mul_f32 v93, s72, v93\n\t"
    "v_mul_f32 v94, s72, v94\n\t"
    "v_mul_f32 v95, s72, v95\n\t"
    "v_mfma_f32_16x16x32_bf16 v[40:43], v[0:3], v[20:23], v[40:43]\n\t"
    "v_cvt_pk_bf16_f32 v108, v76, v77\n\t"
    "v_cvt_pk_bf16_f32 v109, v78, v79\n\t"
    "v_cvt_pk_bf16_f32 v110, v92, v93\n\t"
    "v_mfma_f32_16x16x32_bf16 v[40:43], v[16:19], v[20:23], v[40:43]\n\t"
    "v_cvt_pk_bf16_f32 v111, v94, v95\n\t"
    "v_accvgpr_write_b32 a0, v108\n\t"
    "v_accvgpr_write_b32 a1, v109\n\t"
    "v_mfma_f32_16x16x32_bf16 v[56:59], v[0:3], v[4:7], v[56:59]\n\t"
    "v_accvgpr_write_b32 a2, v110\n\t"
    "v_accvgpr_write_b32 a3, v111\n\t"
    "buffer_load_dwordx4 v199, s[40:43], s65 offen offset:0 nt lds\n\t"
    "v_mfma_f32_16x16x32_bf16 v[56:59], v[16:19], v[4:7], v[56:59]\n\t"
    "s_waitcnt lgkmcnt(2)\n\t"
    "v_lshlrev_b32 v76, 16, v28\n\t"
    "v_and_b32 v77, 0xffff0000, v28\n\t"
    "v_mfma_f32_16x16x32_bf16 v[56:59], v[0:3], v[20:23], v[56:59]\n\t"
    "v_lshlrev_b32 v78, 16, v29\n\t"
    "v_and_b32 v79, 0xffff0000, v29\n\t"
    "v_lshlrev_b32 v92, 16, v30\n\t"
    "v_mfma_f32_16x16x32_bf16 v[56:59], v[16:19], v[20:23], v[56:59]\n\t"
    "v_and_b32 v93, 0xffff0000, v30\n\t"
    "v_lshlrev_b32 v94, 16, v31\n\t"
    "v_and_b32 v95, 0xffff0000, v31\n\t"
    "v_mfma_f32_16x16x32_bf16 v[72:75], v[0:3], v[4:7], v[72:75]\n\t"
    "v_mul_f32 v76, s72, v76\n\t"
    "v_mul_f32 v77, s72, v77\n\t"
    "v_mul_f32 v78, s72, v78\n\t"
    "v_mfma_f32_16x16x32_bf16 v[72:75], v[16:19], v[4:7], v[72:75]\n\t"
    "v_mul_f32 v79, s72, v79\n\t"
    "v_mul_f32 v92, s72, v92\n\t"
    "v_mul_f32 v93, s72, v93\n\t"
    "v_mfma_f32_16x16x32_bf16 v[72:75], v[0:3], v[20:23], v[72:75]\n\t"
    "v_mul_f32 v94, s72, v94\n\t"
    "v_mul_f32 v95, s72, v95\n\t"
    "v_cvt_pk_bf16_f32 v108, v76, v77\n\t"
    "v_mfma_f32_16x16x32_bf16 v[72:75], v[16:19], v[20:23], v[72:75]\n\t"
    "v_cvt_pk_bf16_f32 v109, v78, v79\n\t"
    "v_cvt_pk_bf16_f32 v110, v92, v93\n\t"
    "v_cvt_pk_bf16_f32 v111, v94, v95\n\t"
    "v_mfma_f32_16x16x32_bf16 v[88:91], v[0:3], v[4:7], v[88:91]\n\t"
    "v_accvgpr_write_b32 a4, v108\n\t"
    "v_accvgpr_write_b32 a5, v109\n\t"
    "v_accvgpr_write_b32 a6, v110\n\t"
    "v_mfma_f32_16x16x32_bf16 v[88:91], v[16:19], v[4:7], v[88:91]\n\t"
    "v_accvgpr_write_b32 a7, v111\n\t"
    "buffer_load_dwordx4 v200, s[40:43], s65 offen offset:1024 nt lds\n\t"
    "s_waitcnt lgkmcnt(1)\n\t"
    "v_mfma_f32_16x16x32_bf16 v[88:91], v[0:3], v[20:23], v[88:91]\n\t"
    "v_lshlrev_b32 v76, 16, v44\n\t"
    "v_and_b32 v77, 0xffff0000, v44\n\t"
    "v_lshlrev_b32 v78, 16, v45\n\t"
    "v_mfma_f32_16x16x32_bf16 v[88:91], v[16:19], v[20:23], v[88:91]\n\t"
    "v_and_b32 v79, 0xffff0000, v45\n\t"
    "v_lshlrev_b32 v92, 16, v46\n\t"
    "v_and_b32 v93, 0xffff0000, v46\n\t"
    "v_mfma_f32_16x16x32_bf16 v[104:107], v[0:3], v[4:7], v[104:107]\n\t"
    "v_lshlrev_b32 v94, 16, v47\n\t"
    "v_and_b32 v95, 0xffff0000, v47\n\t"
    "v_mul_f32 v76, s72, v76\n\t"
    "v_mfma_f32_16x16x32_bf16 v[104:107], v[16:19], v[4:7], v[104:107]\n\t"
    "v_mul_f32 v77, s72, v77\n\t"
    "v_mul_f32 v78, s72, v78\n\t"
    "v_mul_f32 v79, s72, v79\n\t"
    "v_mfma_f32_16x16x32_bf16 v[104:107], v[0:3], v[20:23], v[104:107]\n\t"
    "v_mul_f32 v92, s72, v92\n\t"
    "v_mul_f32 v93, s72, v93\n\t"
    "v_mul_f32 v94, s72, v94\n\t"
    "v_mfma_f32_16x16x32_bf16 v[104:107], v[16:19], v[20:23], v[104:107]\n\t"
    "v_mul_f32 v95, s72, v95\n\t"
    "v_cvt_pk_bf16_f32 v108, v76, v77\n\t"
    "v_cvt_pk_bf16_f32 v109, v78, v79\n\t"
    "v_mfma_f32_16x16x32_bf16 v[120:123], v[0:3], v[4:7], v[120:123]\n\t"
    "v_cvt_pk_bf16_f32 v110, v92, v93\n\t"
    "v_cvt_pk_bf16_f32 v111, v94, v95\n\t"
    "v_accvgpr_write_b32 a8, v108\n\t"
    "v_mfma_f32_16x16x32_bf16 v[120:123], v[16:19], v[4:7], v[120:123]\n\t"
    "v_accvgpr_write_b32 a9, v109\n\t"
    "v_accvgpr_write_b32 a10, v110\n\t"
    "v_accvgpr_write_b32 a11, v111\n\t"
    "v_mfma_f32_16x16x32_bf16 v[120:123], v[0:3], v[20:23], v[120:123]\n\t"
    "buffer_load_dwordx4 v201, s[40:43], s65 offen offset:2048 nt lds\n\t"
    "s_waitcnt lgkmcnt(0)\n\t"
    "v_lshlrev_b32 v76, 16, v60\n\t"
    "v_mfma_f32_16x16x32_bf16 v[120:123], v[16:19], v[20:23], v[120:123]\n\t"
    "v_and_b32 v77, 0xffff0000, v60\n\t"
    "v_lshlrev_b32 v78, 16, v61\n\t"
    "v_and_b32 v79, 0xffff0000, v61\n\t"
    "v_mfma_f32_16x16x32_bf16 v[8:11], v[0:3], v[4:7], v[8:11]\n\t"
    "v_lshlrev_b32 v92, 16, v62\n\t"
    "v_and_b32 v93, 0xffff0000, v62\n\t"
    "v_lshlrev_b32 v94, 16, v63\n\t"
    "v_mfma_f32_16x16x32_bf16 v[8:11], v[16:19], v[4:7], v[8:11]\n\t"
    "v_and_b32 v95, 0xffff0000, v63\n\t"
    "v_mul_f32 v76, s72, v76\n\t"
    "v_mul_f32 v77, s72, v77\n\t"
    "v_mfma_f32_16x16x32_bf16 v[8:11], v[0:3], v[20:23], v[8:11]\n\t"
    "v_mul_f32 v78, s72, v78\n\t"
    "v_mul_f32 v79, s72, v79\n\t"
    "v_mul_f32 v92, s72, v92\n\t"
    "v_mfma_f32_16x16x32_bf16 v[8:11], v[16:19], v[20:23], v[8:11]\n\t"
    "v_mul_f32 v93, s72, v93\n\t"
    "v_mul_f32 v94, s72, v94\n\t"
    "v_mul_f32 v95, s72, v95\n\t"
    "v_mfma_f32_16x16x32_bf16 v[24:27], v[0:3], v[4:7], v[24:27]\n\t"
    "v_cvt_pk_bf16_f32 v108, v76, v77\n\t"
    "v_cvt_pk_bf16_f32 v109, v78, v79\n\t"
    "v_cvt_pk_bf16_f32 v110, v92, v93\n\t"
    "v_mfma_f32_16x16x32_bf16 v[24:27], v[16:19], v[4:7], v[24:27]\n\t"
    "v_cvt_pk_bf16_f32 v111, v94, v95\n\t"
    "v_accvgpr_write_b32 a12, v108\n\t"
    "v_accvgpr_write_b32 a13, v109\n\t"
    "v_mfma_f32_16x16x32_bf16 v[24:27], v[0:3], v[20:23], v[24:27]\n\t"
    "v_accvgpr_write_b32 a14, v110\n\t"
    "v_accvgpr_write_b32 a15, v111\n\t"
    "buffer_load_dwordx4 v202, s[40:43], s65 offen offset:3072 nt lds\n\t"
    "v_mfma_f32_16x16x32_bf16 v[24:27], v[16:19], v[20:23], v[24:27]\n\t"
    "s_add_u32 s65, s65, 16384\n\t"
    "s_waitcnt vmcnt(8)\n\t"
    "s_barrier\n\t"
    "v_mfma_f32_16x16x32_bf16 v[40:43], v[0:3], v[4:7], v[40:43]\n\t"
    "ds_read_b128 a[64:67], v196 offset:0\n\t"
    "ds_read_b128 a[68:71], v196 offset:256\n\t"
    "ds_read_b128 a[72:75], v196 offset:512\n\t"
    "v_mfma_f32_16x16x32_bf16 v[40:43], v[16:19], v[4:7], v[40:43]\n\t"
    "ds_read_b128 a[76:79], v196 offset:768\n\t"
    "ds_read_b128 a[80:83], v196 offset:4096\n\t"
    "ds_read_b128 a[84:87], v196 offset:4352\n\t"
    "v_mfma_f32_16x16x32_bf16 v[40:43], v[0:3], v[20:23], v[40:43]\n\t"
    "ds_read_b128 a[88:91], v196 offset:4608\n\t"
    "ds_read_b128 a[92:95], v196 offset:4864\n\t"
    "ds_read_b128 a[96:99], v196 offset:8192\n\t"
    "v_mfma_f32_16x16x32_bf16 v[40:43], v[16:19], v[20:23], v[40:43]\n\t"
    "ds_read_b128 a[100:103], v196 offset:8448\n\t"
    "ds_read_b128 a[104:107], v196 offset:8704\n\t"
    "ds_read_b128 a[108:111], v196 offset:8960\n\t"
    "v_mfma_f32_16x16x32_bf16 v[56:59], v[0:3], v[4:7], v[56:59]\n\t"
    "ds_read_b128 a[112:115], v196 offset:12288\n\t"
    "ds_read_b128 a[116:119], v196 offset:12544\n\t"
    "ds_read_b128 a[120:123], v196 offset:12800\n\t"
    "v_mfma_f32_16x16x32_bf16 v[56:59], v[16:19], v[4:7], v[56:59]\n\t"
    "ds_read_b128 a[124:127], v196 offset:13056\n\t"
    "s_waitcnt vmcnt(4)\n\t"
    "s_add_u32 s70, s88, 8192\n\t"
    "v_mfma_f32_16x16x32_bf16 v[56:59], v[0:3], v[20:23], v[56:59]\n\t"
    "s_add_u32 m0, s71, 8192\n\t"
    "s_nop 0\n\t"
    "ds_read_b128 v[12:15], v246 offset:4096\n\t"
    "v_mfma_f32_16x16x32_bf16 v[56:59], v[16:19], v[20:23], v[56:59]\n\t"
    "ds_read_b128 v[28:31], v246 offset:4352\n\t"
    "ds_read_b128 v[44:47], v246 offset:4608\n\t"
    "ds_read_b128 v[60:63], v246 offset:4864\n\t"
    "v_mfma_f32_16x16x32_bf16 v[72:75], v[0:3], v[4:7], v[72:75]\n\t"
    "s_waitcnt lgkmcnt(3)\n\t"
    "v_lshlrev_b32 v76, 16, v12\n\t"
    "v_and_b32 v77, 0xffff0000, v12\n\t"
    "v_mfma_f32_16x16x32_bf16 v[72:75], v[16:19], v[4:7], v[72:75]\n\t"
    "v_lshlrev_b32 v78, 16, v13\n\t"
    "v_and_b32 v79, 0xffff0000, v13\n\t"
    "v_lshlrev_b32 v92, 16, v14\n\t"
    "v_mfma_f32_16x16x32_bf16 v[72:75], v[0:3], v[20:23], v[72:75]\n\t"
    "v_and_b32 v93, 0xffff0000, v14\n\t"
    "v_lshlrev_b32 v94, 16, v15\n\t"
    "v_and_b32 v95, 0xffff0000, v15\n\t"
    "v_mfma_f32_16x16x32_bf16 v[72:75], v[16:19], v[20:23], v[72:75]\n\t"
    "v_mul_f32 v76, s72, v76\n\t"
    "v_mul_f32 v77, s72, v77\n\t"
    "v_mul_f32 v78, s72, v78\n\t"
    "v_mfma_f32_16x16x32_bf16 v[88:91], v[0:3], v[4:7], v[88:91]\n\t"
    "v_mul_f32 v79, s72, v79\n\t"
    "v_mul_f32 v92, s72, v92\n\t"
    "v_mul_f32 v93, s72, v93\n\t"
    "v_mfma_f32_16x16x32_bf16 v[88:91], v[16:19], v[4:7], v[88:91]\n\t"
    "v_mul_f32 v94, s72, v94\n\t"
    "v_mul_f32 v95, s72, v95\n\t"
    "v_cvt_pk_bf16_f32 v108, v76, v77\n\t"
    "v_mfma_f32_16x16x32_bf16 v[88:91], v[0:3], v[20:23], v[88:91]\n\t"
    "v_cvt_pk_bf16_f32 v109, v78, v79\n\t"
    "v_cvt_pk_bf16_f32 v110, v92, v93\n\t"
    "v_cvt_pk_bf16_f32 v111, v94, v95\n\t"
    "v_mfma_f32_16x16x32_bf16 v[88:91], v[16:19], v[20:23], v[88:91]\n\t"
    "v_accvgpr_write_b32 a16, v108\n\t"
    "v_accvgpr_write_b32 a17, v109\n\t"
    "v_accvgpr_write_b32 a18, v110\n\t"
    "v_mfma_f32_16x16x32_bf16 v[104:107], v[0:3], v[4:7], v[104:107]\n\t"
    "v_accvgpr_write_b32 a19, v111\n\t"
    "buffer_load_dwordx4 v199, s[44:47], s70 offen offset:0 nt lds\n\t"
    "s_waitcnt lgkmcnt(2)\n\t"
    "v_mfma_f32_16x16x32_bf16 v[104:107], v[16:19], v[4:7], v[104:107]\n\t"
    "v_lshlrev_b32 v76, 16, v28\n\t"
    "v_and_b32 v77, 0xffff0000, v28\n\t"
    "v_lshlrev_b32 v78, 16, v29\n\t"
    "v_mfma_f32_16x16x32_bf16 v[104:107], v[0:3], v[20:23], v[104:107]\n\t"
    "v_and_b32 v79, 0xffff0000, v29\n\t"
    "v_lshlrev_b32 v92, 16, v30\n\t"
    "v_and_b32 v93, 0xffff0000, v30\n\t"
    "v_mfma_f32_16x16x32_bf16 v[104:107], v[16:19], v[20:23], v[104:107]\n\t"
    "v_lshlrev_b32 v94, 16, v31\n\t"
    "v_and_b32 v95, 0xffff0000, v31\n\t"
    "v_mul_f32 v76, s72, v76\n\t"
    "v_mfma_f32_16x16x32_bf16 v[120:123], v[0:3], v[4:7], v[120:123]\n\t"
    "v_mul_f32 v77, s72, v77\n\t"
    "v_mul_f32 v78, s72, v78\n\t"
    "v_mul_f32 v79, s72, v79\n\t"
    "v_mfma_f32_16x16x32_bf16 v[120:123], v[16:19], v[4:7], v[120:123]\n\t"
    "v_mul_f32 v92, s72, v92\n\t"
    "v_mul_f32 v93, s72, v93\n\t"
    "v_mul_f32 v94, s72, v94\n\t"
    "v_mfma_f32_16x16x32_bf16 v[120:123], v[0:3], v[20:23], v[120:123]\n\t"
    "v_mul_f32 v95, s72, v95\n\t"
    "v_cvt_pk_bf16_f32 v108, v76, v77\n\t"
    "v_cvt_pk_bf16_f32 v109, v78, v79\n\t"
    "v_mfma_f32_16x16x32_bf16 v[120:123], v[16:19], v[20:23], v[120:123]\n\t"
    "v_cvt_pk_bf16_f32 v110, v92, v93\n\t"
    "v_cvt_pk_bf16_f32 v111, v94, v95\n\t"
    "v_accvgpr_write_b32 a20, v108\n\t"
    "v_mfma_f32_16x16x32_bf16 v[8:11], v[0:3], v[4:7], v[8:11]\n\t"
    "v_accvgpr_write_b32 a21, v109\n\t"
    "v_accvgpr_write_b32 a22, v110\n\t"
    "v_accvgpr_write_b32 a23, v111\n\t"
    "v_mfma_f32_16x16x32_bf16 v[8:11], v[16:19], v[4:7], v[8:11]\n\t"
    "buffer_load_dwordx4 v200, s[44:47], s70 offen offset:1024 nt lds\n\t"
    "s_waitcnt lgkmcnt(1)\n\t"
    "v_lshlrev_b32 v76, 16, v44\n\t"
    "v_mfma_f32_16x16x32_bf16 v[8:11], v[0:3], v[20:23], v[8:11]\n\t"
    "v_and_b32 v77, 0xffff0000, v44\n\t"
    "v_lshlrev_b32 v78, 16, v45\n\t"
    "v_and_b32 v79, 0xffff0000, v45\n\t"
    "v_mfma_f32_16x16x32_bf16 v[8:11], v[16:19], v[20:23], v[8:11]\n\t"
    "v_lshlrev_b32 v92, 16, v46\n\t"
    "v_and_b32 v93, 0xffff0000, v46\n\t"
    "v_lshlrev_b32 v94, 16, v47\n\t"
    "v_mfma_f32_16x16x32_bf16 v[24:27], v[0:3], v[4:7], v[24:27]\n\t"
    "v_and_b32 v95, 0xffff0000, v47\n\t"
    "v_mul_f32 v76, s72, v76\n\t"
    "v_mul_f32 v77, s72, v77\n\t"
    "v_mfma_f32_16x16x32_bf16 v[24:27], v[16:19], v[4:7], v[24:27]\n\t"
    "v_mul_f32 v78, s72, v78\n\t"
    "v_mul_f32 v79, s72, v79\n\t"
    "v_mul_f32 v92, s72, v92\n\t"
    "v_mfma_f32_16x16x32_bf16 v[24:27], v[0:3], v[20:23], v[24:27]\n\t"
    "v_mul_f32 v93, s72, v93\n\t"
    "v_mul_f32 v94, s72, v94\n\t"
    "v_mul_f32 v95, s72, v95\n\t"
    "v_mfma_f32_16x16x32_bf16 v[24:27], v[16:19], v[20:23], v[24:27]\n\t"
    "v_cvt_pk_bf16_f32 v108, v76, v77\n\t"
    "v_cvt_pk_bf16_f32 v109, v78, v79\n\t"
    "v_cvt_pk_bf16_f32 v110, v92, v93\n\t"
    "v_mfma_f32_16x16x32_bf16 v[40:43], v[0:3], v[4:7], v[40:43]\n\t"
    "v_cvt_pk_bf16_f32 v111, v94, v95\n\t"
    "v_accvgpr_write_b32 a24, v108\n\t"
    "v_accvgpr_write_b32 a25, v109\n\t"
    "v_mfma_f32_16x16x32_bf16 v[40:43], v[16:19], v[4:7], v[40:43]\n\t"
    "v_accvgpr_write_b32 a26, v110\n\t"
    "v_accvgpr_write_b32 a27, v111\n\t"
    "buffer_load_dwordx4 v201, s[44:47], s70 offen offset:2048 nt lds\n\t"
    "v_mfma_f32_16x16x32_bf16 v[40:43], v[0:3], v[20:23], v[40:43]\n\t"
    "s_waitcnt lgkmcnt(0)\n\t"
    "v_lshlrev_b32 v76, 16, v60\n\t"
    "v_and_b32 v77, 0xffff0000, v60\n\t"
    "v_mfma_f32_16x16x32_bf16 v[40:43], v[16:19], v[20:23], v[40:43]\n\t"
    "v_lshlrev_b32 v78, 16, v61\n\t"
    "v_and_b32 v79, 0xffff0000, v61\n\t"
    "v_lshlrev_b32 v92, 16, v62\n\t"
    "v_mfma_f32_16x16x32_bf16 v[56:59], v[0:3], v[4:7], v[56:59]\n\t"
    "v_and_b32 v93, 0xffff0000, v62\n\t"
    "v_lshlrev_b32 v94, 16, v63\n\t"
    "v_and_b32 v95, 0xffff0000, v63\n\t"
    "v_mfma_f32_16x16x32_bf16 v[56:59], v[16:19], v[4:7], v[56:59]\n\t"
    "v_mul_f32 v76, s72, v76\n\t"
    "v_mul_f32 v77, s72, v77\n\t"
    "v_mul_f32 v78, s72, v78\n\t"
    "v_mfma_f32_16x16x32_bf16 v[56:59], v[0:3], v[20:23], v[56:59]\n\t"
    "v_mul_f32 v79, s72, v79\n\t"
    "v_mul_f32 v92, s72, v92\n\t"
    "v_mul_f32 v93, s72, v93\n\t"
    "v_mfma_f32_16x16x32_bf16 v[56:59], v[16:19], v[20:23], v[56:59]\n\t"
    "v_mul_f32 v94, s72, v94\n\t"
    "v_mul_f32 v95, s72, v95\n\t"
    "v_cvt_pk_bf16_f32 v108, v76, v77\n\t"
    "v_mfma_f32_16x16x32_bf16 v[72:75], v[0:3], v[4:7], v[72:75]\n\t"
    "v_cvt_pk_bf16_f32 v109, v78, v79\n\t"
    "v_cvt_pk_bf16_f32 v110, v92, v93\n\t"
    "v_cvt_pk_bf16_f32 v111, v94, v95\n\t"
    "v_mfma_f32_16x16x32_bf16 v[72:75], v[16:19], v[4:7], v[72:75]\n\t"
    "v_accvgpr_write_b32 a28, v108\n\t"
    "v_accvgpr_write_b32 a29, v109\n\t"
    "v_accvgpr_write_b32 a30, v110\n\t"
    "v_mfma_f32_16x16x32_bf16 v[72:75], v[0:3], v[20:23], v[72:75]\n\t"
    "v_accvgpr_write_b32 a31, v111\n\t"
    "buffer_load_dwordx4 v202, s[44:47], s70 offen offset:3072 nt lds\n\t"
    "s_nop 1\n\t"
    "v_mfma_f32_16x16x32_bf16 v[128:131], a[64:67], a[0:3], 0\n\t"
    "v_mfma_f32_16x16x32_bf16 v[128:131], a[68:71], a[4:7], v[128:131]\n\t"
    "v_mfma_f32_16x16x32_bf16 v[128:131], a[72:75], a[8:11], v[128:131]\n\t"
    "v_mfma_f32_16x16x32_bf16 v[128:131], a[76:79], a[12:15], v[128:131]\n\t"
    "v_mfma_f32_16x16x32_bf16 v[132:135], a[64:67], a[16:19], 0\n\t"
    "v_mfma_f32_16x16x32_bf16 v[132:135], a[68:71], a[20:23], v[132:135]\n\t"
    "v_mfma_f32_16x16x32_bf16 v[132:135], a[72:75], a[24:27], v[132:135]\n\t"
    "v_mfma_f32_16x16x32_bf16 v[132:135], a[76:79], a[28:31], v[132:135]\n\t"
    "s_add_u32 s70, s88, 12288\n\t"
    "v_mfma_f32_16x16x32_bf16 v[72:75], v[16:19], v[20:23], v[72:75]\n\t"
    "s_add_u32 m0, s71, 12288\n\t"
    "s_nop 0\n\t"
    "buffer_load_dwordx4 v199, s[44:47], s70 offen offset:0 nt lds\n\t"
    "v_mfma_f32_16x16x32_bf16 v[88:91], v[0:3], v[4:7], v[88:91]\n\t"
    "buffer_load_dwordx4 v200, s[44:47], s70 offen offset:1024 nt lds\n\t"
    "buffer_load_dwordx4 v201, s[44:47], s70 offen offset:2048 nt lds\n\t"
    "buffer_load_dwordx4 v202, s[44:47], s70 offen offset:3072 nt lds\n\t"
    "v_mfma_f32_16x16x32_bf16 v[88:91], v[16:19], v[4:7], v[88:91]\n\t"
    "s_nop 0\n\t"
    "v_max3_f32 v236, v128, v129, v130\n\t"
    "v_max3_f32 v237, v132, v133, v134\n\t"
    "v_mfma_f32_16x16x32_bf16 v[88:91], v[0:3], v[20:23], v[88:91]\n\t"
    "v_max_f32 v236, v236, v131\n\t"
    "v_max_f32 v237, v237, v135\n\t"
    "v_mov_b32 v240, v236\n\t"
    "v_mfma_f32_16x16x32_bf16 v[88:91], v[16:19], v[20:23], v[88:91]\n\t"
    "v_mov_b32 v241, v237\n\t"
    "s_nop 0\n\t"
    "v_permlane16_swap_b32 v236, v240\n\t"
    "v_mfma_f32_16x16x32_bf16 v[104:107], v[0:3], v[4:7], v[104:107]\n\t"
    "v_permlane16_swap_b32 v237, v241\n\t"
    "v_max_f32 v236, v236, v240\n\t"
    "v_max_f32 v237, v237, v241\n\t"
    "v_mfma_f32_16x16x32_bf16 v[104:107], v[16:19], v[4:7], v[104:107]\n\t"
    "v_mov_b32 v240, v236\n\t"
    "v_mov_b32 v241, v237\n\t"
    "s_nop 0\n\t"
    "v_mfma_f32_16x16x32_bf16 v[104:107], v[0:3], v[20:23], v[104:107]\n\t"
    "v_permlane32_swap_b32 v236, v240\n\t"
    "v_permlane32_swap_b32 v237, v241\n\t"
    "v_max_f32 v236, v236, v240\n\t"
    "v_mfma_f32_16x16x32_bf16 v[104:107], v[16:19], v[20:23], v[104:107]\n\t"
    "v_max_f32 v237, v237, v241\n\t"
    "s_mov_b64 exec, s[92:93]\n\t"
    "v_sub_f32 v160, 0, v236\n\t"
    "v_sub_f32 v161, 0, v236\n\t"
    "v_sub_f32 v162, 0, v236\n\t"
    "v_sub_f32 v163, 0, v236\n\t"
    "v_sub_f32 v164, 0, v237\n\t"
    "v_sub_f32 v165, 0, v237\n\t"
    "v_sub_f32 v166, 0, v237\n\t"
    "v_sub_f32 v167, 0, v237\n\t"
    "s_mov_b64 exec, -1\n\t"
    "s_nop 7\n\t"
    "v_mfma_f32_16x16x32_bf16 v[128:131], a[64:67], a[0:3], v[160:163]\n\t"
    "v_mfma_f32_16x16x32_bf16 v[128:131], a[68:71], a[4:7], v[128:131]\n\t"
    "v_mfma_f32_16x16x32_bf16 v[128:131], a[72:75], a[8:11], v[128:131]\n\t"
    "v_mfma_f32_16x16x32_bf16 v[128:131], a[76:79], a[12:15], v[128:131]\n\t"
    "v_mfma_f32_16x16x32_bf16 v[132:135], a[92:95], a[12:15], v[160:163]\n\t"
    "v_mfma_f32_16x16x32_bf16 v[132:135], a[88:91], a[8:11], v[132:135]\n\t"
    "v_mfma_f32_16x16x32_bf16 v[132:135], a[84:87], a[4:7], v[132:135]\n\t"
    "v_mfma_f32_16x16x32_bf16 v[132:135], a[80:83], a[0:3], v[132:135]\n\t"
    "v_mfma_f32_16x16x32_bf16 v[136:139], a[96:99], a[0:3], v[160:163]\n\t"
    "s_nop 1\n\t"
    "v_mfma_f32_16x16x32_bf16 v[136:139], a[100:103], a[4:7], v[136:139]\n\t"
    "v_exp_f32 v128, v128\n\t"
    "v_mfma_f32_16x16x32_bf16 v[136:139], a[104:107], a[8:11], v[136:139]\n\t"
    "v_mfma_f32_16x16x32_bf16 v[136:139], a[108:111], a[12:15], v[136:139]\n\t"
    "v_exp_f32 v129, v129\n\t"
    "v_mfma_f32_16x16x32_bf16 v[140:143], a[124:127], a[12:15], v[160:163]\n\t"
    "v_cvt_pk_bf16_f32 v144, v128, v129\n\t"
    "v_mfma_f32_16x16x32_bf16 v[140:143], a[120:123], a[8:11], v[140:143]\n\t"
    "v_exp_f32 v130, v130\n\t"
    "v_mfma_f32_16x16x32_bf16 v[140:143], a[116:119], a[4:7], v[140:143]\n\t"
    "v_mfma_f32_16x16x32_bf16 v[140:143], a[112:115], a[0:3], v[140:143]\n\t"
    "v_exp_f32 v131, v131\n\t"
    "s_waitcnt vmcnt(8)\n\t"
    "s_barrier\n\t"
    "s_add_u32 m0, s64, 16384\n\t"
    "s_nop 0\n\t"
    "buffer_load_dwordx4 v199, s[36:39], s65 offen offset:0 nt lds\n\t"
    "buffer_load_dwordx4 v200, s[36:39], s65 offen offset:1024 nt lds\n\t"
    "buffer_load_dwordx4 v201, s[36:39], s65 offen offset:2048 nt lds\n\t"
    "buffer_load_dwordx4 v202, s[36:39], s65 offen offset:3072 nt lds\n\t"
    "ds_read_b64_tr_b16 a[128:129], v197 offset:0\n\t"
    "v_cvt_pk_bf16_f32 v145, v130, v131\n\t"
    "s_nop 0\n\t"
    "v_exp_f32 v132, v132\n\t"
    "ds_read_b64_tr_b16 a[130:131], v197 offset:4096\n\t"
    "ds_read_b64_tr_b16 a[132:133], v197 offset:8192\n\t"
    "s_nop 0\n\t"
    "v_exp_f32 v133, v133\n\t"
    "ds_read_b64_tr_b16 a[134:135], v197 offset:12288\n\t"
    "v_cvt_pk_bf16_f32 v146, v132, v133\n\t"
    "s_nop 0\n\t"
    "v_exp_f32 v134, v134\n\t"
    "ds_read_b64_tr_b16 a[136:137], v198 offset:0\n\t"
    "ds_read_b64_tr_b16 a[138:139], v198 offset:4096\n\t"
    "s_nop 0\n\t"
    "v_exp_f32 v135, v135\n\t"
    "ds_read_b64_tr_b16 a[140:141], v198 offset:8192\n\t"
    "v_cvt_pk_bf16_f32 v147, v134, v135\n\t"
    "s_nop 0\n\t"
    "v_exp_f32 v136, v136\n\t"
    "ds_read_b64_tr_b16 a[142:143], v198 offset:12288\n\t"
    "ds_read_b64_tr_b16 a[144:145], v197 offset:256\n\t"
    "s_nop 0\n\t"
    "v_exp_f32 v137, v137\n\t"
    "ds_read_b64_tr_b16 a[146:147], v197 offset:4352\n\t"
    "v_cvt_pk_bf16_f32 v148, v136, v137\n\t"
    "s_nop 0\n\t"
    "v_exp_f32 v138, v138\n\t"
    "ds_read_b64_tr_b16 a[148:149], v197 offset:8448\n\t"
    "ds_read_b64_tr_b16 a[150:151], v197 offset:12544\n\t"
    "ds_read_b64_tr_b16 a[154:155], v198 offset:4352\n\t"
    "ds_read_b64_tr_b16 a[156:157], v198 offset:8448\n\t"
    "ds_read_b64_tr_b16 a[158:159], v198 offset:12544\n\t"
    "ds_read_b64_tr_b16 a[160:161], v197 offset:512\n\t"
    "ds_read_b64_tr_b16 a[162:163], v197 offset:4608\n\t"
    "s_nop 0\n\t"
    "v_exp_f32 v139, v139\n\t"
    "ds_read_b64_tr_b16 a[152:153], v198 offset:256\n\t"
    "v_cvt_pk_bf16_f32 v149, v138, v139\n\t"
    "v_mfma_f32_16x16x32_bf16 v[128:131], a[64:67], a[16:19], v[164:167]\n\t"
    "ds_read_b64_tr_b16 a[164:165], v197 offset:8704\n\t"
    "ds_read_b64_tr_b16 a[166:167], v197 offset:12800\n\t"
    "v_mfma_f32_16x16x32_bf16 v[128:131], a[68:71], a[20:23], v[128:131]\n\t"
    "v_exp_f32 v140, v140\n\t"
    "v_mfma_f32_16x16x32_bf16 v[128:131], a[72:75], a[24:27], v[128:131]\n\t"
    "ds_read_b64_tr_b16 a[168:169], v198 offset:512\n\t"
    "ds_read_b64_tr_b16 a[170:171], v198 offset:4608\n\t"
    "v_mfma_f32_16x16x32_bf16 v[128:131], a[76:79], a[28:31], v[128:131]\n\t"
    "v_exp_f32 v141, v141\n\t"
    "v_mfma_f32_16x16x32_bf16 v[132:135], a[92:95], a[28:31], v[164:167]\n\t"
    "ds_read_b64_tr_b16 a[172:173], v198 offset:8704\n\t"
    "v_cvt_pk_bf16_f32 v150, v140, v141\n\t"
    "s_waitcnt vmcnt(8)\n\t"
    "ds_read_b128 v[12:15], v246 offset:8192\n\t"
    "ds_read_b128 v[28:31], v246 offset:8448\n\t"
    "ds_read_b128 v[44:47], v246 offset:8704\n\t"
    "ds_read_b128 v[60:63], v246 offset:8960\n\t"
    "v_mfma_f32_16x16x32_bf16 v[132:135], a[88:91], a[24:27], v[132:135]\n\t"
    "v_exp_f32 v142, v142\n\t"
    "s_waitcnt lgkmcnt(3)\n\t"
    "v_lshlrev_b32 v76, 16, v12\n\t"
    "v_and_b32 v77, 0xffff0000, v12\n\t"
    "v_lshlrev_b32 v78, 16, v13\n\t"
    "v_mfma_f32_16x16x32_bf16 v[132:135], a[84:87], a[20:23], v[132:135]\n\t"
    "ds_read_b64_tr_b16 a[174:175], v198 offset:12800\n\t"
    "ds_read_b64_tr_b16 a[176:177], v197 offset:768\n\t"
    "v_and_b32 v79, 0xffff0000, v13\n\t"
    "v_lshlrev_b32 v92, 16, v14\n\t"
    "v_and_b32 v93, 0xffff0000, v14\n\t"
    "v_lshlrev_b32 v94, 16, v15\n\t"
    "v_mfma_f32_16x16x32_bf16 v[132:135], a[80:83], a[16:19], v[132:135]\n\t"
    "v_exp_f32 v143, v143\n\t"
    "v_and_b32 v95, 0xffff0000, v15\n\t"
    "v_mul_f32 v76, s72, v76\n\t"
    "v_mul_f32 v77, s72, v77\n\t"
    "v_mul_f32 v78, s72, v78\n\t"
    "v_mfma_f32_16x16x32_bf16 v[136:139], a[96:99], a[16:19], v[164:167]\n\t"
    "ds_read_b64_tr_b16 a[178:179], v197 offset:4864\n\t"
    "v_cvt_pk_bf16_f32 v151, v142, v143\n\t"
    "v_mul_f32 v79, s72, v79\n\t"
    "v_mul_f32 v92, s72, v92\n\t"
    "v_mul_f32 v93, s72, v93\n\t"
    "v_mul_f32 v94, s72, v94\n\t"
    "v_mul_f32 v95, s72, v95\n\t"
    "v_mfma_f32_16x16x32_bf16 v[136:139], a[100:103], a[20:23], v[136:139]\n\t"
    "v_exp_f32 v128, v128\n\t"
    "v_cvt_pk_bf16_f32 v108, v76, v77\n\t"
    "v_cvt_pk_bf16_f32 v109, v78, v79\n\t"
    "v_cvt_pk_bf16_f32 v110, v92, v93\n\t"
    "v_cvt_pk_bf16_f32 v111, v94, v95\n\t"
    "v_mfma_f32_16x16x32_bf16 v[136:139], a[104:107], a[24:27], v[136:139]\n\t"
    "ds_read_b64_tr_b16 a[180:181], v197 offset:8960\n\t"
    "ds_read_b64_tr_b16 a[182:183], v197 offset:13056\n\t"
    "v_accvgpr_write_b32 a32, v108\n\t"
    "v_accvgpr_write_b32 a33, v109\n\t"
    "v_accvgpr_write_b32 a34, v110\n\t"
    "v_accvgpr_write_b32 a35, v111\n\t"
    "v_mfma_f32_16x16x32_bf16 v[136:139], a[108:111], a[28:31], v[136:139]\n\t"
    "v_exp_f32 v129, v129\n\t"
    "s_waitcnt lgkmcnt(2)\n\t"
    "v_lshlrev_b32 v76, 16, v28\n\t"
    "v_and_b32 v77, 0xffff0000, v28\n\t"
    "v_lshlrev_b32 v78, 16, v29\n\t"
    "v_mfma_f32_16x16x32_bf16 v[140:143], a[124:127], a[28:31], v[164:167]\n\t"
    "ds_read_b64_tr_b16 a[184:185], v198 offset:768\n\t"
    "v_cvt_pk_bf16_f32 v152, v128, v129\n\t"
    "v_and_b32 v79, 0xffff0000, v29\n\t"
    "v_lshlrev_b32 v92, 16, v30\n\t"
    "v_and_b32 v93, 0xffff0000, v30\n\t"
    "v_lshlrev_b32 v94, 16, v31\n\t"
    "v_and_b32 v95, 0xffff0000, v31\n\t"
    "v_mfma_f32_16x16x32_bf16 v[140:143], a[120:123], a[24:27], v[140:143]\n\t"
    "v_exp_f32 v130, v130\n\t"
    "v_mul_f32 v76, s72, v76\n\t"
    "v_mul_f32 v77, s72, v77\n\t"
    "v_mul_f32 v78, s72, v78\n\t"
    "v_mul_f32 v79, s72, v79\n\t"
    "v_mfma_f32_16x16x32_bf16 v[140:143], a[116:119], a[20:23], v[140:143]\n\t"
    "ds_read_b64_tr_b16 a[186:187], v198 offset:4864\n\t"
    "ds_read_b64_tr_b16 a[188:189], v198 offset:8960\n\t"
    "v_mul_f32 v92, s72, v92\n\t"
    "v_mul_f32 v93, s72, v93\n\t"
    "v_mul_f32 v94, s72, v94\n\t"
    "v_mul_f32 v95, s72, v95\n\t"
    "v_mfma_f32_16x16x32_bf16 v[140:143], a[112:115], a[16:19], v[140:143]\n\t"
    "s_waitcnt lgkmcnt(0)\n\t"
    "v_exp_f32 v131, v131\n\t"
    "v_cvt_pk_bf16_f32 v108, v76, v77\n\t"
    "v_cvt_pk_bf16_f32 v109, v78, v79\n\t"
    "v_cvt_pk_bf16_f32 v110, v92, v93\n\t"
    "v_cvt_pk_bf16_f32 v111, v94, v95\n\t"
    "v_mfma_f32_16x16x32_bf16 v[0:3], a[128:131], v[144:147], 0\n\t"
    "ds_read_b64_tr_b16 a[190:191], v198 offset:13056\n\t"
    "v_cvt_pk_bf16_f32 v153, v130, v131\n\t"
    "v_accvgpr_write_b32 a36, v108\n\t"
    "v_accvgpr_write_b32 a37, v109\n\t"
    "v_accvgpr_write_b32 a38, v110\n\t"
    "v_accvgpr_write_b32 a39, v111\n\t"
    "s_waitcnt lgkmcnt(1)\n\t"
    "v_mfma_f32_16x16x32_bf16 v[0:3], a[132:135], v[148:151], v[0:3]\n\t"
    "v_exp_f32 v132, v132\n\t"
    "v_lshlrev_b32 v76, 16, v44\n\t"
    "v_and_b32 v77, 0xffff0000, v44\n\t"
    "v_lshlrev_b32 v78, 16, v45\n\t"
    "v_and_b32 v79, 0xffff0000, v45\n\t"
    "v_mfma_f32_16x16x32_bf16 v[16:19], a[140:143], v[148:151], 0\n\t"
    "v_lshlrev_b32 v92, 16, v46\n\t"
    "v_and_b32 v93, 0xffff0000, v46\n\t"
    "v_lshlrev_b32 v94, 16, v47\n\t"
    "v_and_b32 v95, 0xffff0000, v47\n\t"
    "v_mfma_f32_16x16x32_bf16 v[16:19], a[136:139], v[144:147], v[16:19]\n\t"
    "v_exp_f32 v133, v133\n\t"
    "v_mul_f32 v76, s72, v76\n\t"
    "v_mul_f32 v77, s72, v77\n\t"
    "v_mul_f32 v78, s72, v78\n\t"
    "v_mul_f32 v79, s72, v79\n\t"
    "v_mfma_f32_16x16x32_bf16 v[32:35], a[144:147], v[144:147], 0\n\t"
    "v_cvt_pk_bf16_f32 v154, v132, v133\n\t"
    "v_mul_f32 v92, s72, v92\n\t"
    "v_mul_f32 v93, s72, v93\n\t"
    "v_mul_f32 v94, s72, v94\n\t"
    "v_mul_f32 v95, s72, v95\n\t"
    "v_cvt_pk_bf16_f32 v108, v76, v77\n\t"
    "v_mfma_f32_16x16x32_bf16 v[32:35], a[148:151], v[148:151], v[32:35]\n\t"
    "v_exp_f32 v134, v134\n\t"
    "v_cvt_pk_bf16_f32 v109, v78, v79\n\t"
    "v_cvt_pk_bf16_f32 v110, v92, v93\n\t"
    "v_cvt_pk_bf16_f32 v111, v94, v95\n\t"
    "v_accvgpr_write_b32 a40, v108\n\t"
    "v_mfma_f32_16x16x32_bf16 v[48:51], a[156:159], v[148:151], 0\n\t"
    "v_accvgpr_write_b32 a41, v109\n\t"
    "v_accvgpr_write_b32 a42, v110\n\t"
    "v_accvgpr_write_b32 a43, v111\n\t"
    "s_waitcnt lgkmcnt(0)\n\t"
    "v_mfma_f32_16x16x32_bf16 v[48:51], a[152:155], v[144:147], v[48:51]\n\t"
    "v_exp_f32 v135, v135\n\t"
    "v_lshlrev_b32 v76, 16, v60\n\t"
    "v_and_b32 v77, 0xffff0000, v60\n\t"
    "v_lshlrev_b32 v78, 16, v61\n\t"
    "v_and_b32 v79, 0xffff0000, v61\n\t"
    "v_mfma_f32_16x16x32_bf16 v[64:67], a[160:163], v[144:147], 0\n\t"
    "v_cvt_pk_bf16_f32 v155, v134, v135\n\t"
    "v_lshlrev_b32 v92, 16, v62\n\t"
    "v_and_b32 v93, 0xffff0000, v62\n\t"
    "v_lshlrev_b32 v94, 16, v63\n\t"
    "v_and_b32 v95, 0xffff0000, v63\n\t"
    "v_mul_f32 v76, s72, v76\n\t"
    "v_mfma_f32_16x16x32_bf16 v[64:67], a[164:167], v[148:151], v[64:67]\n\t"
    "v_exp_f32 v136, v136\n\t"
    "v_mul_f32 v77, s72, v77\n\t"
    "v_mul_f32 v78, s72, v78\n\t"
    "v_mul_f32 v79, s72, v79\n\t"
    "v_mul_f32 v92, s72, v92\n\t"
    "v_mfma_f32_16x16x32_bf16 v[80:83], a[172:175], v[148:151], 0\n\t"
    "v_mul_f32 v93, s72, v93\n\t"
    "v_mul_f32 v94, s72, v94\n\t"
    "v_mul_f32 v95, s72, v95\n\t"
    "v_cvt_pk_bf16_f32 v108, v76, v77\n\t"
    "v_mfma_f32_16x16x32_bf16 v[80:83], a[168:171], v[144:147], v[80:83]\n\t"
    "v_exp_f32 v137, v137\n\t"
    "v_cvt_pk_bf16_f32 v109, v78, v79\n\t"
    "v_cvt_pk_bf16_f32 v110, v92, v93\n\t"
    "v_cvt_pk_bf16_f32 v111, v94, v95\n\t"
    "v_accvgpr_write_b32 a44, v108\n\t"
    "v_mfma_f32_16x16x32_bf16 v[96:99], a[176:179], v[144:147], 0\n\t"
    "v_cvt_pk_bf16_f32 v156, v136, v137\n\t"
    "v_accvgpr_write_b32 a45, v109\n\t"
    "v_accvgpr_write_b32 a46, v110\n\t"
    "v_accvgpr_write_b32 a47, v111\n\t"
    "s_add_u32 m0, s64, 81920\n\ts_nop 0\n\tbuffer_load_dwordx4 v199, s[40:43], s65 offen offset:0 nt lds\n\tbuffer_load_dwordx4 v200, s[40:43], s65 offen offset:1024 nt lds\n\tbuffer_load_dwordx4 v201, s[40:43], s65 offen offset:2048 nt lds\n\tbuffer_load_dwordx4 v202, s[40:43], s65 offen offset:3072 nt lds\n\ts_add_u32 s65, s65, 16384\n\t"
    "s_nop 1\n\t"
    "v_mfma_f32_16x16x32_bf16 v[96:99], a[180:183], v[148:151], v[96:99]\n\t"
    "v_exp_f32 v138, v138\n\t"
    "v_mfma_f32_16x16x32_bf16 v[124:127], a[64:67], a[32:35], 0\n\t"
    "v_mfma_f32_16x16x32_bf16 v[124:127], a[68:71], a[36:39], v[124:127]\n\t"
    "v_mfma_f32_16x16x32_bf16 v[124:127], a[72:75], a[40:43], v[124:127]\n\t"
    "v_mfma_f32_16x16x32_bf16 v[124:127], a[76:79], a[44:47], v[124:127]\n\t"
    "v_mfma_f32_16x16x32_bf16 v[112:115], a[188:191], v[148:151], 0\n\t"
    "s_nop 7\n\t"
    "v_max3_f32 v236, v124, v125, v126\n\t"
    "v_max_f32 v236, v236, v127\n\t"
    "v_mov_b32 v237, v236\n\t"
    "v_mfma_f32_16x16x32_bf16 v[112:115], a[184:187], v[144:147], v[112:115]\n\t"
    "v_exp_f32 v139, v139\n\t"
    "s_nop 1\n\t"
    "v_permlane16_swap_b32 v236, v237\n\t"
    "v_max_f32 v236, v236, v237\n\t"
    "v_mov_b32 v237, v236\n\t"
    "v_smfmac_f32_16x16x64_bf16 v[176:179], v[192:195], v[144:151], v245\n\t"
    "v_cvt_pk_bf16_f32 v157, v138, v139\n\t"
    "s_nop 1\n\t"
    "v_permlane32_swap_b32 v236, v237\n\t"
    "v_max_f32 v236, v236, v237\n\t"
    "s_mov_b64 exec, s[92:93]\n\tv_sub_f32 v168, 0, v236\n\tv_sub_f32 v169, 0, v236\n\tv_sub_f32 v170, 0, v236\n\tv_sub_f32 v171, 0, v236\n\ts_mov_b64 exec, -1\n\ts_nop 3\n\t"
    "v_mfma_f32_16x16x32_bf16 v[128:131], a[64:67], a[32:35], v[168:171]\n\t"
    "s_waitcnt vmcnt(8)\n\t"
    "ds_read_b128 v[12:15], v246 offset:12288\n\t"
    "ds_read_b128 v[28:31], v246 offset:12544\n\t"
    "ds_read_b128 v[44:47], v246 offset:12800\n\t"
    "v_mfma_f32_16x16x32_bf16 v[128:131], a[68:71], a[36:39], v[128:131]\n\t"
    "v_exp_f32 v140, v140\n\t"
    "ds_read_b128 v[60:63], v246 offset:13056\n\t"
    "s_waitcnt lgkmcnt(3)\n\t"
    "v_lshlrev_b32 v76, 16, v12\n\t"
    "v_and_b32 v77, 0xffff0000, v12\n\t"
    "v_mfma_f32_16x16x32_bf16 v[128:131], a[72:75], a[40:43], v[128:131]\n\t"
    "v_lshlrev_b32 v78, 16, v13\n\t"
    "v_and_b32 v79, 0xffff0000, v13\n\t"
    "v_lshlrev_b32 v92, 16, v14\n\t"
    "v_and_b32 v93, 0xffff0000, v14\n\t"
    "v_mfma_f32_16x16x32_bf16 v[128:131], a[76:79], a[44:47], v[128:131]\n\t"
    "v_exp_f32 v141, v141\n\t"
    "v_lshlrev_b32 v94, 16, v15\n\t"
    "v_and_b32 v95, 0xffff0000, v15\n\t"
    "v_mul_f32 v76, s72, v76\n\t"
    "v_mul_f32 v77, s72, v77\n\t"
    "v_mfma_f32_16x16x32_bf16 v[132:135], a[92:95], a[44:47], v[168:171]\n\t"
    "v_cvt_pk_bf16_f32 v158, v140, v141\n\t"
    "v_mul_f32 v78, s72, v78\n\t"
    "v_mul_f32 v79, s72, v79\n\t"
    "v_mul_f32 v92, s72, v92\n\t"
    "v_mfma_f32_16x16x32_bf16 v[132:135], a[88:91], a[40:43], v[132:135]\n\t"
    "v_exp_f32 v142, v142\n\t"
    "v_mul_f32 v93, s72, v93\n\t"
    "v_mul_f32 v94, s72, v94\n\t"
    "v_mul_f32 v95, s72, v95\n\t"
    "v_cvt_pk_bf16_f32 v108, v76, v77\n\t"
    "v_mfma_f32_16x16x32_bf16 v[132:135], a[84:87], a[36:39], v[132:135]\n\t"
    "v_cvt_pk_bf16_f32 v109, v78, v79\n\t"
    "v_cvt_pk_bf16_f32 v110, v92, v93\n\t"
    "v_cvt_pk_bf16_f32 v111, v94, v95\n\t"
    "v_accvgpr_write_b32 a48, v108\n\t"
    "v_mfma_f32_16x16x32_bf16 v[132:135], a[80:83], a[32:35], v[132:135]\n\t"
    "v_exp_f32 v143, v143\n\t"
    "v_accvgpr_write_b32 a49, v109\n\t"
    "v_accvgpr_write_b32 a50, v110\n\t"
    "v_accvgpr_write_b32 a51, v111\n\t"
    "s_waitcnt lgkmcnt(2)\n\t"
    "v_mfma_f32_16x16x32_bf16 v[136:139], a[96:99], a[32:35], v[168:171]\n\t"
    "v_cvt_pk_bf16_f32 v159, v142, v143\n\t"
    "v_lshlrev_b32 v76, 16, v28\n\t"
    "v_and_b32 v77, 0xffff0000, v28\n\t"
    "v_lshlrev_b32 v78, 16, v29\n\t"
    "v_and_b32 v79, 0xffff0000, v29\n\t"
    "v_mfma_f32_16x16x32_bf16 v[136:139], a[100:103], a[36:39], v[136:139]\n\t"
    "v_exp_f32 v128, v128\n\t"
    "v_lshlrev_b32 v92, 16, v30\n\t"
    "v_and_b32 v93, 0xffff0000, v30\n\t"
    "v_lshlrev_b32 v94, 16, v31\n\t"
    "v_mfma_f32_16x16x32_bf16 v[136:139], a[104:107], a[40:43], v[136:139]\n\t"
    "v_and_b32 v95, 0xffff0000, v31\n\t"
    "v_mul_f32 v76, s72, v76\n\t"
    "v_mul_f32 v77, s72, v77\n\t"
    "v_mul_f32 v78, s72, v78\n\t"
    "v_mfma_f32_16x16x32_bf16 v[136:139], a[108:111], a[44:47], v[136:139]\n\t"
    "v_exp_f32 v129, v129\n\t"
    "v_mul_f32 v79, s72, v79\n\t"
    "v_mul_f32 v92, s72, v92\n\t"
    "v_mul_f32 v93, s72, v93\n\t"
    "v_mul_f32 v94, s72, v94\n\t"
    "v_mfma_f32_16x16x32_bf16 v[140:143], a[124:127], a[44:47], v[168:171]\n\t"
    "v_cvt_pk_bf16_f32 v144, v128, v129\n\t"
    "v_mul_f32 v95, s72, v95\n\t"
    "v_cvt_pk_bf16_f32 v108, v76, v77\n\t"
    "v_cvt_pk_bf16_f32 v109, v78, v79\n\t"
    "v_cvt_pk_bf16_f32 v110, v92, v93\n\t"
    "v_mfma_f32_16x16x32_bf16 v[140:143], a[120:123], a[40:43], v[140:143]\n\t"
    "v_exp_f32 v130, v130\n\t"
    "v_cvt_pk_bf16_f32 v111, v94, v95\n\t"
    "v_accvgpr_write_b32 a52, v108\n\t"
    "v_accvgpr_write_b32 a53, v109\n\t"
    "v_accvgpr_write_b32 a54, v110\n\t"
    "v_mfma_f32_16x16x32_bf16 v[140:143], a[116:119], a[36:39], v[140:143]\n\t"
    "v_accvgpr_write_b32 a55, v111\n\t"
    "s_waitcnt lgkmcnt(1)\n\t"
    "v_lshlrev_b32 v76, 16, v44\n\t"
    "v_mfma_f32_16x16x32_bf16 v[140:143], a[112:115], a[32:35], v[140:143]\n\t"
    "v_exp_f32 v131, v131\n\t"
    "v_and_b32 v77, 0xffff0000, v44\n\t"
    "v_lshlrev_b32 v78, 16, v45\n\t"
    "v_and_b32 v79, 0xffff0000, v45\n\t"
    "v_lshlrev_b32 v92, 16, v46\n\t"
    "v_mfma_f32_16x16x32_bf16 v[4:7], a[128:131], v[152:155], 0\n\t"
    "v_cvt_pk_bf16_f32 v145, v130, v131\n\t"
    "v_and_b32 v93, 0xffff0000, v46\n\t"
    "v_lshlrev_b32 v94, 16, v47\n\t"
    "v_and_b32 v95, 0xffff0000, v47\n\t"
    "v_mul_f32 v76, s72, v76\n\t"
    "v_mfma_f32_16x16x32_bf16 v[4:7], a[132:135], v[156:159], v[4:7]\n\t"
    "v_exp_f32 v132, v132\n\t"
    "v_mul_f32 v77, s72, v77\n\t"
    "v_mul_f32 v78, s72, v78\n\t"
    "v_mul_f32 v79, s72, v79\n\t"
    "v_mul_f32 v92, s72, v92\n\t"
    "v_mfma_f32_16x16x32_bf16 v[20:23], a[140:143], v[156:159], 0\n\t"
    "v_mul_f32 v93, s72, v93\n\t"
    "v_mul_f32 v94, s72, v94\n\t"
    "v_mul_f32 v95, s72, v95\n\t"
    "v_mfma_f32_16x16x32_bf16 v[20:23], a[136:139], v[152:155], v[20:23]\n\t"
    "v_exp_f32 v133, v133\n\t"
    "v_cvt_pk_bf16_f32 v108, v76, v77\n\t"
    "v_cvt_pk_bf16_f32 v109, v78, v79\n\t"
    "v_cvt_pk_bf16_f32 v110, v92, v93\n\t"
    "v_cvt_pk_bf16_f32 v111, v94, v95\n\t"
    "v_mfma_f32_16x16x32_bf16 v[36:39], a[144:147], v[152:155], 0\n\t"
    "v_cvt_pk_bf16_f32 v146, v132, v133\n\t"
    "v_accvgpr_write_b32 a56, v108\n\t"
    "v_accvgpr_write_b32 a57, v109\n\t"
    "v_accvgpr_write_b32 a58, v110\n\t"
    "v_accvgpr_write_b32 a59, v111\n\t"
    "v_mfma_f32_16x16x32_bf16 v[36:39], a[148:151], v[156:159], v[36:39]\n\t"
    "v_exp_f32 v134, v134\n\t"
    "s_waitcnt lgkmcnt(0)\n\t"
    "v_lshlrev_b32 v76, 16, v60\n\t"
    "v_and_b32 v77, 0xffff0000, v60\n\t"
    "v_lshlrev_b32 v78, 16, v61\n\t"
    "v_mfma_f32_16x16x32_bf16 v[52:55], a[156:159], v[156:159], 0\n\t"
    "v_and_b32 v79, 0xffff0000, v61\n\t"
    "v_lshlrev_b32 v92, 16, v62\n\t"
    "v_and_b32 v93, 0xffff0000, v62\n\t"
    "v_lshlrev_b32 v94, 16, v63\n\t"
    "v_mfma_f32_16x16x32_bf16 v[52:55], a[152:155], v[152:155], v[52:55]\n\t"
    "v_exp_f32 v135, v135\n\t"
    "v_and_b32 v95, 0xffff0000, v63\n\t"
    "v_mul_f32 v76, s72, v76\n\t"
    "v_mul_f32 v77, s72, v77\n\t"
    "v_mfma_f32_16x16x32_bf16 v[68:71], a[160:163], v[152:155], 0\n\t"
    "v_cvt_pk_bf16_f32 v147, v134, v135\n\t"
    "v_mul_f32 v78, s72, v78\n\t"
    "v_mul_f32 v79, s72, v79\n\t"
    "v_mul_f32 v92, s72, v92\n\t"
    "v_mul_f32 v93, s72, v93\n\t"
    "v_mfma_f32_16x16x32_bf16 v[68:71], a[164:167], v[156:159], v[68:71]\n\t"
    "v_exp_f32 v136, v136\n\t"
    "v_mul_f32 v94, s72, v94\n\t"
    "v_mul_f32 v95, s72, v95\n\t"
    "v_cvt_pk_bf16_f32 v108, v76, v77\n\t"
    "v_cvt_pk_bf16_f32 v109, v78, v79\n\t"
    "v_mfma_f32_16x16x32_bf16 v[84:87], a[172:175], v[156:159], 0\n\t"
    "v_cvt_pk_bf16_f32 v110, v92, v93\n\t"
    "v_cvt_pk_bf16_f32 v111, v94, v95\n\t"
    "v_accvgpr_write_b32 a60, v108\n\t"
    "v_accvgpr_write_b32 a61, v109\n\t"
    "v_mfma_f32_16x16x32_bf16 v[84:87], a[168:171], v[152:155], v[84:87]\n\t"
    "v_exp_f32 v137, v137\n\t"
    "v_accvgpr_write_b32 a62, v110\n\t"
    "v_accvgpr_write_b32 a63, v111\n\t"
    "s_add_u32 m0, s64, 32768\n\ts_nop 0\n\tbuffer_load_dwordx4 v199, s[36:39], s65 offen offset:0 nt lds\n\tbuffer_load_dwordx4 v200, s[36:39], s65 offen offset:1024 nt lds\n\tbuffer_load_dwordx4 v201, s[36:39], s65 offen offset:2048 nt lds\n\tbuffer_load_dwordx4 v202, s[36:39], s65 offen offset:3072 nt lds\n\t"
    "s_nop 1\n\t"
    "v_mfma_f32_16x16x32_bf16 v[100:103], a[176:179], v[152:155], 0\n\t"
    "v_cvt_pk_bf16_f32 v148, v136, v137\n\t"
    "v_mfma_f32_16x16x32_bf16 v[124:127], a[64:67], a[48:51], 0\n\t"
    "v_mfma_f32_16x16x32_bf16 v[124:127], a[68:71], a[52:55], v[124:127]\n\t"
    "v_mfma_f32_16x16x32_bf16 v[124:127], a[72:75], a[56:59], v[124:127]\n\t"
    "v_mfma_f32_16x16x32_bf16 v[100:103], a[180:183], v[156:159], v[100:103]\n\t"
    "v_exp_f32 v138, v138\n\t"
    "v_mfma_f32_16x16x32_bf16 v[124:127], a[76:79], a[60:63], v[124:127]\n\t"
    "s_nop 7\n\t"
    "v_max3_f32 v236, v124, v125, v126\n\t"
    "v_max_f32 v236, v236, v127\n\t"
    "v_mfma_f32_16x16x32_bf16 v[116:119], a[188:191], v[156:159], 0\n\t"
    "v_mov_b32 v237, v236\n\t"
    "s_nop 1\n\t"
    "v_permlane16_swap_b32 v236, v237\n\t"
    "v_max_f32 v236, v236, v237\n\t"
    "v_mfma_f32_16x16x32_bf16 v[116:119], a[184:187], v[152:155], v[116:119]\n\t"
    "v_exp_f32 v139, v139\n\t"
    "v_mov_b32 v237, v236\n\t"
    "s_nop 1\n\t"
    "v_permlane32_swap_b32 v236, v237\n\t"
    "v_max_f32 v236, v236, v237\n\t"
    "v_smfmac_f32_16x16x64_bf16 v[180:183], v[192:195], v[152:159], v245\n\t"
    "v_cvt_pk_bf16_f32 v149, v138, v139\n\t"
    "s_mov_b64 exec, s[92:93]\n\tv_sub_f32 v172, 0, v236\n\tv_sub_f32 v173, 0, v236\n\tv_sub_f32 v174, 0, v236\n\tv_sub_f32 v175, 0, v236\n\ts_mov_b64 exec, -1\n\ts_nop 3\n\t"
    "s_waitcnt vmcnt(8)\n\t"
    "s_barrier\n\t"
    "v_mfma_f32_16x16x32_bf16 v[128:131], a[64:67], a[48:51], v[172:175]\n\t"
    "ds_read_b128 a[64:67], v196 offset:16384\n\t"
    "v_mov_b32 v12, 0\n\t"
    "v_mfma_f32_16x16x32_bf16 v[128:131], a[68:71], a[52:55], v[128:131]\n\t"
    "s_add_u32 m0, s64, 98304\n\t"
    "v_exp_f32 v140, v140\n\t"
    "v_mov_b32 v13, 0\n\t"
    "v_mfma_f32_16x16x32_bf16 v[128:131], a[72:75], a[56:59], v[128:131]\n\t"
    "buffer_load_dwordx4 v199, s[40:43], s65 offen offset:0 nt lds\n\t"
    "ds_read_b128 a[68:71], v196 offset:16640\n\t"
    "v_mov_b32 v14, 0\n\t"
    "v_mfma_f32_16x16x32_bf16 v[128:131], a[76:79], a[60:63], v[128:131]\n\t"
    "v_exp_f32 v141, v141\n\t"
    "v_mov_b32 v15, 0\n\t"
    "v_mfma_f32_16x16x32_bf16 v[132:135], a[92:95], a[60:63], v[172:175]\n\t"
    "ds_read_b128 a[72:75], v196 offset:16896\n\t"
    "v_cvt_pk_bf16_f32 v150, v140, v141\n\t"
    "v_mov_b32 v28, 0\n\t"
    "v_mfma_f32_16x16x32_bf16 v[132:135], a[88:91], a[56:59], v[132:135]\n\t"
    "v_exp_f32 v142, v142\n\t"
    "v_mov_b32 v29, 0\n\t"
    "v_mfma_f32_16x16x32_bf16 v[132:135], a[84:87], a[52:55], v[132:135]\n\t"
    "ds_read_b128 a[76:79], v196 offset:17152\n\t"
    "ds_read_b128 a[92:95], v196 offset:21248\n\t"
    "v_mov_b32 v30, 0\n\t"
    "v_mfma_f32_16x16x32_bf16 v[132:135], a[80:83], a[48:51], v[132:135]\n\t"
    "v_exp_f32 v143, v143\n\t"
    "v_mov_b32 v31, 0\n\t"
    "v_mfma_f32_16x16x32_bf16 v[136:139], a[96:99], a[48:51], v[172:175]\n\t"
    "ds_read_b128 a[88:91], v196 offset:20992\n\t"
    "v_cvt_pk_bf16_f32 v151, v142, v143\n\t"
    "v_mov_b32 v44, 0\n\t"
    "v_mfma_f32_16x16x32_bf16 v[136:139], a[100:103], a[52:55], v[136:139]\n\t"
    "v_exp_f32 v128, v128\n\t"
    "v_mov_b32 v45, 0\n\t"
    "v_mfma_f32_16x16x32_bf16 v[136:139], a[104:107], a[56:59], v[136:139]\n\t"
    "buffer_load_dwordx4 v200, s[40:43], s65 offen offset:1024 nt lds\n\t"
    "ds_read_b128 a[84:87], v196 offset:20736\n\t"
    "v_mov_b32 v46, 0\n\t"
    "v_mfma_f32_16x16x32_bf16 v[136:139], a[108:111], a[60:63], v[136:139]\n\t"
    "v_exp_f32 v129, v129\n\t"
    "v_mov_b32 v47, 0\n\t"
    "v_mfma_f32_16x16x32_bf16 v[140:143], a[124:127], a[60:63], v[172:175]\n\t"
    "ds_read_b128 a[80:83], v196 offset:20480\n\t"
    "v_cvt_pk_bf16_f32 v152, v128, v129\n\t"
    "v_mov_b32 v60, 0\n\t"
    "v_mfma_f32_16x16x32_bf16 v[140:143], a[120:123], a[56:59], v[140:143]\n\t"
    "v_exp_f32 v130, v130\n\t"
    "v_mov_b32 v61, 0\n\t"
    "v_mfma_f32_16x16x32_bf16 v[140:143], a[116:119], a[52:55], v[140:143]\n\t"
    "ds_read_b128 a[96:99], v196 offset:24576\n\t"
    "ds_read_b128 a[100:103], v196 offset:24832\n\t"
    "v_mov_b32 v62, 0\n\t"
    "v_mfma_f32_16x16x32_bf16 v[140:143], a[112:115], a[48:51], v[140:143]\n\t"
    "v_exp_f32 v131, v131\n\t"
    "v_mov_b32 v63, 0\n\t"
    "v_mfma_f32_16x16x32_bf16 v[8:11], a[128:131], v[144:147], 0\n\t"
    "ds_read_b128 a[104:107], v196 offset:25088\n\t"
    "v_cvt_pk_bf16_f32 v153, v130, v131\n\t"
    "v_mov_b32 v76, 0\n\t"
    "v_mfma_f32_16x16x32_bf16 v[8:11], a[132:135], v[148:151], v[8:11]\n\t"
    "v_exp_f32 v132, v132\n\t"
    "v_mov_b32 v77, 0\n\t"
    "v_mfma_f32_16x16x32_bf16 v[24:27], a[140:143], v[148:151], 0\n\t"
    "buffer_load_dwordx4 v201, s[40:43], s65 offen offset:2048 nt lds\n\t"
    "ds_read_b128 a[108:111], v196 offset:25344\n\t"
    "v_mov_b32 v78, 0\n\t"
    "v_mfma_f32_16x16x32_bf16 v[24:27], a[136:139], v[144:147], v[24:27]\n\t"
    "v_exp_f32 v133, v133\n\t"
    "v_mov_b32 v79, 0\n\t"
    "v_mfma_f32_16x16x32_bf16 v[40:43], a[144:147], v[144:147], 0\n\t"
    "ds_read_b128 a[124:127], v196 offset:29440\n\t"
    "v_cvt_pk_bf16_f32 v154, v132, v133\n\t"
    "v_mov_b32 v92, 0\n\t"
    "v_mfma_f32_16x16x32_bf16 v[40:43], a[148:151], v[148:151], v[40:43]\n\t"
    "v_exp_f32 v134, v134\n\t"
    "v_mov_b32 v93, 0\n\t"
    "v_mfma_f32_16x16x32_bf16 v[56:59], a[156:159], v[148:151], 0\n\t"
    "ds_read_b128 a[120:123], v196 offset:29184\n\t"
    "ds_read_b128 a[116:119], v196 offset:28928\n\t"
    "v_mov_b32 v94, 0\n\t"
    "v_mfma_f32_16x16x32_bf16 v[56:59], a[152:155], v[144:147], v[56:59]\n\t"
    "v_exp_f32 v135, v135\n\t"
    "v_mov_b32 v95, 0\n\t"
    "v_mfma_f32_16x16x32_bf16 v[72:75], a[160:163], v[144:147], 0\n\t"
    "ds_read_b128 a[112:115], v196 offset:28672\n\t"
    "v_cvt_pk_bf16_f32 v155, v134, v135\n\t"
    "v_mov_b32 v108, 0\n\t"
    "v_mfma_f32_16x16x32_bf16 v[72:75], a[164:167], v[148:151], v[72:75]\n\t"
    "v_exp_f32 v136, v136\n\t"
    "v_mov_b32 v109, 0\n\t"
    "v_mfma_f32_16x16x32_bf16 v[88:91], a[172:175], v[148:151], 0\n\t"
    "buffer_load_dwordx4 v202, s[40:43], s65 offen offset:3072 nt lds\n\t"
    "v_mov_b32 v110, 0\n\t"
    "v_mfma_f32_16x16x32_bf16 v[88:91], a[168:171], v[144:147], v[88:91]\n\t"
    "v_exp_f32 v137, v137\n\t"
    "v_mov_b32 v111, 0\n\t"
    "v_mfma_f32_16x16x32_bf16 v[104:107], a[176:179], v[144:147], 0\n\t"
    "v_cvt_pk_bf16_f32 v156, v136, v137\n\t"
    "v_mov_b32 v124, 0\n\t"
    "v_mfma_f32_16x16x32_bf16 v[104:107], a[180:183], v[148:151], v[104:107]\n\t"
    "v_exp_f32 v138, v138\n\t"
    "v_mov_b32 v125, 0\n\t"
    "v_mfma_f32_16x16x32_bf16 v[120:123], a[188:191], v[148:151], 0\n\t"
    "v_mov_b32 v126, 0\n\t"
    "v_mfma_f32_16x16x32_bf16 v[120:123], a[184:187], v[144:147], v[120:123]\n\t"
    "s_add_u32 s65, s65, 16384\n\t"
    "v_exp_f32 v139, v139\n\t"
    "v_mov_b32 v127, 0\n\t"
    "v_smfmac_f32_16x16x64_bf16 v[184:187], v[192:195], v[144:151], v245\n\t"
    "s_waitcnt vmcnt(8) lgkmcnt(0)\n\t"
    "s_barrier\n\t"
    "v_cvt_pk_bf16_f32 v157, v138, v139\n\t"
    "v_mfma_f32_16x16x32_bf16 v[128:131], a[64:67], a[0:3], v[160:163]\n\t"
    "buffer_load_dwordx2 v[220:221], v247, s[52:55], 0 offen offset:0\n\t"
    "buffer_load_dwordx2 v[222:223], v247, s[52:55], 0 offen offset:32\n\t"
    "buffer_load_dwordx2 v[224:225], v247, s[52:55], 0 offen offset:1024\n\t"
    "ds_read_b64_tr_b16 a[192:193], v197 offset:16384\n\t"
    "ds_read_b64_tr_b16 a[194:195], v197 offset:20480\n\t"
    "v_mfma_f32_16x16x32_bf16 v[128:131], a[68:71], a[4:7], v[128:131]\n\t"
    "buffer_load_dwordx2 v[226:227], v247, s[52:55], 0 offen offset:1056\n\t"
    "buffer_load_dword v252, off, s[48:51], s78 sc1\n\t"
    "buffer_load_dword v253, off, s[48:51], s77 sc1\n\t"
    "s_add_u32 m0, s64, 49152\n\t"
    "v_exp_f32 v140, v140\n\t"
    "v_mfma_f32_16x16x32_bf16 v[128:131], a[72:75], a[8:11], v[128:131]\n\t"
    "buffer_load_dwordx4 v199, s[36:39], s65 offen offset:0 nt lds\n\t"
    "ds_read_b64_tr_b16 a[196:197], v197 offset:24576\n\t"
    "v_mfma_f32_16x16x32_bf16 v[128:131], a[76:79], a[12:15], v[128:131]\n\t"
    "v_exp_f32 v141, v141\n\t"
    "v_mfma_f32_16x16x32_bf16 v[132:135], a[92:95], a[12:15], v[160:163]\n\t"
    "ds_read_b64_tr_b16 a[198:199], v197 offset:28672\n\t"
    "v_cvt_pk_bf16_f32 v158, v140, v141\n\t"
    "v_mfma_f32_16x16x32_bf16 v[132:135], a[88:91], a[8:11], v[132:135]\n\t"
    "v_exp_f32 v142, v142\n\t"
    "v_mfma_f32_16x16x32_bf16 v[132:135], a[84:87], a[4:7], v[132:135]\n\t"
    "ds_read_b64_tr_b16 a[200:201], v198 offset:16384\n\t"
    "ds_read_b64_tr_b16 a[202:203], v198 offset:20480\n\t"
    "v_mfma_f32_16x16x32_bf16 v[132:135], a[80:83], a[0:3], v[132:135]\n\t"
    "v_exp_f32 v143, v143\n\t"
    "v_mfma_f32_16x16x32_bf16 v[136:139], a[96:99], a[0:3], v[160:163]\n\t"
    "ds_read_b64_tr_b16 a[204:205], v198 offset:24576\n\t"
    "v_cvt_pk_bf16_f32 v159, v142, v143\n\t"
    "v_mfma_f32_16x16x32_bf16 v[136:139], a[100:103], a[4:7], v[136:139]\n\t"
    "v_exp_f32 v128, v128\n\t"
    "v_mfma_f32_16x16x32_bf16 v[136:139], a[104:107], a[8:11], v[136:139]\n\t"
    "buffer_load_dwordx4 v200, s[36:39], s65 offen offset:1024 nt lds\n\t"
    "ds_read_b64_tr_b16 a[206:207], v198 offset:28672\n\t"
    "v_mfma_f32_16x16x32_bf16 v[136:139], a[108:111], a[12:15], v[136:139]\n\t"
    "v_exp_f32 v129, v129\n\t"
    "v_mfma_f32_16x16x32_bf16 v[140:143], a[124:127], a[12:15], v[160:163]\n\t"
    "ds_read_b64_tr_b16 a[208:209], v197 offset:16640\n\t"
    "v_cvt_pk_bf16_f32 v144, v128, v129\n\t"
    "v_mfma_f32_16x16x32_bf16 v[140:143], a[120:123], a[8:11], v[140:143]\n\t"
    "v_exp_f32 v130, v130\n\t"
    "v_mfma_f32_16x16x32_bf16 v[140:143], a[116:119], a[4:7], v[140:143]\n\t"
    "ds_read_b64_tr_b16 a[210:211], v197 offset:20736\n\t"
    "ds_read_b64_tr_b16 a[212:213], v197 offset:24832\n\t"
    "v_mfma_f32_16x16x32_bf16 v[140:143], a[112:115], a[0:3], v[140:143]\n\t"
    "v_exp_f32 v131, v131\n\t"
    "v_mfma_f32_16x16x32_bf16 v[12:15], a[128:131], v[152:155], v[12:15]\n\t"
    "ds_read_b64_tr_b16 a[214:215], v197 offset:28928\n\t"
    "v_cvt_pk_bf16_f32 v145, v130, v131\n\t"
    "v_mfma_f32_16x16x32_bf16 v[12:15], a[132:135], v[156:159], v[12:15]\n\t"
    "v_exp_f32 v132, v132\n\t"
    "v_mfma_f32_16x16x32_bf16 v[28:31], a[140:143], v[156:159], v[28:31]\n\t"
    "buffer_load_dwordx4 v201, s[36:39], s65 offen offset:2048 nt lds\n\t"
    "ds_read_b64_tr_b16 a[216:217], v198 offset:16640\n\t"
    "v_mfma_f32_16x16x32_bf16 v[28:31], a[136:139], v[152:155], v[28:31]\n\t"
    "v_exp_f32 v133, v133\n\t"
    "v_mfma_f32_16x16x32_bf16 v[44:47], a[144:147], v[152:155], v[44:47]\n\t"
    "ds_read_b64_tr_b16 a[218:219], v198 offset:20736\n\t"
    "v_cvt_pk_bf16_f32 v146, v132, v133\n\t"
    "v_mfma_f32_16x16x32_bf16 v[44:47], a[148:151], v[156:159], v[44:47]\n\t"
    "v_exp_f32 v134, v134\n\t"
    "v_mfma_f32_16x16x32_bf16 v[60:63], a[156:159], v[156:159], v[60:63]\n\t"
    "ds_read_b64_tr_b16 a[220:221], v198 offset:24832\n\t"
    "ds_read_b64_tr_b16 a[222:223], v198 offset:28928\n\t"
    "v_mfma_f32_16x16x32_bf16 v[60:63], a[152:155], v[152:155], v[60:63]\n\t"
    "v_exp_f32 v135, v135\n\t"
    "v_mfma_f32_16x16x32_bf16 v[76:79], a[160:163], v[152:155], v[76:79]\n\t"
    "ds_read_b64_tr_b16 a[224:225], v197 offset:16896\n\t"
    "v_cvt_pk_bf16_f32 v147, v134, v135\n\t"
    "v_mfma_f32_16x16x32_bf16 v[76:79], a[164:167], v[156:159], v[76:79]\n\t"
    "v_exp_f32 v136, v136\n\t"
    "v_mfma_f32_16x16x32_bf16 v[92:95], a[172:175], v[156:159], v[92:95]\n\t"
    "buffer_load_dwordx4 v202, s[36:39], s65 offen offset:3072 nt lds\n\t"
    "ds_read_b64_tr_b16 a[226:227], v197 offset:20992\n\t"
    "v_mfma_f32_16x16x32_bf16 v[92:95], a[168:171], v[152:155], v[92:95]\n\t"
    "v_exp_f32 v137, v137\n\t"
    "v_mfma_f32_16x16x32_bf16 v[108:111], a[176:179], v[152:155], v[108:111]\n\t"
    "ds_read_b64_tr_b16 a[228:229], v197 offset:25088\n\t"
    "v_cvt_pk_bf16_f32 v148, v136, v137\n\t"
    "v_mfma_f32_16x16x32_bf16 v[108:111], a[180:183], v[156:159], v[108:111]\n\t"
    "v_exp_f32 v138, v138\n\t"
    "v_mfma_f32_16x16x32_bf16 v[124:127], a[188:191], v[156:159], v[124:127]\n\t"
    "ds_read_b64_tr_b16 a[230:231], v197 offset:29184\n\t"
    "ds_read_b64_tr_b16 a[232:233], v198 offset:16896\n\t"
    "ds_read_b64_tr_b16 a[236:237], v198 offset:25088\n\t"
    "ds_read_b64_tr_b16 a[238:239], v198 offset:29184\n\t"
    "v_mfma_f32_16x16x32_bf16 v[124:127], a[184:187], v[152:155], v[124:127]\n\t"
    "v_exp_f32 v139, v139\n\t"
    "v_smfmac_f32_16x16x64_bf16 v[188:191], v[192:195], v[152:159], v245\n\t"
    "ds_read_b64_tr_b16 a[234:235], v198 offset:20992\n\t"
    "v_cvt_pk_bf16_f32 v149, v138, v139\n\t"
    "v_mfma_f32_16x16x32_bf16 v[128:131], a[64:67], a[16:19], v[164:167]\n\t"
    "ds_read_b64_tr_b16 a[240:241], v197 offset:17152\n\t"
    "ds_read_b64_tr_b16 a[242:243], v197 offset:21248\n\t"
    "v_mfma_f32_16x16x32_bf16 v[128:131], a[68:71], a[20:23], v[128:131]\n\t"
    "s_add_u32 m0, s64, 114688\n\t"
    "v_exp_f32 v140, v140\n\t"
    "v_mfma_f32_16x16x32_bf16 v[128:131], a[72:75], a[24:27], v[128:131]\n\t"
    "buffer_load_dwordx4 v199, s[40:43], s65 offen offset:0 nt lds\n\t"
    "ds_read_b64_tr_b16 a[244:245], v197 offset:25344\n\t"
    "v_mfma_f32_16x16x32_bf16 v[128:131], a[76:79], a[28:31], v[128:131]\n\t"
    "v_exp_f32 v141, v141\n\t"
    "v_mfma_f32_16x16x32_bf16 v[132:135], a[92:95], a[28:31], v[164:167]\n\t"
    "ds_read_b64_tr_b16 a[246:247], v197 offset:29440\n\t"
    "v_cvt_pk_bf16_f32 v150, v140, v141\n\t"
    "v_mfma_f32_16x16x32_bf16 v[132:135], a[88:91], a[24:27], v[132:135]\n\t"
    "v_exp_f32 v142, v142\n\t"
    "v_mfma_f32_16x16x32_bf16 v[132:135], a[84:87], a[20:23], v[132:135]\n\t"
    "ds_read_b64_tr_b16 a[248:249], v198 offset:17152\n\t"
    "ds_read_b64_tr_b16 a[250:251], v198 offset:21248\n\t"
    "v_mfma_f32_16x16x32_bf16 v[132:135], a[80:83], a[16:19], v[132:135]\n\t"
    "v_exp_f32 v143, v143\n\t"
    "v_mfma_f32_16x16x32_bf16 v[136:139], a[96:99], a[16:19], v[164:167]\n\t"
    "ds_read_b64_tr_b16 a[252:253], v198 offset:25344\n\t"
    "v_cvt_pk_bf16_f32 v151, v142, v143\n\t"
    "v_mfma_f32_16x16x32_bf16 v[136:139], a[100:103], a[20:23], v[136:139]\n\t"
    "v_exp_f32 v128, v128\n\t"
    "v_mfma_f32_16x16x32_bf16 v[136:139], a[104:107], a[24:27], v[136:139]\n\t"
    "buffer_load_dwordx4 v200, s[40:43], s65 offen offset:1024 nt lds\n\t"
    "ds_read_b64_tr_b16 a[254:255], v198 offset:29440\n\t"
    "v_mfma_f32_16x16x32_bf16 v[136:139], a[108:111], a[28:31], v[136:139]\n\t"
    "v_exp_f32 v129, v129\n\t"
    "v_mfma_f32_16x16x32_bf16 v[140:143], a[124:127], a[28:31], v[164:167]\n\t"
    "v_cvt_pk_bf16_f32 v152, v128, v129\n\t"
    "v_mfma_f32_16x16x32_bf16 v[140:143], a[120:123], a[24:27], v[140:143]\n\t"
    "v_exp_f32 v130, v130\n\t"
    "v_mfma_f32_16x16x32_bf16 v[140:143], a[116:119], a[20:23], v[140:143]\n\t"
    "v_mfma_f32_16x16x32_bf16 v[140:143], a[112:115], a[16:19], v[140:143]\n\t"
    "s_waitcnt lgkmcnt(0)\n\t"
    "v_exp_f32 v131, v131\n\t"
    "v_mfma_f32_16x16x32_bf16 v[0:3], a[192:195], v[144:147], v[0:3]\n\t"
    "v_cvt_pk_bf16_f32 v153, v130, v131\n\t"
    "v_mfma_f32_16x16x32_bf16 v[0:3], a[196:199], v[148:151], v[0:3]\n\t"
    "v_exp_f32 v132, v132\n\t"
    "v_mfma_f32_16x16x32_bf16 v[16:19], a[204:207], v[148:151], v[16:19]\n\t"
    "buffer_load_dwordx4 v201, s[40:43], s65 offen offset:2048 nt lds\n\t"
    "v_mfma_f32_16x16x32_bf16 v[16:19], a[200:203], v[144:147], v[16:19]\n\t"
    "v_exp_f32 v133, v133\n\t"
    "v_mfma_f32_16x16x32_bf16 v[32:35], a[208:211], v[144:147], v[32:35]\n\t"
    "v_cvt_pk_bf16_f32 v154, v132, v133\n\t"
    "v_mfma_f32_16x16x32_bf16 v[32:35], a[212:215], v[148:151], v[32:35]\n\t"
    "v_exp_f32 v134, v134\n\t"
    "v_mfma_f32_16x16x32_bf16 v[48:51], a[220:223], v[148:151], v[48:51]\n\t"
    "v_mfma_f32_16x16x32_bf16 v[48:51], a[216:219], v[144:147], v[48:51]\n\t"
    "v_exp_f32 v135, v135\n\t"
    "v_mfma_f32_16x16x32_bf16 v[64:67], a[224:227], v[144:147], v[64:67]\n\t"
    "v_cvt_pk_bf16_f32 v155, v134, v135\n\t"
    "v_mfma_f32_16x16x32_bf16 v[64:67], a[228:231], v[148:151], v[64:67]\n\t"
    "v_exp_f32 v136, v136\n\t"
    "v_mfma_f32_16x16x32_bf16 v[80:83], a[236:239], v[148:151], v[80:83]\n\t"
    "buffer_load_dwordx4 v202, s[40:43], s65 offen offset:3072 nt lds\n\t"
    "v_mfma_f32_16x16x32_bf16 v[80:83], a[232:235], v[144:147], v[80:83]\n\t"
    "v_exp_f32 v137, v137\n\t"
    "v_mfma_f32_16x16x32_bf16 v[96:99], a[240:243], v[144:147], v[96:99]\n\t"
    "v_cvt_pk_bf16_f32 v156, v136, v137\n\t"
    "v_mfma_f32_16x16x32_bf16 v[96:99], a[244:247], v[148:151], v[96:99]\n\t"
    "v_exp_f32 v138, v138\n\t"
    "v_mfma_f32_16x16x32_bf16 v[112:115], a[252:255], v[148:151], v[112:115]\n\t"
    "s_add_u32 s65, s65, 16384\n\t"
    "v_mfma_f32_16x16x32_bf16 v[112:115], a[248:251], v[144:147], v[112:115]\n\t"
    "v_exp_f32 v139, v139\n\t"
    "v_smfmac_f32_16x16x64_bf16 v[176:179], v[192:195], v[144:151], v245\n\t"
    "v_cvt_pk_bf16_f32 v157, v138, v139\n\t"
    "v_mfma_f32_16x16x32_bf16 v[128:131], a[64:67], a[32:35], v[168:171]\n\t"
    "v_mfma_f32_16x16x32_bf16 v[128:131], a[68:71], a[36:39], v[128:131]\n\t"
    "s_add_u32 m0, s64, 0\n\t"
    "v_exp_f32 v140, v140\n\t"
    "v_mfma_f32_16x16x32_bf16 v[128:131], a[72:75], a[40:43], v[128:131]\n\t"
    "buffer_load_dwordx4 v199, s[36:39], s65 offen offset:0 nt lds\n\t"
    "v_mfma_f32_16x16x32_bf16 v[128:131], a[76:79], a[44:47], v[128:131]\n\t"
    "v_exp_f32 v141, v141\n\t"
    "v_mfma_f32_16x16x32_bf16 v[132:135], a[92:95], a[44:47], v[168:171]\n\t"
    "v_cvt_pk_bf16_f32 v158, v140, v141\n\t"
    "v_mfma_f32_16x16x32_bf16 v[132:135], a[88:91], a[40:43], v[132:135]\n\t"
    "v_exp_f32 v142, v142\n\t"
    "v_mfma_f32_16x16x32_bf16 v[132:135], a[84:87], a[36:39], v[132:135]\n\t"
    "v_mfma_f32_16x16x32_bf16 v[132:135], a[80:83], a[32:35], v[132:135]\n\t"
    "v_exp_f32 v143, v143\n\t"
    "v_mfma_f32_16x16x32_bf16 v[136:139], a[96:99], a[32:35], v[168:171]\n\t"
    "v_cvt_pk_bf16_f32 v159, v142, v143\n\t"
    "v_mfma_f32_16x16x32_bf16 v[136:139], a[100:103], a[36:39], v[136:139]\n\t"
    "v_exp_f32 v128, v128\n\t"
    "v_mfma_f32_16x16x32_bf16 v[136:139], a[104:107], a[40:43], v[136:139]\n\t"
    "buffer_load_dwordx4 v200, s[36:39], s65 offen offset:1024 nt lds\n\t"
    "v_mfma_f32_16x16x32_bf16 v[136:139], a[108:111], a[44:47], v[136:139]\n\t"
    "v_exp_f32 v129, v129\n\t"
    "v_mfma_f32_16x16x32_bf16 v[140:143], a[124:127], a[44:47], v[168:171]\n\t"
    "v_cvt_pk_bf16_f32 v144, v128, v129\n\t"
    "v_mfma_f32_16x16x32_bf16 v[140:143], a[120:123], a[40:43], v[140:143]\n\t"
    "v_exp_f32 v130, v130\n\t"
    "v_mfma_f32_16x16x32_bf16 v[140:143], a[116:119], a[36:39], v[140:143]\n\t"
    "v_mfma_f32_16x16x32_bf16 v[140:143], a[112:115], a[32:35], v[140:143]\n\t"
    "v_exp_f32 v131, v131\n\t"
    "v_mfma_f32_16x16x32_bf16 v[4:7], a[192:195], v[152:155], v[4:7]\n\t"
    "v_cvt_pk_bf16_f32 v145, v130, v131\n\t"
    "v_mfma_f32_16x16x32_bf16 v[4:7], a[196:199], v[156:159], v[4:7]\n\t"
    "v_exp_f32 v132, v132\n\t"
    "v_mfma_f32_16x16x32_bf16 v[20:23], a[204:207], v[156:159], v[20:23]\n\t"
    "buffer_load_dwordx4 v201, s[36:39], s65 offen offset:2048 nt lds\n\t"
    "v_mfma_f32_16x16x32_bf16 v[20:23], a[200:203], v[152:155], v[20:23]\n\t"
    "v_exp_f32 v133, v133\n\t"
    "v_mfma_f32_16x16x32_bf16 v[36:39], a[208:211], v[152:155], v[36:39]\n\t"
    "v_cvt_pk_bf16_f32 v146, v132, v133\n\t"
    "v_mfma_f32_16x16x32_bf16 v[36:39], a[212:215], v[156:159], v[36:39]\n\t"
    "v_exp_f32 v134, v134\n\t"
    "v_mfma_f32_16x16x32_bf16 v[52:55], a[220:223], v[156:159], v[52:55]\n\t"
    "v_mfma_f32_16x16x32_bf16 v[52:55], a[216:219], v[152:155], v[52:55]\n\t"
    "v_exp_f32 v135, v135\n\t"
    "v_mfma_f32_16x16x32_bf16 v[68:71], a[224:227], v[152:155], v[68:71]\n\t"
    "v_cvt_pk_bf16_f32 v147, v134, v135\n\t"
    "v_mfma_f32_16x16x32_bf16 v[68:71], a[228:231], v[156:159], v[68:71]\n\t"
    "v_exp_f32 v136, v136\n\t"
    "v_mfma_f32_16x16x32_bf16 v[84:87], a[236:239], v[156:159], v[84:87]\n\t"
    "buffer_load_dwordx4 v202, s[36:39], s65 offen offset:3072 nt lds\n\t"
    "v_mfma_f32_16x16x32_bf16 v[84:87], a[232:235], v[152:155], v[84:87]\n\t"
    "v_exp_f32 v137, v137\n\t"
    "v_mfma_f32_16x16x32_bf16 v[100:103], a[240:243], v[152:155], v[100:103]\n\t"
    "v_cvt_pk_bf16_f32 v148, v136, v137\n\t"
    "v_mfma_f32_16x16x32_bf16 v[100:103], a[244:247], v[156:159], v[100:103]\n\t"
    "v_exp_f32 v138, v138\n\t"
    "v_mfma_f32_16x16x32_bf16 v[116:119], a[252:255], v[156:159], v[116:119]\n\t"
    "v_mfma_f32_16x16x32_bf16 v[116:119], a[248:251], v[152:155], v[116:119]\n\t"
    "v_exp_f32 v139, v139\n\t"
    "v_smfmac_f32_16x16x64_bf16 v[180:183], v[192:195], v[152:159], v245\n\t"
    "s_waitcnt vmcnt(18)\n\t"
    "s_barrier\n\t"
    "v_cvt_pk_bf16_f32 v149, v138, v139\n\t"
    "v_mfma_f32_16x16x32_bf16 v[128:131], a[64:67], a[48:51], v[172:175]\n\t"
    "ds_read_b128 a[64:67], v196 offset:32768\n\t"
    "v_mfma_f32_16x16x32_bf16 v[128:131], a[68:71], a[52:55], v[128:131]\n\t"
    "s_add_u32 m0, s64, 65536\n\t"
    "v_exp_f32 v140, v140\n\t"
    "v_mfma_f32_16x16x32_bf16 v[128:131], a[72:75], a[56:59], v[128:131]\n\t"
    "buffer_load_dwordx4 v199, s[40:43], s65 offen offset:0 nt lds\n\t"
    "ds_read_b128 a[68:71], v196 offset:33024\n\t"
    "v_mfma_f32_16x16x32_bf16 v[128:131], a[76:79], a[60:63], v[128:131]\n\t"
    "v_exp_f32 v141, v141\n\t"
    "v_mfma_f32_16x16x32_bf16 v[132:135], a[92:95], a[60:63], v[172:175]\n\t"
    "ds_read_b128 a[72:75], v196 offset:33280\n\t"
    "v_cvt_pk_bf16_f32 v150, v140, v141\n\t"
    "v_mfma_f32_16x16x32_bf16 v[132:135], a[88:91], a[56:59], v[132:135]\n\t"
    "v_exp_f32 v142, v142\n\t"
    "v_mfma_f32_16x16x32_bf16 v[132:135], a[84:87], a[52:55], v[132:135]\n\t"
    "ds_read_b128 a[76:79], v196 offset:33536\n\t"
    "ds_read_b128 a[92:95], v196 offset:37632\n\t"
    "v_mfma_f32_16x16x32_bf16 v[132:135], a[80:83], a[48:51], v[132:135]\n\t"
    "v_exp_f32 v143, v143\n\t"
    "v_mfma_f32_16x16x32_bf16 v[136:139], a[96:99], a[48:51], v[172:175]\n\t"
    "ds_read_b128 a[88:91], v196 offset:37376\n\t"
    "v_cvt_pk_bf16_f32 v151, v142, v143\n\t"
    "v_mfma_f32_16x16x32_bf16 v[136:139], a[100:103], a[52:55], v[136:139]\n\t"
    "v_exp_f32 v128, v128\n\t"
    "v_mfma_f32_16x16x32_bf16 v[136:139], a[104:107], a[56:59], v[136:139]\n\t"
    "buffer_load_dwordx4 v200, s[40:43], s65 offen offset:1024 nt lds\n\t"
    "ds_read_b128 a[84:87], v196 offset:37120\n\t"
    "v_mfma_f32_16x16x32_bf16 v[136:139], a[108:111], a[60:63], v[136:139]\n\t"
    "v_exp_f32 v129, v129\n\t"
    "v_mfma_f32_16x16x32_bf16 v[140:143], a[124:127], a[60:63], v[172:175]\n\t"
    "ds_read_b128 a[80:83], v196 offset:36864\n\t"
    "v_cvt_pk_bf16_f32 v152, v128, v129\n\t"
    "v_mfma_f32_16x16x32_bf16 v[140:143], a[120:123], a[56:59], v[140:143]\n\t"
    "v_exp_f32 v130, v130\n\t"
    "v_mfma_f32_16x16x32_bf16 v[140:143], a[116:119], a[52:55], v[140:143]\n\t"
    "ds_read_b128 a[96:99], v196 offset:40960\n\t"
    "ds_read_b128 a[100:103], v196 offset:41216\n\t"
    "v_mfma_f32_16x16x32_bf16 v[140:143], a[112:115], a[48:51], v[140:143]\n\t"
    "v_exp_f32 v131, v131\n\t"
    "v_mfma_f32_16x16x32_bf16 v[8:11], a[192:195], v[144:147], v[8:11]\n\t"
    "ds_read_b128 a[104:107], v196 offset:41472\n\t"
    "v_cvt_pk_bf16_f32 v153, v130, v131\n\t"
    "v_mfma_f32_16x16x32_bf16 v[8:11], a[196:199], v[148:151], v[8:11]\n\t"
    "v_exp_f32 v132, v132\n\t"
    "v_mfma_f32_16x16x32_bf16 v[24:27], a[204:207], v[148:151], v[24:27]\n\t"
    "buffer_load_dwordx4 v201, s[40:43], s65 offen offset:2048 nt lds\n\t"
    "ds_read_b128 a[108:111], v196 offset:41728\n\t"
    "v_mfma_f32_16x16x32_bf16 v[24:27], a[200:203], v[144:147], v[24:27]\n\t"
    "v_exp_f32 v133, v133\n\t"
    "v_mfma_f32_16x16x32_bf16 v[40:43], a[208:211], v[144:147], v[40:43]\n\t"
    "ds_read_b128 a[124:127], v196 offset:45824\n\t"
    "v_cvt_pk_bf16_f32 v154, v132, v133\n\t"
    "v_mfma_f32_16x16x32_bf16 v[40:43], a[212:215], v[148:151], v[40:43]\n\t"
    "v_exp_f32 v134, v134\n\t"
    "v_mfma_f32_16x16x32_bf16 v[56:59], a[220:223], v[148:151], v[56:59]\n\t"
    "ds_read_b128 a[120:123], v196 offset:45568\n\t"
    "ds_read_b128 a[116:119], v196 offset:45312\n\t"
    "v_mfma_f32_16x16x32_bf16 v[56:59], a[216:219], v[144:147], v[56:59]\n\t"
    "v_exp_f32 v135, v135\n\t"
    "v_mfma_f32_16x16x32_bf16 v[72:75], a[224:227], v[144:147], v[72:75]\n\t"
    "ds_read_b128 a[112:115], v196 offset:45056\n\t"
    "v_cvt_pk_bf16_f32 v155, v134, v135\n\t"
    "v_mfma_f32_16x16x32_bf16 v[72:75], a[228:231], v[148:151], v[72:75]\n\t"
    "v_exp_f32 v136, v136\n\t"
    "v_mfma_f32_16x16x32_bf16 v[88:91], a[236:239], v[148:151], v[88:91]\n\t"
    "buffer_load_dwordx4 v202, s[40:43], s65 offen offset:3072 nt lds\n\t"
    "ds_read_b64_tr_b16 a[128:129], v197 offset:32768\n\t"
    "v_mfma_f32_16x16x32_bf16 v[88:91], a[232:235], v[144:147], v[88:91]\n\t"
    "v_exp_f32 v137, v137\n\t"
    "v_mfma_f32_16x16x32_bf16 v[104:107], a[240:243], v[144:147], v[104:107]\n\t"
    "ds_read_b64_tr_b16 a[130:131], v197 offset:36864\n\t"
    "v_cvt_pk_bf16_f32 v156, v136, v137\n\t"
    "v_mfma_f32_16x16x32_bf16 v[104:107], a[244:247], v[148:151], v[104:107]\n\t"
    "v_exp_f32 v138, v138\n\t"
    "v_mfma_f32_16x16x32_bf16 v[120:123], a[252:255], v[148:151], v[120:123]\n\t"
    "ds_read_b64_tr_b16 a[132:133], v197 offset:40960\n\t"
    "ds_read_b64_tr_b16 a[134:135], v197 offset:45056\n\t"
    "ds_read_b64_tr_b16 a[138:139], v198 offset:36864\n\t"
    "ds_read_b64_tr_b16 a[140:141], v198 offset:40960\n\t"
    "ds_read_b64_tr_b16 a[142:143], v198 offset:45056\n\t"
    "v_mfma_f32_16x16x32_bf16 v[120:123], a[248:251], v[144:147], v[120:123]\n\t"
    "s_add_u32 s65, s65, 16384\n\t"
    "v_exp_f32 v139, v139\n\t"
    "v_smfmac_f32_16x16x64_bf16 v[184:187], v[192:195], v[144:151], v245\n\t"
    "ds_read_b64_tr_b16 a[136:137], v198 offset:32768\n\t"
    "s_waitcnt lgkmcnt(8)\n\t"
    "v_cvt_pk_bf16_f32 v157, v138, v139\n\t"
    ".p2align 4\n\t"
    "4:\n\t"
    "v_mfma_f32_16x16x32_bf16 v[128:131], a[64:67], a[0:3], v[160:163]\n\t"
    "ds_read_b64_tr_b16 a[144:145], v197 offset:33024\n\t"
    "ds_read_b64_tr_b16 a[146:147], v197 offset:37120\n\t"
    "v_mfma_f32_16x16x32_bf16 v[128:131], a[68:71], a[4:7], v[128:131]\n\t"
    "v_exp_f32 v140, v140\n\t"
    "v_mfma_f32_16x16x32_bf16 v[128:131], a[72:75], a[8:11], v[128:131]\n\t"
    "ds_read_b64_tr_b16 a[148:149], v197 offset:41216\n\t"
    "ds_read_b64_tr_b16 a[150:151], v197 offset:45312\n\t"
    "v_mfma_f32_16x16x32_bf16 v[128:131], a[76:79], a[12:15], v[128:131]\n\t"
    "v_exp_f32 v141, v141\n\t"
    "v_mfma_f32_16x16x32_bf16 v[132:135], a[92:95], a[12:15], v[160:163]\n\t"
    "ds_read_b64_tr_b16 a[152:153], v198 offset:33024\n\t"
    "v_cvt_pk_bf16_f32 v158, v140, v141\n\t"
    "v_mfma_f32_16x16x32_bf16 v[132:135], a[88:91], a[8:11], v[132:135]\n\t"
    "v_exp_f32 v142, v142\n\t"
    "v_mfma_f32_16x16x32_bf16 v[132:135], a[84:87], a[4:7], v[132:135]\n\t"
    "ds_read_b64_tr_b16 a[154:155], v198 offset:37120\n\t"
    "ds_read_b64_tr_b16 a[156:157], v198 offset:41216\n\t"
    "v_mfma_f32_16x16x32_bf16 v[132:135], a[80:83], a[0:3], v[132:135]\n\t"
    "v_exp_f32 v143, v143\n\t"
    "v_mfma_f32_16x16x32_bf16 v[136:139], a[96:99], a[0:3], v[160:163]\n\t"
    "ds_read_b64_tr_b16 a[158:159], v198 offset:45312\n\t"
    "v_cvt_pk_bf16_f32 v159, v142, v143\n\t"
    "v_mfma_f32_16x16x32_bf16 v[136:139], a[100:103], a[4:7], v[136:139]\n\t"
    "v_exp_f32 v128, v128\n\t"
    "v_mfma_f32_16x16x32_bf16 v[136:139], a[104:107], a[8:11], v[136:139]\n\t"
    "ds_read_b64_tr_b16 a[160:161], v197 offset:33280\n\t"
    "ds_read_b64_tr_b16 a[162:163], v197 offset:37376\n\t"
    "v_mfma_f32_16x16x32_bf16 v[136:139], a[108:111], a[12:15], v[136:139]\n\t"
    "v_exp_f32 v129, v129\n\t"
    "v_mfma_f32_16x16x32_bf16 v[140:143], a[124:127], a[12:15], v[160:163]\n\t"
    "ds_read_b64_tr_b16 a[164:165], v197 offset:41472\n\t"
    "v_cvt_pk_bf16_f32 v144, v128, v129\n\t"
    "v_mfma_f32_16x16x32_bf16 v[140:143], a[120:123], a[8:11], v[140:143]\n\t"
    "v_exp_f32 v130, v130\n\t"
    "v_mfma_f32_16x16x32_bf16 v[140:143], a[116:119], a[4:7], v[140:143]\n\t"
    "ds_read_b64_tr_b16 a[166:167], v197 offset:45568\n\t"
    "ds_read_b64_tr_b16 a[168:169], v198 offset:33280\n\t"
    "v_mfma_f32_16x16x32_bf16 v[140:143], a[112:115], a[0:3], v[140:143]\n\t"
    "v_exp_f32 v131, v131\n\t"
    "v_mfma_f32_16x16x32_bf16 v[12:15], a[192:195], v[152:155], v[12:15]\n\t"
    "ds_read_b64_tr_b16 a[170:171], v198 offset:37376\n\t"
    "v_cvt_pk_bf16_f32 v145, v130, v131\n\t"
    "v_mfma_f32_16x16x32_bf16 v[12:15], a[196:199], v[156:159], v[12:15]\n\t"
    "v_exp_f32 v132, v132\n\t"
    "v_mfma_f32_16x16x32_bf16 v[28:31], a[204:207], v[156:159], v[28:31]\n\t"
    "ds_read_b64_tr_b16 a[172:173], v198 offset:41472\n\t"
    "ds_read_b64_tr_b16 a[174:175], v198 offset:45568\n\t"
    "v_mfma_f32_16x16x32_bf16 v[28:31], a[200:203], v[152:155], v[28:31]\n\t"
    "v_exp_f32 v133, v133\n\t"
    "v_mfma_f32_16x16x32_bf16 v[44:47], a[208:211], v[152:155], v[44:47]\n\t"
    "ds_read_b64_tr_b16 a[176:177], v197 offset:33536\n\t"
    "v_cvt_pk_bf16_f32 v146, v132, v133\n\t"
    "v_mfma_f32_16x16x32_bf16 v[44:47], a[212:215], v[156:159], v[44:47]\n\t"
    "v_exp_f32 v134, v134\n\t"
    "v_mfma_f32_16x16x32_bf16 v[60:63], a[220:223], v[156:159], v[60:63]\n\t"
    "ds_read_b64_tr_b16 a[178:179], v197 offset:37632\n\t"
    "ds_read_b64_tr_b16 a[180:181], v197 offset:41728\n\t"
    "v_mfma_f32_16x16x32_bf16 v[60:63], a[216:219], v[152:155], v[60:63]\n\t"
    "v_exp_f32 v135, v135\n\t"
    "v_mfma_f32_16x16x32_bf16 v[76:79], a[224:227], v[152:155], v[76:79]\n\t"
    "ds_read_b64_tr_b16 a[182:183], v197 offset:45824\n\t"
    "v_cvt_pk_bf16_f32 v147, v134, v135\n\t"
    "v_mfma_f32_16x16x32_bf16 v[76:79], a[228:231], v[156:159], v[76:79]\n\t"
    "v_exp_f32 v136, v136\n\t"
    "v_mfma_f32_16x16x32_bf16 v[92:95], a[236:239], v[156:159], v[92:95]\n\t"
    "ds_read_b64_tr_b16 a[184:185], v198 offset:33536\n\t"
    "ds_read_b64_tr_b16 a[186:187], v198 offset:37632\n\t"
    "v_mfma_f32_16x16x32_bf16 v[92:95], a[232:235], v[152:155], v[92:95]\n\t"
    "v_exp_f32 v137, v137\n\t"
    "v_mfma_f32_16x16x32_bf16 v[108:111], a[240:243], v[152:155], v[108:111]\n\t"
    "ds_read_b64_tr_b16 a[188:189], v198 offset:41728\n\t"
    "v_cvt_pk_bf16_f32 v148, v136, v137\n\t"
    "v_mfma_f32_16x16x32_bf16 v[108:111], a[244:247], v[156:159], v[108:111]\n\t"
    "v_exp_f32 v138, v138\n\t"
    "v_mfma_f32_16x16x32_bf16 v[124:127], a[252:255], v[156:159], v[124:127]\n\t"
    "ds_read_b64_tr_b16 a[190:191], v198 offset:45824\n\t"
    "v_mfma_f32_16x16x32_bf16 v[124:127], a[248:251], v[152:155], v[124:127]\n\t"
    "v_exp_f32 v139, v139\n\t"
    "v_smfmac_f32_16x16x64_bf16 v[188:191], v[192:195], v[152:159], v245\n\t"
    "v_cvt_pk_bf16_f32 v149, v138, v139\n\t"
    "v_mfma_f32_16x16x32_bf16 v[128:131], a[64:67], a[16:19], v[164:167]\n\t"
    "v_mfma_f32_16x16x32_bf16 v[128:131], a[68:71], a[20:23], v[128:131]\n\t"
    "s_add_u32 m0, s64, 16384\n\t"
    "v_exp_f32 v140, v140\n\t"
    "v_mfma_f32_16x16x32_bf16 v[128:131], a[72:75], a[24:27], v[128:131]\n\t"
    "buffer_load_dwordx4 v199, s[36:39], s65 offen offset:0 nt lds\n\t"
    "v_mfma_f32_16x16x32_bf16 v[128:131], a[76:79], a[28:31], v[128:131]\n\t"
    "v_exp_f32 v141, v141\n\t"
    "v_mfma_f32_16x16x32_bf16 v[132:135], a[92:95], a[28:31], v[164:167]\n\t"
    "v_cvt_pk_bf16_f32 v150, v140, v141\n\t"
    "v_mfma_f32_16x16x32_bf16 v[132:135], a[88:91], a[24:27], v[132:135]\n\t"
    "v_exp_f32 v142, v142\n\t"
    "v_mfma_f32_16x16x32_bf16 v[132:135], a[84:87], a[20:23], v[132:135]\n\t"
    "v_mfma_f32_16x16x32_bf16 v[132:135], a[80:83], a[16:19], v[132:135]\n\t"
    "v_exp_f32 v143, v143\n\t"
    "v_mfma_f32_16x16x32_bf16 v[136:139], a[96:99], a[16:19], v[164:167]\n\t"
    "v_cvt_pk_bf16_f32 v151, v142, v143\n\t"
    "v_mfma_f32_16x16x32_bf16 v[136:139], a[100:103], a[20:23], v[136:139]\n\t"
    "v_exp_f32 v128, v128\n\t"
    "v_mfma_f32_16x16x32_bf16 v[136:139], a[104:107], a[24:27], v[136:139]\n\t"
    "buffer_load_dwordx4 v200, s[36:39], s65 offen offset:1024 nt lds\n\t"
    "v_mfma_f32_16x16x32_bf16 v[136:139], a[108:111], a[28:31], v[136:139]\n\t"
    "v_exp_f32 v129, v129\n\t"
    "v_mfma_f32_16x16x32_bf16 v[140:143], a[124:127], a[28:31], v[164:167]\n\t"
    "v_cvt_pk_bf16_f32 v152, v128, v129\n\t"
    "v_mfma_f32_16x16x32_bf16 v[140:143], a[120:123], a[24:27], v[140:143]\n\t"
    "s_waitcnt lgkmcnt(0)\n\t"
    "v_exp_f32 v130, v130\n\t"
    "v_mfma_f32_16x16x32_bf16 v[140:143], a[116:119], a[20:23], v[140:143]\n\t"
    "v_mfma_f32_16x16x32_bf16 v[140:143], a[112:115], a[16:19], v[140:143]\n\t"
    "v_exp_f32 v131, v131\n\t"
    "v_mfma_f32_16x16x32_bf16 v[0:3], a[128:131], v[144:147], v[0:3]\n\t"
    "v_cvt_pk_bf16_f32 v153, v130, v131\n\t"
    "v_mfma_f32_16x16x32_bf16 v[0:3], a[132:135], v[148:151], v[0:3]\n\t"
    "v_exp_f32 v132, v132\n\t"
    "v_mfma_f32_16x16x32_bf16 v[16:19], a[140:143], v[148:151], v[16:19]\n\t"
    "buffer_load_dwordx4 v201, s[36:39], s65 offen offset:2048 nt lds\n\t"
    "v_mfma_f32_16x16x32_bf16 v[16:19], a[136:139], v[144:147], v[16:19]\n\t"
    "v_exp_f32 v133, v133\n\t"
    "v_mfma_f32_16x16x32_bf16 v[32:35], a[144:147], v[144:147], v[32:35]\n\t"
    "v_cvt_pk_bf16_f32 v154, v132, v133\n\t"
    "v_mfma_f32_16x16x32_bf16 v[32:35], a[148:151], v[148:151], v[32:35]\n\t"
    "v_exp_f32 v134, v134\n\t"
    "v_mfma_f32_16x16x32_bf16 v[48:51], a[156:159], v[148:151], v[48:51]\n\t"
    "v_mfma_f32_16x16x32_bf16 v[48:51], a[152:155], v[144:147], v[48:51]\n\t"
    "v_exp_f32 v135, v135\n\t"
    "v_mfma_f32_16x16x32_bf16 v[64:67], a[160:163], v[144:147], v[64:67]\n\t"
    "v_cvt_pk_bf16_f32 v155, v134, v135\n\t"
    "v_mfma_f32_16x16x32_bf16 v[64:67], a[164:167], v[148:151], v[64:67]\n\t"
    "v_exp_f32 v136, v136\n\t"
    "v_mfma_f32_16x16x32_bf16 v[80:83], a[172:175], v[148:151], v[80:83]\n\t"
    "buffer_load_dwordx4 v202, s[36:39], s65 offen offset:3072 nt lds\n\t"
    "v_mfma_f32_16x16x32_bf16 v[80:83], a[168:171], v[144:147], v[80:83]\n\t"
    "v_exp_f32 v137, v137\n\t"
    "v_mfma_f32_16x16x32_bf16 v[96:99], a[176:179], v[144:147], v[96:99]\n\t"
    "v_cvt_pk_bf16_f32 v156, v136, v137\n\t"
    "v_mfma_f32_16x16x32_bf16 v[96:99], a[180:183], v[148:151], v[96:99]\n\t"
    "v_exp_f32 v138, v138\n\t"
    "v_mfma_f32_16x16x32_bf16 v[112:115], a[188:191], v[148:151], v[112:115]\n\t"
    "v_mfma_f32_16x16x32_bf16 v[112:115], a[184:187], v[144:147], v[112:115]\n\t"
    "v_exp_f32 v139, v139\n\t"
    "v_smfmac_f32_16x16x64_bf16 v[176:179], v[192:195], v[144:151], v245\n\t"
    "v_cvt_pk_bf16_f32 v157, v138, v139\n\t"
    "v_mfma_f32_16x16x32_bf16 v[128:131], a[64:67], a[32:35], v[168:171]\n\t"
    "v_mfma_f32_16x16x32_bf16 v[128:131], a[68:71], a[36:39], v[128:131]\n\t"
    "s_add_u32 m0, s64, 81920\n\t"
    "v_exp_f32 v140, v140\n\t"
    "v_mfma_f32_16x16x32_bf16 v[128:131], a[72:75], a[40:43], v[128:131]\n\t"
    "buffer_load_dwordx4 v199, s[40:43], s65 offen offset:0 nt lds\n\t"
    "v_mfma_f32_16x16x32_bf16 v[128:131], a[76:79], a[44:47], v[128:131]\n\t"
    "v_exp_f32 v141, v141\n\t"
    "v_mfma_f32_16x16x32_bf16 v[132:135], a[92:95], a[44:47], v[168:171]\n\t"
    "v_cvt_pk_bf16_f32 v158, v140, v141\n\t"
    "v_mfma_f32_16x16x32_bf16 v[132:135], a[88:91], a[40:43], v[132:135]\n\t"
    "v_exp_f32 v142, v142\n\t"
    "v_mfma_f32_16x16x32_bf16 v[132:135], a[84:87], a[36:39], v[132:135]\n\t"
    "v_mfma_f32_16x16x32_bf16 v[132:135], a[80:83], a[32:35], v[132:135]\n\t"
    "v_exp_f32 v143, v143\n\t"
    "v_mfma_f32_16x16x32_bf16 v[136:139], a[96:99], a[32:35], v[168:171]\n\t"
    "v_cvt_pk_bf16_f32 v159, v142, v143\n\t"
    "v_mfma_f32_16x16x32_bf16 v[136:139], a[100:103], a[36:39], v[136:139]\n\t"
    "v_exp_f32 v128, v128\n\t"
    "v_mfma_f32_16x16x32_bf16 v[136:139], a[104:107], a[40:43], v[136:139]\n\t"
    "buffer_load_dwordx4 v200, s[40:43], s65 offen offset:1024 nt lds\n\t"
    "v_mfma_f32_16x16x32_bf16 v[136:139], a[108:111], a[44:47], v[136:139]\n\t"
    "v_exp_f32 v129, v129\n\t"
    "v_mfma_f32_16x16x32_bf16 v[140:143], a[124:127], a[44:47], v[168:171]\n\t"
    "v_cvt_pk_bf16_f32 v144, v128, v129\n\t"
    "v_mfma_f32_16x16x32_bf16 v[140:143], a[120:123], a[40:43], v[140:143]\n\t"
    "v_exp_f32 v130, v130\n\t"
    "v_mfma_f32_16x16x32_bf16 v[140:143], a[116:119], a[36:39], v[140:143]\n\t"
    "v_mfma_f32_16x16x32_bf16 v[140:143], a[112:115], a[32:35], v[140:143]\n\t"
    "v_exp_f32 v131, v131\n\t"
    "v_mfma_f32_16x16x32_bf16 v[4:7], a[128:131], v[152:155], v[4:7]\n\t"
    "v_cvt_pk_bf16_f32 v145, v130, v131\n\t"
    "v_mfma_f32_16x16x32_bf16 v[4:7], a[132:135], v[156:159], v[4:7]\n\t"
    "v_exp_f32 v132, v132\n\t"
    "v_mfma_f32_16x16x32_bf16 v[20:23], a[140:143], v[156:159], v[20:23]\n\t"
    "buffer_load_dwordx4 v201, s[40:43], s65 offen offset:2048 nt lds\n\t"
    "v_mfma_f32_16x16x32_bf16 v[20:23], a[136:139], v[152:155], v[20:23]\n\t"
    "v_exp_f32 v133, v133\n\t"
    "v_mfma_f32_16x16x32_bf16 v[36:39], a[144:147], v[152:155], v[36:39]\n\t"
    "v_cvt_pk_bf16_f32 v146, v132, v133\n\t"
    "v_mfma_f32_16x16x32_bf16 v[36:39], a[148:151], v[156:159], v[36:39]\n\t"
    "v_exp_f32 v134, v134\n\t"
    "v_mfma_f32_16x16x32_bf16 v[52:55], a[156:159], v[156:159], v[52:55]\n\t"
    "v_mfma_f32_16x16x32_bf16 v[52:55], a[152:155], v[152:155], v[52:55]\n\t"
    "v_exp_f32 v135, v135\n\t"
    "v_mfma_f32_16x16x32_bf16 v[68:71], a[160:163], v[152:155], v[68:71]\n\t"
    "v_cvt_pk_bf16_f32 v147, v134, v135\n\t"
    "v_mfma_f32_16x16x32_bf16 v[68:71], a[164:167], v[156:159], v[68:71]\n\t"
    "v_exp_f32 v136, v136\n\t"
    "v_mfma_f32_16x16x32_bf16 v[84:87], a[172:175], v[156:159], v[84:87]\n\t"
    "buffer_load_dwordx4 v202, s[40:43], s65 offen offset:3072 nt lds\n\t"
    "v_mfma_f32_16x16x32_bf16 v[84:87], a[168:171], v[152:155], v[84:87]\n\t"
    "v_exp_f32 v137, v137\n\t"
    "v_mfma_f32_16x16x32_bf16 v[100:103], a[176:179], v[152:155], v[100:103]\n\t"
    "v_cvt_pk_bf16_f32 v148, v136, v137\n\t"
    "v_mfma_f32_16x16x32_bf16 v[100:103], a[180:183], v[156:159], v[100:103]\n\t"
    "v_exp_f32 v138, v138\n\t"
    "v_mfma_f32_16x16x32_bf16 v[116:119], a[188:191], v[156:159], v[116:119]\n\t"
    "s_add_u32 s65, s65, 16384\n\t"
    "v_mfma_f32_16x16x32_bf16 v[116:119], a[184:187], v[152:155], v[116:119]\n\t"
    "v_exp_f32 v139, v139\n\t"
    "v_smfmac_f32_16x16x64_bf16 v[180:183], v[192:195], v[152:159], v245\n\t"
    "s_waitcnt vmcnt(16)\n\t"
    "s_barrier\n\t"
    "v_cvt_pk_bf16_f32 v149, v138, v139\n\t"
    "v_mfma_f32_16x16x32_bf16 v[128:131], a[64:67], a[48:51], v[172:175]\n\t"
    "ds_read_b128 a[64:67], v196 offset:49152\n\t"
    "v_mfma_f32_16x16x32_bf16 v[128:131], a[68:71], a[52:55], v[128:131]\n\t"
    "v_exp_f32 v140, v140\n\t"
    "v_mfma_f32_16x16x32_bf16 v[128:131], a[72:75], a[56:59], v[128:131]\n\t"
    "ds_read_b128 a[68:71], v196 offset:49408\n\t"
    "ds_read_b128 a[72:75], v196 offset:49664\n\t"
    "v_mfma_f32_16x16x32_bf16 v[128:131], a[76:79], a[60:63], v[128:131]\n\t"
    "v_exp_f32 v141, v141\n\t"
    "v_mfma_f32_16x16x32_bf16 v[132:135], a[92:95], a[60:63], v[172:175]\n\t"
    "ds_read_b128 a[76:79], v196 offset:49920\n\t"
    "v_cvt_pk_bf16_f32 v150, v140, v141\n\t"
    "v_mfma_f32_16x16x32_bf16 v[132:135], a[88:91], a[56:59], v[132:135]\n\t"
    "v_exp_f32 v142, v142\n\t"
    "v_mfma_f32_16x16x32_bf16 v[132:135], a[84:87], a[52:55], v[132:135]\n\t"
    "ds_read_b128 a[92:95], v196 offset:54016\n\t"
    "ds_read_b128 a[88:91], v196 offset:53760\n\t"
    "v_mfma_f32_16x16x32_bf16 v[132:135], a[80:83], a[48:51], v[132:135]\n\t"
    "v_exp_f32 v143, v143\n\t"
    "v_mfma_f32_16x16x32_bf16 v[136:139], a[96:99], a[48:51], v[172:175]\n\t"
    "ds_read_b128 a[84:87], v196 offset:53504\n\t"
    "v_cvt_pk_bf16_f32 v151, v142, v143\n\t"
    "v_mfma_f32_16x16x32_bf16 v[136:139], a[100:103], a[52:55], v[136:139]\n\t"
    "v_exp_f32 v128, v128\n\t"
    "v_mfma_f32_16x16x32_bf16 v[136:139], a[104:107], a[56:59], v[136:139]\n\t"
    "ds_read_b128 a[80:83], v196 offset:53248\n\t"
    "ds_read_b128 a[96:99], v196 offset:57344\n\t"
    "v_mfma_f32_16x16x32_bf16 v[136:139], a[108:111], a[60:63], v[136:139]\n\t"
    "v_exp_f32 v129, v129\n\t"
    "v_mfma_f32_16x16x32_bf16 v[140:143], a[124:127], a[60:63], v[172:175]\n\t"
    "ds_read_b128 a[100:103], v196 offset:57600\n\t"
    "v_cvt_pk_bf16_f32 v152, v128, v129\n\t"
    "v_mfma_f32_16x16x32_bf16 v[140:143], a[120:123], a[56:59], v[140:143]\n\t"
    "v_exp_f32 v130, v130\n\t"
    "v_mfma_f32_16x16x32_bf16 v[140:143], a[116:119], a[52:55], v[140:143]\n\t"
    "ds_read_b128 a[104:107], v196 offset:57856\n\t"
    "ds_read_b128 a[108:111], v196 offset:58112\n\t"
    "v_mfma_f32_16x16x32_bf16 v[140:143], a[112:115], a[48:51], v[140:143]\n\t"
    "v_exp_f32 v131, v131\n\t"
    "v_mfma_f32_16x16x32_bf16 v[8:11], a[128:131], v[144:147], v[8:11]\n\t"
    "ds_read_b128 a[124:127], v196 offset:62208\n\t"
    "v_cvt_pk_bf16_f32 v153, v130, v131\n\t"
    "v_mfma_f32_16x16x32_bf16 v[8:11], a[132:135], v[148:151], v[8:11]\n\t"
    "v_exp_f32 v132, v132\n\t"
    "v_mfma_f32_16x16x32_bf16 v[24:27], a[140:143], v[148:151], v[24:27]\n\t"
    "ds_read_b128 a[120:123], v196 offset:61952\n\t"
    "ds_read_b128 a[116:119], v196 offset:61696\n\t"
    "v_mfma_f32_16x16x32_bf16 v[24:27], a[136:139], v[144:147], v[24:27]\n\t"
    "v_exp_f32 v133, v133\n\t"
    "v_mfma_f32_16x16x32_bf16 v[40:43], a[144:147], v[144:147], v[40:43]\n\t"
    "ds_read_b128 a[112:115], v196 offset:61440\n\t"
    "v_cvt_pk_bf16_f32 v154, v132, v133\n\t"
    "v_mfma_f32_16x16x32_bf16 v[40:43], a[148:151], v[148:151], v[40:43]\n\t"
    "v_exp_f32 v134, v134\n\t"
    "v_mfma_f32_16x16x32_bf16 v[56:59], a[156:159], v[148:151], v[56:59]\n\t"
    "ds_read_b64_tr_b16 a[192:193], v197 offset:49152\n\t"
    "ds_read_b64_tr_b16 a[194:195], v197 offset:53248\n\t"
    "v_mfma_f32_16x16x32_bf16 v[56:59], a[152:155], v[144:147], v[56:59]\n\t"
    "v_exp_f32 v135, v135\n\t"
    "v_mfma_f32_16x16x32_bf16 v[72:75], a[160:163], v[144:147], v[72:75]\n\t"
    "ds_read_b64_tr_b16 a[196:197], v197 offset:57344\n\t"
    "v_cvt_pk_bf16_f32 v155, v134, v135\n\t"
    "v_mfma_f32_16x16x32_bf16 v[72:75], a[164:167], v[148:151], v[72:75]\n\t"
    "v_exp_f32 v136, v136\n\t"
    "v_mfma_f32_16x16x32_bf16 v[88:91], a[172:175], v[148:151], v[88:91]\n\t"
    "ds_read_b64_tr_b16 a[198:199], v197 offset:61440\n\t"
    "ds_read_b64_tr_b16 a[200:201], v198 offset:49152\n\t"
    "v_mfma_f32_16x16x32_bf16 v[88:91], a[168:171], v[144:147], v[88:91]\n\t"
    "v_exp_f32 v137, v137\n\t"
    "v_mfma_f32_16x16x32_bf16 v[104:107], a[176:179], v[144:147], v[104:107]\n\t"
    "ds_read_b64_tr_b16 a[202:203], v198 offset:53248\n\t"
    "v_cvt_pk_bf16_f32 v156, v136, v137\n\t"
    "v_mfma_f32_16x16x32_bf16 v[104:107], a[180:183], v[148:151], v[104:107]\n\t"
    "v_exp_f32 v138, v138\n\t"
    "v_mfma_f32_16x16x32_bf16 v[120:123], a[188:191], v[148:151], v[120:123]\n\t"
    "ds_read_b64_tr_b16 a[204:205], v198 offset:57344\n\t"
    "ds_read_b64_tr_b16 a[206:207], v198 offset:61440\n\t"
    "v_mfma_f32_16x16x32_bf16 v[120:123], a[184:187], v[144:147], v[120:123]\n\t"
    "v_exp_f32 v139, v139\n\t"
    "v_smfmac_f32_16x16x64_bf16 v[184:187], v[192:195], v[144:151], v245\n\t"
    "s_waitcnt lgkmcnt(8)\n\t"
    "v_cvt_pk_bf16_f32 v157, v138, v139\n\t"
    "v_mfma_f32_16x16x32_bf16 v[128:131], a[64:67], a[0:3], v[160:163]\n\t"
    "ds_read_b64_tr_b16 a[208:209], v197 offset:49408\n\t"
    "ds_read_b64_tr_b16 a[210:211], v197 offset:53504\n\t"
    "v_mfma_f32_16x16x32_bf16 v[128:131], a[68:71], a[4:7], v[128:131]\n\t"
    "v_exp_f32 v140, v140\n\t"
    "v_mfma_f32_16x16x32_bf16 v[128:131], a[72:75], a[8:11], v[128:131]\n\t"
    "ds_read_b64_tr_b16 a[212:213], v197 offset:57600\n\t"
    "ds_read_b64_tr_b16 a[214:215], v197 offset:61696\n\t"
    "v_mfma_f32_16x16x32_bf16 v[128:131], a[76:79], a[12:15], v[128:131]\n\t"
    "v_exp_f32 v141, v141\n\t"
    "v_mfma_f32_16x16x32_bf16 v[132:135], a[92:95], a[12:15], v[160:163]\n\t"
    "ds_read_b64_tr_b16 a[216:217], v198 offset:49408\n\t"
    "v_cvt_pk_bf16_f32 v158, v140, v141\n\t"
    "v_mfma_f32_16x16x32_bf16 v[132:135], a[88:91], a[8:11], v[132:135]\n\t"
    "v_exp_f32 v142, v142\n\t"
    "v_mfma_f32_16x16x32_bf16 v[132:135], a[84:87], a[4:7], v[132:135]\n\t"
    "ds_read_b64_tr_b16 a[218:219], v198 offset:53504\n\t"
    "ds_read_b64_tr_b16 a[220:221], v198 offset:57600\n\t"
    "v_mfma_f32_16x16x32_bf16 v[132:135], a[80:83], a[0:3], v[132:135]\n\t"
    "v_exp_f32 v143, v143\n\t"
    "v_mfma_f32_16x16x32_bf16 v[136:139], a[96:99], a[0:3], v[160:163]\n\t"
    "ds_read_b64_tr_b16 a[222:223], v198 offset:61696\n\t"
    "v_cvt_pk_bf16_f32 v159, v142, v143\n\t"
    "v_mfma_f32_16x16x32_bf16 v[136:139], a[100:103], a[4:7], v[136:139]\n\t"
    "v_exp_f32 v128, v128\n\t"
    "v_mfma_f32_16x16x32_bf16 v[136:139], a[104:107], a[8:11], v[136:139]\n\t"
    "ds_read_b64_tr_b16 a[224:225], v197 offset:49664\n\t"
    "ds_read_b64_tr_b16 a[226:227], v197 offset:53760\n\t"
    "v_mfma_f32_16x16x32_bf16 v[136:139], a[108:111], a[12:15], v[136:139]\n\t"
    "v_exp_f32 v129, v129\n\t"
    "v_mfma_f32_16x16x32_bf16 v[140:143], a[124:127], a[12:15], v[160:163]\n\t"
    "ds_read_b64_tr_b16 a[228:229], v197 offset:57856\n\t"
    "v_cvt_pk_bf16_f32 v144, v128, v129\n\t"
    "v_mfma_f32_16x16x32_bf16 v[140:143], a[120:123], a[8:11], v[140:143]\n\t"
    "v_exp_f32 v130, v130\n\t"
    "v_mfma_f32_16x16x32_bf16 v[140:143], a[116:119], a[4:7], v[140:143]\n\t"
    "ds_read_b64_tr_b16 a[230:231], v197 offset:61952\n\t"
    "ds_read_b64_tr_b16 a[232:233], v198 offset:49664\n\t"
    "v_mfma_f32_16x16x32_bf16 v[140:143], a[112:115], a[0:3], v[140:143]\n\t"
    "v_exp_f32 v131, v131\n\t"
    "v_mfma_f32_16x16x32_bf16 v[12:15], a[128:131], v[152:155], v[12:15]\n\t"
    "ds_read_b64_tr_b16 a[234:235], v198 offset:53760\n\t"
    "v_cvt_pk_bf16_f32 v145, v130, v131\n\t"
    "v_mfma_f32_16x16x32_bf16 v[12:15], a[132:135], v[156:159], v[12:15]\n\t"
    "v_exp_f32 v132, v132\n\t"
    "v_mfma_f32_16x16x32_bf16 v[28:31], a[140:143], v[156:159], v[28:31]\n\t"
    "ds_read_b64_tr_b16 a[236:237], v198 offset:57856\n\t"
    "ds_read_b64_tr_b16 a[238:239], v198 offset:61952\n\t"
    "v_mfma_f32_16x16x32_bf16 v[28:31], a[136:139], v[152:155], v[28:31]\n\t"
    "v_exp_f32 v133, v133\n\t"
    "v_mfma_f32_16x16x32_bf16 v[44:47], a[144:147], v[152:155], v[44:47]\n\t"
    "ds_read_b64_tr_b16 a[240:241], v197 offset:49920\n\t"
    "v_cvt_pk_bf16_f32 v146, v132, v133\n\t"
    "v_mfma_f32_16x16x32_bf16 v[44:47], a[148:151], v[156:159], v[44:47]\n\t"
    "v_exp_f32 v134, v134\n\t"
    "v_mfma_f32_16x16x32_bf16 v[60:63], a[156:159], v[156:159], v[60:63]\n\t"
    "ds_read_b64_tr_b16 a[242:243], v197 offset:54016\n\t"
    "ds_read_b64_tr_b16 a[244:245], v197 offset:58112\n\t"
    "v_mfma_f32_16x16x32_bf16 v[60:63], a[152:155], v[152:155], v[60:63]\n\t"
    "v_exp_f32 v135, v135\n\t"
    "v_mfma_f32_16x16x32_bf16 v[76:79], a[160:163], v[152:155], v[76:79]\n\t"
    "ds_read_b64_tr_b16 a[246:247], v197 offset:62208\n\t"
    "v_cvt_pk_bf16_f32 v147, v134, v135\n\t"
    "v_mfma_f32_16x16x32_bf16 v[76:79], a[164:167], v[156:159], v[76:79]\n\t"
    "v_exp_f32 v136, v136\n\t"
    "v_mfma_f32_16x16x32_bf16 v[92:95], a[172:175], v[156:159], v[92:95]\n\t"
    "ds_read_b64_tr_b16 a[248:249], v198 offset:49920\n\t"
    "ds_read_b64_tr_b16 a[250:251], v198 offset:54016\n\t"
    "v_mfma_f32_16x16x32_bf16 v[92:95], a[168:171], v[152:155], v[92:95]\n\t"
    "v_exp_f32 v137, v137\n\t"
    "v_mfma_f32_16x16x32_bf16 v[108:111], a[176:179], v[152:155], v[108:111]\n\t"
    "ds_read_b64_tr_b16 a[252:253], v198 offset:58112\n\t"
    "v_cvt_pk_bf16_f32 v148, v136, v137\n\t"
    "v_mfma_f32_16x16x32_bf16 v[108:111], a[180:183], v[156:159], v[108:111]\n\t"
    "v_exp_f32 v138, v138\n\t"
    "v_mfma_f32_16x16x32_bf16 v[124:127], a[188:191], v[156:159], v[124:127]\n\t"
    "ds_read_b64_tr_b16 a[254:255], v198 offset:62208\n\t"
    "v_mfma_f32_16x16x32_bf16 v[124:127], a[184:187], v[152:155], v[124:127]\n\t"
    "v_exp_f32 v139, v139\n\t"
    "v_smfmac_f32_16x16x64_bf16 v[188:191], v[192:195], v[152:159], v245\n\t"
    "v_cvt_pk_bf16_f32 v149, v138, v139\n\t"
    "v_mfma_f32_16x16x32_bf16 v[128:131], a[64:67], a[16:19], v[164:167]\n\t"
    "v_mfma_f32_16x16x32_bf16 v[128:131], a[68:71], a[20:23], v[128:131]\n\t"
    "s_add_u32 m0, s64, 32768\n\t"
    "v_exp_f32 v140, v140\n\t"
    "v_mfma_f32_16x16x32_bf16 v[128:131], a[72:75], a[24:27], v[128:131]\n\t"
    "buffer_load_dwordx4 v199, s[36:39], s65 offen offset:0 nt lds\n\t"
    "v_mfma_f32_16x16x32_bf16 v[128:131], a[76:79], a[28:31], v[128:131]\n\t"
    "v_exp_f32 v141, v141\n\t"
    "v_mfma_f32_16x16x32_bf16 v[132:135], a[92:95], a[28:31], v[164:167]\n\t"
    "v_cvt_pk_bf16_f32 v150, v140, v141\n\t"
    "v_mfma_f32_16x16x32_bf16 v[132:135], a[88:91], a[24:27], v[132:135]\n\t"
    "v_exp_f32 v142, v142\n\t"
    "v_mfma_f32_16x16x32_bf16 v[132:135], a[84:87], a[20:23], v[132:135]\n\t"
    "v_mfma_f32_16x16x32_bf16 v[132:135], a[80:83], a[16:19], v[132:135]\n\t"
    "v_exp_f32 v143, v143\n\t"
    "v_mfma_f32_16x16x32_bf16 v[136:139], a[96:99], a[16:19], v[164:167]\n\t"
    "v_cvt_pk_bf16_f32 v151, v142, v143\n\t"
    "v_mfma_f32_16x16x32_bf16 v[136:139], a[100:103], a[20:23], v[136:139]\n\t"
    "v_exp_f32 v128, v128\n\t"
    "v_mfma_f32_16x16x32_bf16 v[136:139], a[104:107], a[24:27], v[136:139]\n\t"
    "buffer_load_dwordx4 v200, s[36:39], s65 offen offset:1024 nt lds\n\t"
    "v_mfma_f32_16x16x32_bf16 v[136:139], a[108:111], a[28:31], v[136:139]\n\t"
    "v_exp_f32 v129, v129\n\t"
    "v_mfma_f32_16x16x32_bf16 v[140:143], a[124:127], a[28:31], v[164:167]\n\t"
    "v_cvt_pk_bf16_f32 v152, v128, v129\n\t"
    "v_mfma_f32_16x16x32_bf16 v[140:143], a[120:123], a[24:27], v[140:143]\n\t"
    "s_waitcnt lgkmcnt(0)\n\t"
    "v_exp_f32 v130, v130\n\t"
    "v_mfma_f32_16x16x32_bf16 v[140:143], a[116:119], a[20:23], v[140:143]\n\t"
    "v_mfma_f32_16x16x32_bf16 v[140:143], a[112:115], a[16:19], v[140:143]\n\t"
    "v_exp_f32 v131, v131\n\t"
    "v_mfma_f32_16x16x32_bf16 v[0:3], a[192:195], v[144:147], v[0:3]\n\t"
    "v_cvt_pk_bf16_f32 v153, v130, v131\n\t"
    "v_mfma_f32_16x16x32_bf16 v[0:3], a[196:199], v[148:151], v[0:3]\n\t"
    "v_exp_f32 v132, v132\n\t"
    "v_mfma_f32_16x16x32_bf16 v[16:19], a[204:207], v[148:151], v[16:19]\n\t"
    "buffer_load_dwordx4 v201, s[36:39], s65 offen offset:2048 nt lds\n\t"
    "v_mfma_f32_16x16x32_bf16 v[16:19], a[200:203], v[144:147], v[16:19]\n\t"
    "v_exp_f32 v133, v133\n\t"
    "v_mfma_f32_16x16x32_bf16 v[32:35], a[208:211], v[144:147], v[32:35]\n\t"
    "v_cvt_pk_bf16_f32 v154, v132, v133\n\t"
    "v_mfma_f32_16x16x32_bf16 v[32:35], a[212:215], v[148:151], v[32:35]\n\t"
    "v_exp_f32 v134, v134\n\t"
    "v_mfma_f32_16x16x32_bf16 v[48:51], a[220:223], v[148:151], v[48:51]\n\t"
    "v_mfma_f32_16x16x32_bf16 v[48:51], a[216:219], v[144:147], v[48:51]\n\t"
    "v_exp_f32 v135, v135\n\t"
    "v_mfma_f32_16x16x32_bf16 v[64:67], a[224:227], v[144:147], v[64:67]\n\t"
    "v_cvt_pk_bf16_f32 v155, v134, v135\n\t"
    "v_mfma_f32_16x16x32_bf16 v[64:67], a[228:231], v[148:151], v[64:67]\n\t"
    "v_exp_f32 v136, v136\n\t"
    "v_mfma_f32_16x16x32_bf16 v[80:83], a[236:239], v[148:151], v[80:83]\n\t"
    "buffer_load_dwordx4 v202, s[36:39], s65 offen offset:3072 nt lds\n\t"
    "v_mfma_f32_16x16x32_bf16 v[80:83], a[232:235], v[144:147], v[80:83]\n\t"
    "v_exp_f32 v137, v137\n\t"
    "v_mfma_f32_16x16x32_bf16 v[96:99], a[240:243], v[144:147], v[96:99]\n\t"
    "v_cvt_pk_bf16_f32 v156, v136, v137\n\t"
    "v_mfma_f32_16x16x32_bf16 v[96:99], a[244:247], v[148:151], v[96:99]\n\t"
    "v_exp_f32 v138, v138\n\t"
    "v_mfma_f32_16x16x32_bf16 v[112:115], a[252:255], v[148:151], v[112:115]\n\t"
    "v_mfma_f32_16x16x32_bf16 v[112:115], a[248:251], v[144:147], v[112:115]\n\t"
    "v_exp_f32 v139, v139\n\t"
    "v_smfmac_f32_16x16x64_bf16 v[176:179], v[192:195], v[144:151], v245\n\t"
    "v_cvt_pk_bf16_f32 v157, v138, v139\n\t"
    "v_mfma_f32_16x16x32_bf16 v[128:131], a[64:67], a[32:35], v[168:171]\n\t"
    "v_mfma_f32_16x16x32_bf16 v[128:131], a[68:71], a[36:39], v[128:131]\n\t"
    "s_add_u32 m0, s64, 98304\n\t"
    "v_exp_f32 v140, v140\n\t"
    "v_mfma_f32_16x16x32_bf16 v[128:131], a[72:75], a[40:43], v[128:131]\n\t"
    "buffer_load_dwordx4 v199, s[40:43], s65 offen offset:0 nt lds\n\t"
    "v_mfma_f32_16x16x32_bf16 v[128:131], a[76:79], a[44:47], v[128:131]\n\t"
    "v_exp_f32 v141, v141\n\t"
    "v_mfma_f32_16x16x32_bf16 v[132:135], a[92:95], a[44:47], v[168:171]\n\t"
    "v_cvt_pk_bf16_f32 v158, v140, v141\n\t"
    "v_mfma_f32_16x16x32_bf16 v[132:135], a[88:91], a[40:43], v[132:135]\n\t"
    "v_exp_f32 v142, v142\n\t"
    "v_mfma_f32_16x16x32_bf16 v[132:135], a[84:87], a[36:39], v[132:135]\n\t"
    "v_mfma_f32_16x16x32_bf16 v[132:135], a[80:83], a[32:35], v[132:135]\n\t"
    "v_exp_f32 v143, v143\n\t"
    "v_mfma_f32_16x16x32_bf16 v[136:139], a[96:99], a[32:35], v[168:171]\n\t"
    "v_cvt_pk_bf16_f32 v159, v142, v143\n\t"
    "v_mfma_f32_16x16x32_bf16 v[136:139], a[100:103], a[36:39], v[136:139]\n\t"
    "v_exp_f32 v128, v128\n\t"
    "v_mfma_f32_16x16x32_bf16 v[136:139], a[104:107], a[40:43], v[136:139]\n\t"
    "buffer_load_dwordx4 v200, s[40:43], s65 offen offset:1024 nt lds\n\t"
    "v_mfma_f32_16x16x32_bf16 v[136:139], a[108:111], a[44:47], v[136:139]\n\t"
    "v_exp_f32 v129, v129\n\t"
    "v_mfma_f32_16x16x32_bf16 v[140:143], a[124:127], a[44:47], v[168:171]\n\t"
    "v_cvt_pk_bf16_f32 v144, v128, v129\n\t"
    "v_mfma_f32_16x16x32_bf16 v[140:143], a[120:123], a[40:43], v[140:143]\n\t"
    "v_exp_f32 v130, v130\n\t"
    "v_mfma_f32_16x16x32_bf16 v[140:143], a[116:119], a[36:39], v[140:143]\n\t"
    "v_mfma_f32_16x16x32_bf16 v[140:143], a[112:115], a[32:35], v[140:143]\n\t"
    "v_exp_f32 v131, v131\n\t"
    "v_mfma_f32_16x16x32_bf16 v[4:7], a[192:195], v[152:155], v[4:7]\n\t"
    "v_cvt_pk_bf16_f32 v145, v130, v131\n\t"
    "v_mfma_f32_16x16x32_bf16 v[4:7], a[196:199], v[156:159], v[4:7]\n\t"
    "v_exp_f32 v132, v132\n\t"
    "v_mfma_f32_16x16x32_bf16 v[20:23], a[204:207], v[156:159], v[20:23]\n\t"
    "buffer_load_dwordx4 v201, s[40:43], s65 offen offset:2048 nt lds\n\t"
    "v_mfma_f32_16x16x32_bf16 v[20:23], a[200:203], v[152:155], v[20:23]\n\t"
    "v_exp_f32 v133, v133\n\t"
    "v_mfma_f32_16x16x32_bf16 v[36:39], a[208:211], v[152:155], v[36:39]\n\t"
    "v_cvt_pk_bf16_f32 v146, v132, v133\n\t"
    "v_mfma_f32_16x16x32_bf16 v[36:39], a[212:215], v[156:159], v[36:39]\n\t"
    "v_exp_f32 v134, v134\n\t"
    "v_mfma_f32_16x16x32_bf16 v[52:55], a[220:223], v[156:159], v[52:55]\n\t"
    "v_mfma_f32_16x16x32_bf16 v[52:55], a[216:219], v[152:155], v[52:55]\n\t"
    "v_exp_f32 v135, v135\n\t"
    "v_mfma_f32_16x16x32_bf16 v[68:71], a[224:227], v[152:155], v[68:71]\n\t"
    "v_cvt_pk_bf16_f32 v147, v134, v135\n\t"
    "v_mfma_f32_16x16x32_bf16 v[68:71], a[228:231], v[156:159], v[68:71]\n\t"
    "v_exp_f32 v136, v136\n\t"
    "v_mfma_f32_16x16x32_bf16 v[84:87], a[236:239], v[156:159], v[84:87]\n\t"
    "buffer_load_dwordx4 v202, s[40:43], s65 offen offset:3072 nt lds\n\t"
    "v_mfma_f32_16x16x32_bf16 v[84:87], a[232:235], v[152:155], v[84:87]\n\t"
    "v_exp_f32 v137, v137\n\t"
    "v_mfma_f32_16x16x32_bf16 v[100:103], a[240:243], v[152:155], v[100:103]\n\t"
    "v_cvt_pk_bf16_f32 v148, v136, v137\n\t"
    "v_mfma_f32_16x16x32_bf16 v[100:103], a[244:247], v[156:159], v[100:103]\n\t"
    "v_exp_f32 v138, v138\n\t"
    "v_mfma_f32_16x16x32_bf16 v[116:119], a[252:255], v[156:159], v[116:119]\n\t"
    "s_add_u32 s65, s65, 16384\n\t"
    "v_mfma_f32_16x16x32_bf16 v[116:119], a[248:251], v[152:155], v[116:119]\n\t"
    "v_exp_f32 v139, v139\n\t"
    "v_smfmac_f32_16x16x64_bf16 v[180:183], v[192:195], v[152:159], v245\n\t"
    "s_waitcnt vmcnt(16)\n\t"
    "s_barrier\n\t"
    "v_cvt_pk_bf16_f32 v149, v138, v139\n\t"
    "v_mfma_f32_16x16x32_bf16 v[128:131], a[64:67], a[48:51], v[172:175]\n\t"
    "ds_read_b128 a[64:67], v196 offset:0\n\t"
    "v_mfma_f32_16x16x32_bf16 v[128:131], a[68:71], a[52:55], v[128:131]\n\t"
    "v_exp_f32 v140, v140\n\t"
    "v_mfma_f32_16x16x32_bf16 v[128:131], a[72:75], a[56:59], v[128:131]\n\t"
    "ds_read_b128 a[68:71], v196 offset:256\n\t"
    "ds_read_b128 a[72:75], v196 offset:512\n\t"
    "v_mfma_f32_16x16x32_bf16 v[128:131], a[76:79], a[60:63], v[128:131]\n\t"
    "v_exp_f32 v141, v141\n\t"
    "v_mfma_f32_16x16x32_bf16 v[132:135], a[92:95], a[60:63], v[172:175]\n\t"
    "ds_read_b128 a[76:79], v196 offset:768\n\t"
    "v_cvt_pk_bf16_f32 v150, v140, v141\n\t"
    "v_mfma_f32_16x16x32_bf16 v[132:135], a[88:91], a[56:59], v[132:135]\n\t"
    "v_exp_f32 v142, v142\n\t"
    "v_mfma_f32_16x16x32_bf16 v[132:135], a[84:87], a[52:55], v[132:135]\n\t"
    "ds_read_b128 a[92:95], v196 offset:4864\n\t"
    "ds_read_b128 a[88:91], v196 offset:4608\n\t"
    "v_mfma_f32_16x16x32_bf16 v[132:135], a[80:83], a[48:51], v[132:135]\n\t"
    "v_exp_f32 v143, v143\n\t"
    "v_mfma_f32_16x16x32_bf16 v[136:139], a[96:99], a[48:51], v[172:175]\n\t"
    "ds_read_b128 a[84:87], v196 offset:4352\n\t"
    "v_cvt_pk_bf16_f32 v151, v142, v143\n\t"
    "v_mfma_f32_16x16x32_bf16 v[136:139], a[100:103], a[52:55], v[136:139]\n\t"
    "v_exp_f32 v128, v128\n\t"
    "v_mfma_f32_16x16x32_bf16 v[136:139], a[104:107], a[56:59], v[136:139]\n\t"
    "ds_read_b128 a[80:83], v196 offset:4096\n\t"
    "ds_read_b128 a[96:99], v196 offset:8192\n\t"
    "v_mfma_f32_16x16x32_bf16 v[136:139], a[108:111], a[60:63], v[136:139]\n\t"
    "v_exp_f32 v129, v129\n\t"
    "v_mfma_f32_16x16x32_bf16 v[140:143], a[124:127], a[60:63], v[172:175]\n\t"
    "ds_read_b128 a[100:103], v196 offset:8448\n\t"
    "v_cvt_pk_bf16_f32 v152, v128, v129\n\t"
    "v_mfma_f32_16x16x32_bf16 v[140:143], a[120:123], a[56:59], v[140:143]\n\t"
    "v_exp_f32 v130, v130\n\t"
    "v_mfma_f32_16x16x32_bf16 v[140:143], a[116:119], a[52:55], v[140:143]\n\t"
    "ds_read_b128 a[104:107], v196 offset:8704\n\t"
    "ds_read_b128 a[108:111], v196 offset:8960\n\t"
    "v_mfma_f32_16x16x32_bf16 v[140:143], a[112:115], a[48:51], v[140:143]\n\t"
    "v_exp_f32 v131, v131\n\t"
    "v_mfma_f32_16x16x32_bf16 v[8:11], a[192:195], v[144:147], v[8:11]\n\t"
    "ds_read_b128 a[124:127], v196 offset:13056\n\t"
    "v_cvt_pk_bf16_f32 v153, v130, v131\n\t"
    "v_mfma_f32_16x16x32_bf16 v[8:11], a[196:199], v[148:151], v[8:11]\n\t"
    "v_exp_f32 v132, v132\n\t"
    "v_mfma_f32_16x16x32_bf16 v[24:27], a[204:207], v[148:151], v[24:27]\n\t"
    "ds_read_b128 a[120:123], v196 offset:12800\n\t"
    "ds_read_b128 a[116:119], v196 offset:12544\n\t"
    "v_mfma_f32_16x16x32_bf16 v[24:27], a[200:203], v[144:147], v[24:27]\n\t"
    "v_exp_f32 v133, v133\n\t"
    "v_mfma_f32_16x16x32_bf16 v[40:43], a[208:211], v[144:147], v[40:43]\n\t"
    "ds_read_b128 a[112:115], v196 offset:12288\n\t"
    "v_cvt_pk_bf16_f32 v154, v132, v133\n\t"
    "v_mfma_f32_16x16x32_bf16 v[40:43], a[212:215], v[148:151], v[40:43]\n\t"
    "v_exp_f32 v134, v134\n\t"
    "v_mfma_f32_16x16x32_bf16 v[56:59], a[220:223], v[148:151], v[56:59]\n\t"
    "ds_read_b64_tr_b16 a[128:129], v197 offset:0\n\t"
    "ds_read_b64_tr_b16 a[130:131], v197 offset:4096\n\t"
    "v_mfma_f32_16x16x32_bf16 v[56:59], a[216:219], v[144:147], v[56:59]\n\t"
    "v_exp_f32 v135, v135\n\t"
    "v_mfma_f32_16x16x32_bf16 v[72:75], a[224:227], v[144:147], v[72:75]\n\t"
    "ds_read_b64_tr_b16 a[132:133], v197 offset:8192\n\t"
    "v_cvt_pk_bf16_f32 v155, v134, v135\n\t"
    "v_mfma_f32_16x16x32_bf16 v[72:75], a[228:231], v[148:151], v[72:75]\n\t"
    "v_exp_f32 v136, v136\n\t"
    "v_mfma_f32_16x16x32_bf16 v[88:91], a[236:239], v[148:151], v[88:91]\n\t"
    "ds_read_b64_tr_b16 a[134:135], v197 offset:12288\n\t"
    "ds_read_b64_tr_b16 a[136:137], v198 offset:0\n\t"
    "v_mfma_f32_16x16x32_bf16 v[88:91], a[232:235], v[144:147], v[88:91]\n\t"
    "v_exp_f32 v137, v137\n\t"
    "v_mfma_f32_16x16x32_bf16 v[104:107], a[240:243], v[144:147], v[104:107]\n\t"
    "ds_read_b64_tr_b16 a[138:139], v198 offset:4096\n\t"
    "v_cvt_pk_bf16_f32 v156, v136, v137\n\t"
    "v_mfma_f32_16x16x32_bf16 v[104:107], a[244:247], v[148:151], v[104:107]\n\t"
    "v_exp_f32 v138, v138\n\t"
    "v_mfma_f32_16x16x32_bf16 v[120:123], a[252:255], v[148:151], v[120:123]\n\t"
    "ds_read_b64_tr_b16 a[140:141], v198 offset:8192\n\t"
    "ds_read_b64_tr_b16 a[142:143], v198 offset:12288\n\t"
    "v_mfma_f32_16x16x32_bf16 v[120:123], a[248:251], v[144:147], v[120:123]\n\t"
    "v_exp_f32 v139, v139\n\t"
    "v_smfmac_f32_16x16x64_bf16 v[184:187], v[192:195], v[144:151], v245\n\t"
    "s_waitcnt lgkmcnt(8)\n\t"
    "v_cvt_pk_bf16_f32 v157, v138, v139\n\t"
    "v_mfma_f32_16x16x32_bf16 v[128:131], a[64:67], a[0:3], v[160:163]\n\t"
    "ds_read_b64_tr_b16 a[144:145], v197 offset:256\n\t"
    "ds_read_b64_tr_b16 a[146:147], v197 offset:4352\n\t"
    "v_mfma_f32_16x16x32_bf16 v[128:131], a[68:71], a[4:7], v[128:131]\n\t"
    "v_exp_f32 v140, v140\n\t"
    "v_mfma_f32_16x16x32_bf16 v[128:131], a[72:75], a[8:11], v[128:131]\n\t"
    "ds_read_b64_tr_b16 a[148:149], v197 offset:8448\n\t"
    "ds_read_b64_tr_b16 a[150:151], v197 offset:12544\n\t"
    "v_mfma_f32_16x16x32_bf16 v[128:131], a[76:79], a[12:15], v[128:131]\n\t"
    "v_exp_f32 v141, v141\n\t"
    "v_mfma_f32_16x16x32_bf16 v[132:135], a[92:95], a[12:15], v[160:163]\n\t"
    "ds_read_b64_tr_b16 a[152:153], v198 offset:256\n\t"
    "v_cvt_pk_bf16_f32 v158, v140, v141\n\t"
    "v_mfma_f32_16x16x32_bf16 v[132:135], a[88:91], a[8:11], v[132:135]\n\t"
    "v_exp_f32 v142, v142\n\t"
    "v_mfma_f32_16x16x32_bf16 v[132:135], a[84:87], a[4:7], v[132:135]\n\t"
    "ds_read_b64_tr_b16 a[154:155], v198 offset:4352\n\t"
    "ds_read_b64_tr_b16 a[156:157], v198 offset:8448\n\t"
    "v_mfma_f32_16x16x32_bf16 v[132:135], a[80:83], a[0:3], v[132:135]\n\t"
    "v_exp_f32 v143, v143\n\t"
    "v_mfma_f32_16x16x32_bf16 v[136:139], a[96:99], a[0:3], v[160:163]\n\t"
    "ds_read_b64_tr_b16 a[158:159], v198 offset:12544\n\t"
    "v_cvt_pk_bf16_f32 v159, v142, v143\n\t"
    "v_mfma_f32_16x16x32_bf16 v[136:139], a[100:103], a[4:7], v[136:139]\n\t"
    "v_exp_f32 v128, v128\n\t"
    "v_mfma_f32_16x16x32_bf16 v[136:139], a[104:107], a[8:11], v[136:139]\n\t"
    "ds_read_b64_tr_b16 a[160:161], v197 offset:512\n\t"
    "ds_read_b64_tr_b16 a[162:163], v197 offset:4608\n\t"
    "v_mfma_f32_16x16x32_bf16 v[136:139], a[108:111], a[12:15], v[136:139]\n\t"
    "v_exp_f32 v129, v129\n\t"
    "v_mfma_f32_16x16x32_bf16 v[140:143], a[124:127], a[12:15], v[160:163]\n\t"
    "ds_read_b64_tr_b16 a[164:165], v197 offset:8704\n\t"
    "v_cvt_pk_bf16_f32 v144, v128, v129\n\t"
    "v_mfma_f32_16x16x32_bf16 v[140:143], a[120:123], a[8:11], v[140:143]\n\t"
    "v_exp_f32 v130, v130\n\t"
    "v_mfma_f32_16x16x32_bf16 v[140:143], a[116:119], a[4:7], v[140:143]\n\t"
    "ds_read_b64_tr_b16 a[166:167], v197 offset:12800\n\t"
    "ds_read_b64_tr_b16 a[168:169], v198 offset:512\n\t"
    "v_mfma_f32_16x16x32_bf16 v[140:143], a[112:115], a[0:3], v[140:143]\n\t"
    "v_exp_f32 v131, v131\n\t"
    "v_mfma_f32_16x16x32_bf16 v[12:15], a[192:195], v[152:155], v[12:15]\n\t"
    "ds_read_b64_tr_b16 a[170:171], v198 offset:4608\n\t"
    "v_cvt_pk_bf16_f32 v145, v130, v131\n\t"
    "v_mfma_f32_16x16x32_bf16 v[12:15], a[196:199], v[156:159], v[12:15]\n\t"
    "v_exp_f32 v132, v132\n\t"
    "v_mfma_f32_16x16x32_bf16 v[28:31], a[204:207], v[156:159], v[28:31]\n\t"
    "ds_read_b64_tr_b16 a[172:173], v198 offset:8704\n\t"
    "ds_read_b64_tr_b16 a[174:175], v198 offset:12800\n\t"
    "v_mfma_f32_16x16x32_bf16 v[28:31], a[200:203], v[152:155], v[28:31]\n\t"
    "v_exp_f32 v133, v133\n\t"
    "v_mfma_f32_16x16x32_bf16 v[44:47], a[208:211], v[152:155], v[44:47]\n\t"
    "ds_read_b64_tr_b16 a[176:177], v197 offset:768\n\t"
    "v_cvt_pk_bf16_f32 v146, v132, v133\n\t"
    "v_mfma_f32_16x16x32_bf16 v[44:47], a[212:215], v[156:159], v[44:47]\n\t"
    "v_exp_f32 v134, v134\n\t"
    "v_mfma_f32_16x16x32_bf16 v[60:63], a[220:223], v[156:159], v[60:63]\n\t"
    "ds_read_b64_tr_b16 a[178:179], v197 offset:4864\n\t"
    "ds_read_b64_tr_b16 a[180:181], v197 offset:8960\n\t"
    "v_mfma_f32_16x16x32_bf16 v[60:63], a[216:219], v[152:155], v[60:63]\n\t"
    "v_exp_f32 v135, v135\n\t"
    "v_mfma_f32_16x16x32_bf16 v[76:79], a[224:227], v[152:155], v[76:79]\n\t"
    "ds_read_b64_tr_b16 a[182:183], v197 offset:13056\n\t"
    "v_cvt_pk_bf16_f32 v147, v134, v135\n\t"
    "v_mfma_f32_16x16x32_bf16 v[76:79], a[228:231], v[156:159], v[76:79]\n\t"
    "v_exp_f32 v136, v136\n\t"
    "v_mfma_f32_16x16x32_bf16 v[92:95], a[236:239], v[156:159], v[92:95]\n\t"
    "ds_read_b64_tr_b16 a[184:185], v198 offset:768\n\t"
    "ds_read_b64_tr_b16 a[186:187], v198 offset:4864\n\t"
    "v_mfma_f32_16x16x32_bf16 v[92:95], a[232:235], v[152:155], v[92:95]\n\t"
    "v_exp_f32 v137, v137\n\t"
    "v_mfma_f32_16x16x32_bf16 v[108:111], a[240:243], v[152:155], v[108:111]\n\t"
    "ds_read_b64_tr_b16 a[188:189], v198 offset:8960\n\t"
    "v_cvt_pk_bf16_f32 v148, v136, v137\n\t"
    "v_mfma_f32_16x16x32_bf16 v[108:111], a[244:247], v[156:159], v[108:111]\n\t"
    "v_exp_f32 v138, v138\n\t"
    "v_mfma_f32_16x16x32_bf16 v[124:127], a[252:255], v[156:159], v[124:127]\n\t"
    "ds_read_b64_tr_b16 a[190:191], v198 offset:13056\n\t"
    "v_mfma_f32_16x16x32_bf16 v[124:127], a[248:251], v[152:155], v[124:127]\n\t"
    "v_exp_f32 v139, v139\n\t"
    "v_smfmac_f32_16x16x64_bf16 v[188:191], v[192:195], v[152:159], v245\n\t"
    "v_cvt_pk_bf16_f32 v149, v138, v139\n\t"
    "v_mfma_f32_16x16x32_bf16 v[128:131], a[64:67], a[16:19], v[164:167]\n\t"
    "v_mfma_f32_16x16x32_bf16 v[128:131], a[68:71], a[20:23], v[128:131]\n\t"
    "s_add_u32 m0, s64, 49152\n\t"
    "v_exp_f32 v140, v140\n\t"
    "v_mfma_f32_16x16x32_bf16 v[128:131], a[72:75], a[24:27], v[128:131]\n\t"
    "buffer_load_dwordx4 v199, s[36:39], s65 offen offset:0 nt lds\n\t"
    "v_mfma_f32_16x16x32_bf16 v[128:131], a[76:79], a[28:31], v[128:131]\n\t"
    "v_exp_f32 v141, v141\n\t"
    "v_mfma_f32_16x16x32_bf16 v[132:135], a[92:95], a[28:31], v[164:167]\n\t"
    "v_cvt_pk_bf16_f32 v150, v140, v141\n\t"
    "v_mfma_f32_16x16x32_bf16 v[132:135], a[88:91], a[24:27], v[132:135]\n\t"
    "v_exp_f32 v142, v142\n\t"
    "v_mfma_f32_16x16x32_bf16 v[132:135], a[84:87], a[20:23], v[132:135]\n\t"
    "v_mfma_f32_16x16x32_bf16 v[132:135], a[80:83], a[16:19], v[132:135]\n\t"
    "v_exp_f32 v143, v143\n\t"
    "v_mfma_f32_16x16x32_bf16 v[136:139], a[96:99], a[16:19], v[164:167]\n\t"
    "v_cvt_pk_bf16_f32 v151, v142, v143\n\t"
    "v_mfma_f32_16x16x32_bf16 v[136:139], a[100:103], a[20:23], v[136:139]\n\t"
    "v_exp_f32 v128, v128\n\t"
    "v_mfma_f32_16x16x32_bf16 v[136:139], a[104:107], a[24:27], v[136:139]\n\t"
    "buffer_load_dwordx4 v200, s[36:39], s65 offen offset:1024 nt lds\n\t"
    "v_mfma_f32_16x16x32_bf16 v[136:139], a[108:111], a[28:31], v[136:139]\n\t"
    "v_exp_f32 v129, v129\n\t"
    "v_mfma_f32_16x16x32_bf16 v[140:143], a[124:127], a[28:31], v[164:167]\n\t"
    "v_cvt_pk_bf16_f32 v152, v128, v129\n\t"
    "v_mfma_f32_16x16x32_bf16 v[140:143], a[120:123], a[24:27], v[140:143]\n\t"
    "s_waitcnt lgkmcnt(0)\n\t"
    "v_exp_f32 v130, v130\n\t"
    "v_mfma_f32_16x16x32_bf16 v[140:143], a[116:119], a[20:23], v[140:143]\n\t"
    "v_mfma_f32_16x16x32_bf16 v[140:143], a[112:115], a[16:19], v[140:143]\n\t"
    "v_exp_f32 v131, v131\n\t"
    "v_mfma_f32_16x16x32_bf16 v[0:3], a[128:131], v[144:147], v[0:3]\n\t"
    "v_cvt_pk_bf16_f32 v153, v130, v131\n\t"
    "v_mfma_f32_16x16x32_bf16 v[0:3], a[132:135], v[148:151], v[0:3]\n\t"
    "v_exp_f32 v132, v132\n\t"
    "v_mfma_f32_16x16x32_bf16 v[16:19], a[140:143], v[148:151], v[16:19]\n\t"
    "buffer_load_dwordx4 v201, s[36:39], s65 offen offset:2048 nt lds\n\t"
    "v_mfma_f32_16x16x32_bf16 v[16:19], a[136:139], v[144:147], v[16:19]\n\t"
    "v_exp_f32 v133, v133\n\t"
    "v_mfma_f32_16x16x32_bf16 v[32:35], a[144:147], v[144:147], v[32:35]\n\t"
    "v_cvt_pk_bf16_f32 v154, v132, v133\n\t"
    "v_mfma_f32_16x16x32_bf16 v[32:35], a[148:151], v[148:151], v[32:35]\n\t"
    "v_exp_f32 v134, v134\n\t"
    "v_mfma_f32_16x16x32_bf16 v[48:51], a[156:159], v[148:151], v[48:51]\n\t"
    "v_mfma_f32_16x16x32_bf16 v[48:51], a[152:155], v[144:147], v[48:51]\n\t"
    "v_exp_f32 v135, v135\n\t"
    "v_mfma_f32_16x16x32_bf16 v[64:67], a[160:163], v[144:147], v[64:67]\n\t"
    "v_cvt_pk_bf16_f32 v155, v134, v135\n\t"
    "v_mfma_f32_16x16x32_bf16 v[64:67], a[164:167], v[148:151], v[64:67]\n\t"
    "v_exp_f32 v136, v136\n\t"
    "v_mfma_f32_16x16x32_bf16 v[80:83], a[172:175], v[148:151], v[80:83]\n\t"
    "buffer_load_dwordx4 v202, s[36:39], s65 offen offset:3072 nt lds\n\t"
    "v_mfma_f32_16x16x32_bf16 v[80:83], a[168:171], v[144:147], v[80:83]\n\t"
    "v_exp_f32 v137, v137\n\t"
    "v_mfma_f32_16x16x32_bf16 v[96:99], a[176:179], v[144:147], v[96:99]\n\t"
    "v_cvt_pk_bf16_f32 v156, v136, v137\n\t"
    "v_mfma_f32_16x16x32_bf16 v[96:99], a[180:183], v[148:151], v[96:99]\n\t"
    "v_exp_f32 v138, v138\n\t"
    "v_mfma_f32_16x16x32_bf16 v[112:115], a[188:191], v[148:151], v[112:115]\n\t"
    "v_mfma_f32_16x16x32_bf16 v[112:115], a[184:187], v[144:147], v[112:115]\n\t"
    "v_exp_f32 v139, v139\n\t"
    "v_smfmac_f32_16x16x64_bf16 v[176:179], v[192:195], v[144:151], v245\n\t"
    "v_cvt_pk_bf16_f32 v157, v138, v139\n\t"
    "v_mfma_f32_16x16x32_bf16 v[128:131], a[64:67], a[32:35], v[168:171]\n\t"
    "v_mfma_f32_16x16x32_bf16 v[128:131], a[68:71], a[36:39], v[128:131]\n\t"
    "s_add_u32 m0, s64, 114688\n\t"
    "v_exp_f32 v140, v140\n\t"
    "v_mfma_f32_16x16x32_bf16 v[128:131], a[72:75], a[40:43], v[128:131]\n\t"
    "buffer_load_dwordx4 v199, s[40:43], s65 offen offset:0 nt lds\n\t"
    "v_mfma_f32_16x16x32_bf16 v[128:131], a[76:79], a[44:47], v[128:131]\n\t"
    "v_exp_f32 v141, v141\n\t"
    "v_mfma_f32_16x16x32_bf16 v[132:135], a[92:95], a[44:47], v[168:171]\n\t"
    "v_cvt_pk_bf16_f32 v158, v140, v141\n\t"
    "v_mfma_f32_16x16x32_bf16 v[132:135], a[88:91], a[40:43], v[132:135]\n\t"
    "v_exp_f32 v142, v142\n\t"
    "v_mfma_f32_16x16x32_bf16 v[132:135], a[84:87], a[36:39], v[132:135]\n\t"
    "v_mfma_f32_16x16x32_bf16 v[132:135], a[80:83], a[32:35], v[132:135]\n\t"
    "v_exp_f32 v143, v143\n\t"
    "v_mfma_f32_16x16x32_bf16 v[136:139], a[96:99], a[32:35], v[168:171]\n\t"
    "v_cvt_pk_bf16_f32 v159, v142, v143\n\t"
    "v_mfma_f32_16x16x32_bf16 v[136:139], a[100:103], a[36:39], v[136:139]\n\t"
    "v_exp_f32 v128, v128\n\t"
    "v_mfma_f32_16x16x32_bf16 v[136:139], a[104:107], a[40:43], v[136:139]\n\t"
    "buffer_load_dwordx4 v200, s[40:43], s65 offen offset:1024 nt lds\n\t"
    "v_mfma_f32_16x16x32_bf16 v[136:139], a[108:111], a[44:47], v[136:139]\n\t"
    "v_exp_f32 v129, v129\n\t"
    "v_mfma_f32_16x16x32_bf16 v[140:143], a[124:127], a[44:47], v[168:171]\n\t"
    "v_cvt_pk_bf16_f32 v144, v128, v129\n\t"
    "v_mfma_f32_16x16x32_bf16 v[140:143], a[120:123], a[40:43], v[140:143]\n\t"
    "v_exp_f32 v130, v130\n\t"
    "v_mfma_f32_16x16x32_bf16 v[140:143], a[116:119], a[36:39], v[140:143]\n\t"
    "v_mfma_f32_16x16x32_bf16 v[140:143], a[112:115], a[32:35], v[140:143]\n\t"
    "v_exp_f32 v131, v131\n\t"
    "v_mfma_f32_16x16x32_bf16 v[4:7], a[128:131], v[152:155], v[4:7]\n\t"
    "v_cvt_pk_bf16_f32 v145, v130, v131\n\t"
    "v_mfma_f32_16x16x32_bf16 v[4:7], a[132:135], v[156:159], v[4:7]\n\t"
    "v_exp_f32 v132, v132\n\t"
    "v_mfma_f32_16x16x32_bf16 v[20:23], a[140:143], v[156:159], v[20:23]\n\t"
    "buffer_load_dwordx4 v201, s[40:43], s65 offen offset:2048 nt lds\n\t"
    "v_mfma_f32_16x16x32_bf16 v[20:23], a[136:139], v[152:155], v[20:23]\n\t"
    "v_exp_f32 v133, v133\n\t"
    "v_mfma_f32_16x16x32_bf16 v[36:39], a[144:147], v[152:155], v[36:39]\n\t"
    "v_cvt_pk_bf16_f32 v146, v132, v133\n\t"
    "v_mfma_f32_16x16x32_bf16 v[36:39], a[148:151], v[156:159], v[36:39]\n\t"
    "v_exp_f32 v134, v134\n\t"
    "v_mfma_f32_16x16x32_bf16 v[52:55], a[156:159], v[156:159], v[52:55]\n\t"
    "v_mfma_f32_16x16x32_bf16 v[52:55], a[152:155], v[152:155], v[52:55]\n\t"
    "v_exp_f32 v135, v135\n\t"
    "v_mfma_f32_16x16x32_bf16 v[68:71], a[160:163], v[152:155], v[68:71]\n\t"
    "v_cvt_pk_bf16_f32 v147, v134, v135\n\t"
    "v_mfma_f32_16x16x32_bf16 v[68:71], a[164:167], v[156:159], v[68:71]\n\t"
    "v_exp_f32 v136, v136\n\t"
    "v_mfma_f32_16x16x32_bf16 v[84:87], a[172:175], v[156:159], v[84:87]\n\t"
    "buffer_load_dwordx4 v202, s[40:43], s65 offen offset:3072 nt lds\n\t"
    "v_mfma_f32_16x16x32_bf16 v[84:87], a[168:171], v[152:155], v[84:87]\n\t"
    "v_exp_f32 v137, v137\n\t"
    "v_mfma_f32_16x16x32_bf16 v[100:103], a[176:179], v[152:155], v[100:103]\n\t"
    "v_cvt_pk_bf16_f32 v148, v136, v137\n\t"
    "v_mfma_f32_16x16x32_bf16 v[100:103], a[180:183], v[156:159], v[100:103]\n\t"
    "v_exp_f32 v138, v138\n\t"
    "v_mfma_f32_16x16x32_bf16 v[116:119], a[188:191], v[156:159], v[116:119]\n\t"
    "s_add_u32 s65, s65, 16384\n\t"
    "v_mfma_f32_16x16x32_bf16 v[116:119], a[184:187], v[152:155], v[116:119]\n\t"
    "v_exp_f32 v139, v139\n\t"
    "v_smfmac_f32_16x16x64_bf16 v[180:183], v[192:195], v[152:159], v245\n\t"
    "s_waitcnt vmcnt(16)\n\t"
    "s_barrier\n\t"
    "v_cvt_pk_bf16_f32 v149, v138, v139\n\t"
    "v_mfma_f32_16x16x32_bf16 v[128:131], a[64:67], a[48:51], v[172:175]\n\t"
    "ds_read_b128 a[64:67], v196 offset:16384\n\t"
    "v_mfma_f32_16x16x32_bf16 v[128:131], a[68:71], a[52:55], v[128:131]\n\t"
    "v_exp_f32 v140, v140\n\t"
    "v_mfma_f32_16x16x32_bf16 v[128:131], a[72:75], a[56:59], v[128:131]\n\t"
    "ds_read_b128 a[68:71], v196 offset:16640\n\t"
    "ds_read_b128 a[72:75], v196 offset:16896\n\t"
    "v_mfma_f32_16x16x32_bf16 v[128:131], a[76:79], a[60:63], v[128:131]\n\t"
    "v_exp_f32 v141, v141\n\t"
    "v_mfma_f32_16x16x32_bf16 v[132:135], a[92:95], a[60:63], v[172:175]\n\t"
    "ds_read_b128 a[76:79], v196 offset:17152\n\t"
    "v_cvt_pk_bf16_f32 v150, v140, v141\n\t"
    "v_mfma_f32_16x16x32_bf16 v[132:135], a[88:91], a[56:59], v[132:135]\n\t"
    "v_exp_f32 v142, v142\n\t"
    "v_mfma_f32_16x16x32_bf16 v[132:135], a[84:87], a[52:55], v[132:135]\n\t"
    "ds_read_b128 a[92:95], v196 offset:21248\n\t"
    "ds_read_b128 a[88:91], v196 offset:20992\n\t"
    "v_mfma_f32_16x16x32_bf16 v[132:135], a[80:83], a[48:51], v[132:135]\n\t"
    "v_exp_f32 v143, v143\n\t"
    "v_mfma_f32_16x16x32_bf16 v[136:139], a[96:99], a[48:51], v[172:175]\n\t"
    "ds_read_b128 a[84:87], v196 offset:20736\n\t"
    "v_cvt_pk_bf16_f32 v151, v142, v143\n\t"
    "v_mfma_f32_16x16x32_bf16 v[136:139], a[100:103], a[52:55], v[136:139]\n\t"
    "v_exp_f32 v128, v128\n\t"
    "v_mfma_f32_16x16x32_bf16 v[136:139], a[104:107], a[56:59], v[136:139]\n\t"
    "ds_read_b128 a[80:83], v196 offset:20480\n\t"
    "ds_read_b128 a[96:99], v196 offset:24576\n\t"
    "v_mfma_f32_16x16x32_bf16 v[136:139], a[108:111], a[60:63], v[136:139]\n\t"
    "v_exp_f32 v129, v129\n\t"
    "v_mfma_f32_16x16x32_bf16 v[140:143], a[124:127], a[60:63], v[172:175]\n\t"
    "ds_read_b128 a[100:103], v196 offset:24832\n\t"
    "v_cvt_pk_bf16_f32 v152, v128, v129\n\t"
    "v_mfma_f32_16x16x32_bf16 v[140:143], a[120:123], a[56:59], v[140:143]\n\t"
    "v_exp_f32 v130, v130\n\t"
    "v_mfma_f32_16x16x32_bf16 v[140:143], a[116:119], a[52:55], v[140:143]\n\t"
    "ds_read_b128 a[104:107], v196 offset:25088\n\t"
    "ds_read_b128 a[108:111], v196 offset:25344\n\t"
    "v_mfma_f32_16x16x32_bf16 v[140:143], a[112:115], a[48:51], v[140:143]\n\t"
    "v_exp_f32 v131, v131\n\t"
    "v_mfma_f32_16x16x32_bf16 v[8:11], a[128:131], v[144:147], v[8:11]\n\t"
    "ds_read_b128 a[124:127], v196 offset:29440\n\t"
    "v_cvt_pk_bf16_f32 v153, v130, v131\n\t"
    "v_mfma_f32_16x16x32_bf16 v[8:11], a[132:135], v[148:151], v[8:11]\n\t"
    "v_exp_f32 v132, v132\n\t"
    "v_mfma_f32_16x16x32_bf16 v[24:27], a[140:143], v[148:151], v[24:27]\n\t"
    "ds_read_b128 a[120:123], v196 offset:29184\n\t"
    "ds_read_b128 a[116:119], v196 offset:28928\n\t"
    "v_mfma_f32_16x16x32_bf16 v[24:27], a[136:139], v[144:147], v[24:27]\n\t"
    "v_exp_f32 v133, v133\n\t"
    "v_mfma_f32_16x16x32_bf16 v[40:43], a[144:147], v[144:147], v[40:43]\n\t"
    "ds_read_b128 a[112:115], v196 offset:28672\n\t"
    "v_cvt_pk_bf16_f32 v154, v132, v133\n\t"
    "v_mfma_f32_16x16x32_bf16 v[40:43], a[148:151], v[148:151], v[40:43]\n\t"
    "v_exp_f32 v134, v134\n\t"
    "v_mfma_f32_16x16x32_bf16 v[56:59], a[156:159], v[148:151], v[56:59]\n\t"
    "ds_read_b64_tr_b16 a[192:193], v197 offset:16384\n\t"
    "ds_read_b64_tr_b16 a[194:195], v197 offset:20480\n\t"
    "v_mfma_f32_16x16x32_bf16 v[56:59], a[152:155], v[144:147], v[56:59]\n\t"
    "v_exp_f32 v135, v135\n\t"
    "v_mfma_f32_16x16x32_bf16 v[72:75], a[160:163], v[144:147], v[72:75]\n\t"
    "ds_read_b64_tr_b16 a[196:197], v197 offset:24576\n\t"
    "v_cvt_pk_bf16_f32 v155, v134, v135\n\t"
    "v_mfma_f32_16x16x32_bf16 v[72:75], a[164:167], v[148:151], v[72:75]\n\t"
    "v_exp_f32 v136, v136\n\t"
    "v_mfma_f32_16x16x32_bf16 v[88:91], a[172:175], v[148:151], v[88:91]\n\t"
    "ds_read_b64_tr_b16 a[198:199], v197 offset:28672\n\t"
    "ds_read_b64_tr_b16 a[200:201], v198 offset:16384\n\t"
    "v_mfma_f32_16x16x32_bf16 v[88:91], a[168:171], v[144:147], v[88:91]\n\t"
    "v_exp_f32 v137, v137\n\t"
    "v_mfma_f32_16x16x32_bf16 v[104:107], a[176:179], v[144:147], v[104:107]\n\t"
    "ds_read_b64_tr_b16 a[202:203], v198 offset:20480\n\t"
    "v_cvt_pk_bf16_f32 v156, v136, v137\n\t"
    "v_mfma_f32_16x16x32_bf16 v[104:107], a[180:183], v[148:151], v[104:107]\n\t"
    "v_exp_f32 v138, v138\n\t"
    "v_mfma_f32_16x16x32_bf16 v[120:123], a[188:191], v[148:151], v[120:123]\n\t"
    "ds_read_b64_tr_b16 a[204:205], v198 offset:24576\n\t"
    "ds_read_b64_tr_b16 a[206:207], v198 offset:28672\n\t"
    "v_mfma_f32_16x16x32_bf16 v[120:123], a[184:187], v[144:147], v[120:123]\n\t"
    "v_exp_f32 v139, v139\n\t"
    "v_smfmac_f32_16x16x64_bf16 v[184:187], v[192:195], v[144:151], v245\n\t"
    "s_waitcnt lgkmcnt(8)\n\t"
    "v_cvt_pk_bf16_f32 v157, v138, v139\n\t"
    "s_cmp_eq_u32 s66, 2\n\t"
    "s_cbranch_scc1 5f\n\t"
    "v_mfma_f32_16x16x32_bf16 v[128:131], a[64:67], a[0:3], v[160:163]\n\t"
    "ds_read_b64_tr_b16 a[208:209], v197 offset:16640\n\t"
    "ds_read_b64_tr_b16 a[210:211], v197 offset:20736\n\t"
    "v_mfma_f32_16x16x32_bf16 v[128:131], a[68:71], a[4:7], v[128:131]\n\t"
    "v_exp_f32 v140, v140\n\t"
    "v_mfma_f32_16x16x32_bf16 v[128:131], a[72:75], a[8:11], v[128:131]\n\t"
    "ds_read_b64_tr_b16 a[212:213], v197 offset:24832\n\t"
    "ds_read_b64_tr_b16 a[214:215], v197 offset:28928\n\t"
    "v_mfma_f32_16x16x32_bf16 v[128:131], a[76:79], a[12:15], v[128:131]\n\t"
    "v_exp_f32 v141, v141\n\t"
    "v_mfma_f32_16x16x32_bf16 v[132:135], a[92:95], a[12:15], v[160:163]\n\t"
    "ds_read_b64_tr_b16 a[216:217], v198 offset:16640\n\t"
    "v_cvt_pk_bf16_f32 v158, v140, v141\n\t"
    "v_mfma_f32_16x16x32_bf16 v[132:135], a[88:91], a[8:11], v[132:135]\n\t"
    "v_exp_f32 v142, v142\n\t"
    "v_mfma_f32_16x16x32_bf16 v[132:135], a[84:87], a[4:7], v[132:135]\n\t"
    "ds_read_b64_tr_b16 a[218:219], v198 offset:20736\n\t"
    "ds_read_b64_tr_b16 a[220:221], v198 offset:24832\n\t"
    "v_mfma_f32_16x16x32_bf16 v[132:135], a[80:83], a[0:3], v[132:135]\n\t"
    "v_exp_f32 v143, v143\n\t"
    "v_mfma_f32_16x16x32_bf16 v[136:139], a[96:99], a[0:3], v[160:163]\n\t"
    "ds_read_b64_tr_b16 a[222:223], v198 offset:28928\n\t"
    "v_cvt_pk_bf16_f32 v159, v142, v143\n\t"
    "v_mfma_f32_16x16x32_bf16 v[136:139], a[100:103], a[4:7], v[136:139]\n\t"
    "v_exp_f32 v128, v128\n\t"
    "v_mfma_f32_16x16x32_bf16 v[136:139], a[104:107], a[8:11], v[136:139]\n\t"
    "ds_read_b64_tr_b16 a[224:225], v197 offset:16896\n\t"
    "ds_read_b64_tr_b16 a[226:227], v197 offset:20992\n\t"
    "v_mfma_f32_16x16x32_bf16 v[136:139], a[108:111], a[12:15], v[136:139]\n\t"
    "v_exp_f32 v129, v129\n\t"
    "v_mfma_f32_16x16x32_bf16 v[140:143], a[124:127], a[12:15], v[160:163]\n\t"
    "ds_read_b64_tr_b16 a[228:229], v197 offset:25088\n\t"
    "v_cvt_pk_bf16_f32 v144, v128, v129\n\t"
    "v_mfma_f32_16x16x32_bf16 v[140:143], a[120:123], a[8:11], v[140:143]\n\t"
    "v_exp_f32 v130, v130\n\t"
    "v_mfma_f32_16x16x32_bf16 v[140:143], a[116:119], a[4:7], v[140:143]\n\t"
    "ds_read_b64_tr_b16 a[230:231], v197 offset:29184\n\t"
    "ds_read_b64_tr_b16 a[232:233], v198 offset:16896\n\t"
    "v_mfma_f32_16x16x32_bf16 v[140:143], a[112:115], a[0:3], v[140:143]\n\t"
    "v_exp_f32 v131, v131\n\t"
    "v_mfma_f32_16x16x32_bf16 v[12:15], a[128:131], v[152:155], v[12:15]\n\t"
    "ds_read_b64_tr_b16 a[234:235], v198 offset:20992\n\t"
    "v_cvt_pk_bf16_f32 v145, v130, v131\n\t"
    "v_mfma_f32_16x16x32_bf16 v[12:15], a[132:135], v[156:159], v[12:15]\n\t"
    "v_exp_f32 v132, v132\n\t"
    "v_mfma_f32_16x16x32_bf16 v[28:31], a[140:143], v[156:159], v[28:31]\n\t"
    "ds_read_b64_tr_b16 a[236:237], v198 offset:25088\n\t"
    "ds_read_b64_tr_b16 a[238:239], v198 offset:29184\n\t"
    "v_mfma_f32_16x16x32_bf16 v[28:31], a[136:139], v[152:155], v[28:31]\n\t"
    "v_exp_f32 v133, v133\n\t"
    "v_mfma_f32_16x16x32_bf16 v[44:47], a[144:147], v[152:155], v[44:47]\n\t"
    "ds_read_b64_tr_b16 a[240:241], v197 offset:17152\n\t"
    "v_cvt_pk_bf16_f32 v146, v132, v133\n\t"
    "v_mfma_f32_16x16x32_bf16 v[44:47], a[148:151], v[156:159], v[44:47]\n\t"
    "v_exp_f32 v134, v134\n\t"
    "v_mfma_f32_16x16x32_bf16 v[60:63], a[156:159], v[156:159], v[60:63]\n\t"
    "ds_read_b64_tr_b16 a[242:243], v197 offset:21248\n\t"
    "ds_read_b64_tr_b16 a[244:245], v197 offset:25344\n\t"
    "v_mfma_f32_16x16x32_bf16 v[60:63], a[152:155], v[152:155], v[60:63]\n\t"
    "v_exp_f32 v135, v135\n\t"
    "v_mfma_f32_16x16x32_bf16 v[76:79], a[160:163], v[152:155], v[76:79]\n\t"
    "ds_read_b64_tr_b16 a[246:247], v197 offset:29440\n\t"
    "v_cvt_pk_bf16_f32 v147, v134, v135\n\t"
    "v_mfma_f32_16x16x32_bf16 v[76:79], a[164:167], v[156:159], v[76:79]\n\t"
    "v_exp_f32 v136, v136\n\t"
    "v_mfma_f32_16x16x32_bf16 v[92:95], a[172:175], v[156:159], v[92:95]\n\t"
    "ds_read_b64_tr_b16 a[248:249], v198 offset:17152\n\t"
    "ds_read_b64_tr_b16 a[250:251], v198 offset:21248\n\t"
    "v_mfma_f32_16x16x32_bf16 v[92:95], a[168:171], v[152:155], v[92:95]\n\t"
    "v_exp_f32 v137, v137\n\t"
    "v_mfma_f32_16x16x32_bf16 v[108:111], a[176:179], v[152:155], v[108:111]\n\t"
    "ds_read_b64_tr_b16 a[252:253], v198 offset:25344\n\t"
    "v_cvt_pk_bf16_f32 v148, v136, v137\n\t"
    "v_mfma_f32_16x16x32_bf16 v[108:111], a[180:183], v[156:159], v[108:111]\n\t"
    "v_exp_f32 v138, v138\n\t"
    "v_mfma_f32_16x16x32_bf16 v[124:127], a[188:191], v[156:159], v[124:127]\n\t"
    "ds_read_b64_tr_b16 a[254:255], v198 offset:29440\n\t"
    "v_mfma_f32_16x16x32_bf16 v[124:127], a[184:187], v[152:155], v[124:127]\n\t"
    "v_exp_f32 v139, v139\n\t"
    "v_smfmac_f32_16x16x64_bf16 v[188:191], v[192:195], v[152:159], v245\n\t"
    "v_cvt_pk_bf16_f32 v149, v138, v139\n\t"
    "v_mfma_f32_16x16x32_bf16 v[128:131], a[64:67], a[16:19], v[164:167]\n\t"
    "v_mfma_f32_16x16x32_bf16 v[128:131], a[68:71], a[20:23], v[128:131]\n\t"
    "s_add_u32 m0, s64, 0\n\t"
    "v_exp_f32 v140, v140\n\t"
    "v_mfma_f32_16x16x32_bf16 v[128:131], a[72:75], a[24:27], v[128:131]\n\t"
    "buffer_load_dwordx4 v199, s[36:39], s65 offen offset:0 nt lds\n\t"
    "v_mfma_f32_16x16x32_bf16 v[128:131], a[76:79], a[28:31], v[128:131]\n\t"
    "v_exp_f32 v141, v141\n\t"
    "v_mfma_f32_16x16x32_bf16 v[132:135], a[92:95], a[28:31], v[164:167]\n\t"
    "v_cvt_pk_bf16_f32 v150, v140, v141\n\t"
    "v_mfma_f32_16x16x32_bf16 v[132:135], a[88:91], a[24:27], v[132:135]\n\t"
    "v_exp_f32 v142, v142\n\t"
    "v_mfma_f32_16x16x32_bf16 v[132:135], a[84:87], a[20:23], v[132:135]\n\t"
    "v_mfma_f32_16x16x32_bf16 v[132:135], a[80:83], a[16:19], v[132:135]\n\t"
    "v_exp_f32 v143, v143\n\t"
    "v_mfma_f32_16x16x32_bf16 v[136:139], a[96:99], a[16:19], v[164:167]\n\t"
    "v_cvt_pk_bf16_f32 v151, v142, v143\n\t"
    "v_mfma_f32_16x16x32_bf16 v[136:139], a[100:103], a[20:23], v[136:139]\n\t"
    "v_exp_f32 v128, v128\n\t"
    "v_mfma_f32_16x16x32_bf16 v[136:139], a[104:107], a[24:27], v[136:139]\n\t"
    "buffer_load_dwordx4 v200, s[36:39], s65 offen offset:1024 nt lds\n\t"
    "v_mfma_f32_16x16x32_bf16 v[136:139], a[108:111], a[28:31], v[136:139]\n\t"
    "v_exp_f32 v129, v129\n\t"
    "v_mfma_f32_16x16x32_bf16 v[140:143], a[124:127], a[28:31], v[164:167]\n\t"
    "v_cvt_pk_bf16_f32 v152, v128, v129\n\t"
    "v_mfma_f32_16x16x32_bf16 v[140:143], a[120:123], a[24:27], v[140:143]\n\t"
    "s_waitcnt lgkmcnt(0)\n\t"
    "v_exp_f32 v130, v130\n\t"
    "v_mfma_f32_16x16x32_bf16 v[140:143], a[116:119], a[20:23], v[140:143]\n\t"
    "v_mfma_f32_16x16x32_bf16 v[140:143], a[112:115], a[16:19], v[140:143]\n\t"
    "v_exp_f32 v131, v131\n\t"
    "v_mfma_f32_16x16x32_bf16 v[0:3], a[192:195], v[144:147], v[0:3]\n\t"
    "v_cvt_pk_bf16_f32 v153, v130, v131\n\t"
    "v_mfma_f32_16x16x32_bf16 v[0:3], a[196:199], v[148:151], v[0:3]\n\t"
    "v_exp_f32 v132, v132\n\t"
    "v_mfma_f32_16x16x32_bf16 v[16:19], a[204:207], v[148:151], v[16:19]\n\t"
    "buffer_load_dwordx4 v201, s[36:39], s65 offen offset:2048 nt lds\n\t"
    "v_mfma_f32_16x16x32_bf16 v[16:19], a[200:203], v[144:147], v[16:19]\n\t"
    "v_exp_f32 v133, v133\n\t"
    "v_mfma_f32_16x16x32_bf16 v[32:35], a[208:211], v[144:147], v[32:35]\n\t"
    "v_cvt_pk_bf16_f32 v154, v132, v133\n\t"
    "v_mfma_f32_16x16x32_bf16 v[32:35], a[212:215], v[148:151], v[32:35]\n\t"
    "v_exp_f32 v134, v134\n\t"
    "v_mfma_f32_16x16x32_bf16 v[48:51], a[220:223], v[148:151], v[48:51]\n\t"
    "v_mfma_f32_16x16x32_bf16 v[48:51], a[216:219], v[144:147], v[48:51]\n\t"
    "v_exp_f32 v135, v135\n\t"
    "v_mfma_f32_16x16x32_bf16 v[64:67], a[224:227], v[144:147], v[64:67]\n\t"
    "v_cvt_pk_bf16_f32 v155, v134, v135\n\t"
    "v_mfma_f32_16x16x32_bf16 v[64:67], a[228:231], v[148:151], v[64:67]\n\t"
    "v_exp_f32 v136, v136\n\t"
    "v_mfma_f32_16x16x32_bf16 v[80:83], a[236:239], v[148:151], v[80:83]\n\t"
    "buffer_load_dwordx4 v202, s[36:39], s65 offen offset:3072 nt lds\n\t"
    "v_mfma_f32_16x16x32_bf16 v[80:83], a[232:235], v[144:147], v[80:83]\n\t"
    "v_exp_f32 v137, v137\n\t"
    "v_mfma_f32_16x16x32_bf16 v[96:99], a[240:243], v[144:147], v[96:99]\n\t"
    "v_cvt_pk_bf16_f32 v156, v136, v137\n\t"
    "v_mfma_f32_16x16x32_bf16 v[96:99], a[244:247], v[148:151], v[96:99]\n\t"
    "v_exp_f32 v138, v138\n\t"
    "v_mfma_f32_16x16x32_bf16 v[112:115], a[252:255], v[148:151], v[112:115]\n\t"
    "v_mfma_f32_16x16x32_bf16 v[112:115], a[248:251], v[144:147], v[112:115]\n\t"
    "v_exp_f32 v139, v139\n\t"
    "v_smfmac_f32_16x16x64_bf16 v[176:179], v[192:195], v[144:151], v245\n\t"
    "v_cvt_pk_bf16_f32 v157, v138, v139\n\t"
    "v_mfma_f32_16x16x32_bf16 v[128:131], a[64:67], a[32:35], v[168:171]\n\t"
    "v_mfma_f32_16x16x32_bf16 v[128:131], a[68:71], a[36:39], v[128:131]\n\t"
    "s_add_u32 m0, s64, 65536\n\t"
    "v_exp_f32 v140, v140\n\t"
    "v_mfma_f32_16x16x32_bf16 v[128:131], a[72:75], a[40:43], v[128:131]\n\t"
    "buffer_load_dwordx4 v199, s[40:43], s65 offen offset:0 nt lds\n\t"
    "v_mfma_f32_16x16x32_bf16 v[128:131], a[76:79], a[44:47], v[128:131]\n\t"
    "v_exp_f32 v141, v141\n\t"
    "v_mfma_f32_16x16x32_bf16 v[132:135], a[92:95], a[44:47], v[168:171]\n\t"
    "v_cvt_pk_bf16_f32 v158, v140, v141\n\t"
    "v_mfma_f32_16x16x32_bf16 v[132:135], a[88:91], a[40:43], v[132:135]\n\t"
    "v_exp_f32 v142, v142\n\t"
    "v_mfma_f32_16x16x32_bf16 v[132:135], a[84:87], a[36:39], v[132:135]\n\t"
    "v_mfma_f32_16x16x32_bf16 v[132:135], a[80:83], a[32:35], v[132:135]\n\t"
    "v_exp_f32 v143, v143\n\t"
    "v_mfma_f32_16x16x32_bf16 v[136:139], a[96:99], a[32:35], v[168:171]\n\t"
    "v_cvt_pk_bf16_f32 v159, v142, v143\n\t"
    "v_mfma_f32_16x16x32_bf16 v[136:139], a[100:103], a[36:39], v[136:139]\n\t"
    "v_exp_f32 v128, v128\n\t"
    "v_mfma_f32_16x16x32_bf16 v[136:139], a[104:107], a[40:43], v[136:139]\n\t"
    "buffer_load_dwordx4 v200, s[40:43], s65 offen offset:1024 nt lds\n\t"
    "v_mfma_f32_16x16x32_bf16 v[136:139], a[108:111], a[44:47], v[136:139]\n\t"
    "v_exp_f32 v129, v129\n\t"
    "v_mfma_f32_16x16x32_bf16 v[140:143], a[124:127], a[44:47], v[168:171]\n\t"
    "v_cvt_pk_bf16_f32 v144, v128, v129\n\t"
    "v_mfma_f32_16x16x32_bf16 v[140:143], a[120:123], a[40:43], v[140:143]\n\t"
    "v_exp_f32 v130, v130\n\t"
    "v_mfma_f32_16x16x32_bf16 v[140:143], a[116:119], a[36:39], v[140:143]\n\t"
    "v_mfma_f32_16x16x32_bf16 v[140:143], a[112:115], a[32:35], v[140:143]\n\t"
    "v_exp_f32 v131, v131\n\t"
    "v_mfma_f32_16x16x32_bf16 v[4:7], a[192:195], v[152:155], v[4:7]\n\t"
    "v_cvt_pk_bf16_f32 v145, v130, v131\n\t"
    "v_mfma_f32_16x16x32_bf16 v[4:7], a[196:199], v[156:159], v[4:7]\n\t"
    "v_exp_f32 v132, v132\n\t"
    "v_mfma_f32_16x16x32_bf16 v[20:23], a[204:207], v[156:159], v[20:23]\n\t"
    "buffer_load_dwordx4 v201, s[40:43], s65 offen offset:2048 nt lds\n\t"
    "v_mfma_f32_16x16x32_bf16 v[20:23], a[200:203], v[152:155], v[20:23]\n\t"
    "v_exp_f32 v133, v133\n\t"
    "v_mfma_f32_16x16x32_bf16 v[36:39], a[208:211], v[152:155], v[36:39]\n\t"
    "v_cvt_pk_bf16_f32 v146, v132, v133\n\t"
    "v_mfma_f32_16x16x32_bf16 v[36:39], a[212:215], v[156:159], v[36:39]\n\t"
    "v_exp_f32 v134, v134\n\t"
    "v_mfma_f32_16x16x32_bf16 v[52:55], a[220:223], v[156:159], v[52:55]\n\t"
    "v_mfma_f32_16x16x32_bf16 v[52:55], a[216:219], v[152:155], v[52:55]\n\t"
    "v_exp_f32 v135, v135\n\t"
    "v_mfma_f32_16x16x32_bf16 v[68:71], a[224:227], v[152:155], v[68:71]\n\t"
    "v_cvt_pk_bf16_f32 v147, v134, v135\n\t"
    "v_mfma_f32_16x16x32_bf16 v[68:71], a[228:231], v[156:159], v[68:71]\n\t"
    "v_exp_f32 v136, v136\n\t"
    "v_mfma_f32_16x16x32_bf16 v[84:87], a[236:239], v[156:159], v[84:87]\n\t"
    "buffer_load_dwordx4 v202, s[40:43], s65 offen offset:3072 nt lds\n\t"
    "v_mfma_f32_16x16x32_bf16 v[84:87], a[232:235], v[152:155], v[84:87]\n\t"
    "v_exp_f32 v137, v137\n\t"
    "v_mfma_f32_16x16x32_bf16 v[100:103], a[240:243], v[152:155], v[100:103]\n\t"
    "v_cvt_pk_bf16_f32 v148, v136, v137\n\t"
    "v_mfma_f32_16x16x32_bf16 v[100:103], a[244:247], v[156:159], v[100:103]\n\t"
    "v_exp_f32 v138, v138\n\t"
    "v_mfma_f32_16x16x32_bf16 v[116:119], a[252:255], v[156:159], v[116:119]\n\t"
    "s_add_u32 s65, s65, 16384\n\t"
    "v_mfma_f32_16x16x32_bf16 v[116:119], a[248:251], v[152:155], v[116:119]\n\t"
    "v_exp_f32 v139, v139\n\t"
    "v_smfmac_f32_16x16x64_bf16 v[180:183], v[192:195], v[152:159], v245\n\t"
    "s_waitcnt vmcnt(16)\n\t"
    "s_barrier\n\t"
    "v_cvt_pk_bf16_f32 v149, v138, v139\n\t"
    "v_mfma_f32_16x16x32_bf16 v[128:131], a[64:67], a[48:51], v[172:175]\n\t"
    "ds_read_b128 a[64:67], v196 offset:32768\n\t"
    "v_mfma_f32_16x16x32_bf16 v[128:131], a[68:71], a[52:55], v[128:131]\n\t"
    "v_exp_f32 v140, v140\n\t"
    "v_mfma_f32_16x16x32_bf16 v[128:131], a[72:75], a[56:59], v[128:131]\n\t"
    "ds_read_b128 a[68:71], v196 offset:33024\n\t"
    "ds_read_b128 a[72:75], v196 offset:33280\n\t"
    "v_mfma_f32_16x16x32_bf16 v[128:131], a[76:79], a[60:63], v[128:131]\n\t"
    "v_exp_f32 v141, v141\n\t"
    "v_mfma_f32_16x16x32_bf16 v[132:135], a[92:95], a[60:63], v[172:175]\n\t"
    "ds_read_b128 a[76:79], v196 offset:33536\n\t"
    "v_cvt_pk_bf16_f32 v150, v140, v141\n\t"
    "v_mfma_f32_16x16x32_bf16 v[132:135], a[88:91], a[56:59], v[132:135]\n\t"
    "v_exp_f32 v142, v142\n\t"
    "v_mfma_f32_16x16x32_bf16 v[132:135], a[84:87], a[52:55], v[132:135]\n\t"
    "ds_read_b128 a[92:95], v196 offset:37632\n\t"
    "ds_read_b128 a[88:91], v196 offset:37376\n\t"
    "v_mfma_f32_16x16x32_bf16 v[132:135], a[80:83], a[48:51], v[132:135]\n\t"
    "v_exp_f32 v143, v143\n\t"
    "v_mfma_f32_16x16x32_bf16 v[136:139], a[96:99], a[48:51], v[172:175]\n\t"
    "ds_read_b128 a[84:87], v196 offset:37120\n\t"
    "v_cvt_pk_bf16_f32 v151, v142, v143\n\t"
    "v_mfma_f32_16x16x32_bf16 v[136:139], a[100:103], a[52:55], v[136:139]\n\t"
    "v_exp_f32 v128, v128\n\t"
    "v_mfma_f32_16x16x32_bf16 v[136:139], a[104:107], a[56:59], v[136:139]\n\t"
    "ds_read_b128 a[80:83], v196 offset:36864\n\t"
    "ds_read_b128 a[96:99], v196 offset:40960\n\t"
    "v_mfma_f32_16x16x32_bf16 v[136:139], a[108:111], a[60:63], v[136:139]\n\t"
    "v_exp_f32 v129, v129\n\t"
    "v_mfma_f32_16x16x32_bf16 v[140:143], a[124:127], a[60:63], v[172:175]\n\t"
    "ds_read_b128 a[100:103], v196 offset:41216\n\t"
    "v_cvt_pk_bf16_f32 v152, v128, v129\n\t"
    "v_mfma_f32_16x16x32_bf16 v[140:143], a[120:123], a[56:59], v[140:143]\n\t"
    "v_exp_f32 v130, v130\n\t"
    "v_mfma_f32_16x16x32_bf16 v[140:143], a[116:119], a[52:55], v[140:143]\n\t"
    "ds_read_b128 a[104:107], v196 offset:41472\n\t"
    "ds_read_b128 a[108:111], v196 offset:41728\n\t"
    "v_mfma_f32_16x16x32_bf16 v[140:143], a[112:115], a[48:51], v[140:143]\n\t"
    "v_exp_f32 v131, v131\n\t"
    "v_mfma_f32_16x16x32_bf16 v[8:11], a[192:195], v[144:147], v[8:11]\n\t"
    "ds_read_b128 a[124:127], v196 offset:45824\n\t"
    "v_cvt_pk_bf16_f32 v153, v130, v131\n\t"
    "v_mfma_f32_16x16x32_bf16 v[8:11], a[196:199], v[148:151], v[8:11]\n\t"
    "v_exp_f32 v132, v132\n\t"
    "v_mfma_f32_16x16x32_bf16 v[24:27], a[204:207], v[148:151], v[24:27]\n\t"
    "ds_read_b128 a[120:123], v196 offset:45568\n\t"
    "ds_read_b128 a[116:119], v196 offset:45312\n\t"
    "v_mfma_f32_16x16x32_bf16 v[24:27], a[200:203], v[144:147], v[24:27]\n\t"
    "v_exp_f32 v133, v133\n\t"
    "v_mfma_f32_16x16x32_bf16 v[40:43], a[208:211], v[144:147], v[40:43]\n\t"
    "ds_read_b128 a[112:115], v196 offset:45056\n\t"
    "v_cvt_pk_bf16_f32 v154, v132, v133\n\t"
    "v_mfma_f32_16x16x32_bf16 v[40:43], a[212:215], v[148:151], v[40:43]\n\t"
    "v_exp_f32 v134, v134\n\t"
    "v_mfma_f32_16x16x32_bf16 v[56:59], a[220:223], v[148:151], v[56:59]\n\t"
    "ds_read_b64_tr_b16 a[128:129], v197 offset:32768\n\t"
    "ds_read_b64_tr_b16 a[130:131], v197 offset:36864\n\t"
    "v_mfma_f32_16x16x32_bf16 v[56:59], a[216:219], v[144:147], v[56:59]\n\t"
    "v_exp_f32 v135, v135\n\t"
    "v_mfma_f32_16x16x32_bf16 v[72:75], a[224:227], v[144:147], v[72:75]\n\t"
    "ds_read_b64_tr_b16 a[132:133], v197 offset:40960\n\t"
    "v_cvt_pk_bf16_f32 v155, v134, v135\n\t"
    "v_mfma_f32_16x16x32_bf16 v[72:75], a[228:231], v[148:151], v[72:75]\n\t"
    "v_exp_f32 v136, v136\n\t"
    "v_mfma_f32_16x16x32_bf16 v[88:91], a[236:239], v[148:151], v[88:91]\n\t"
    "ds_read_b64_tr_b16 a[134:135], v197 offset:45056\n\t"
    "ds_read_b64_tr_b16 a[136:137], v198 offset:32768\n\t"
    "v_mfma_f32_16x16x32_bf16 v[88:91], a[232:235], v[144:147], v[88:91]\n\t"
    "v_exp_f32 v137, v137\n\t"
    "v_mfma_f32_16x16x32_bf16 v[104:107], a[240:243], v[144:147], v[104:107]\n\t"
    "ds_read_b64_tr_b16 a[138:139], v198 offset:36864\n\t"
    "v_cvt_pk_bf16_f32 v156, v136, v137\n\t"
    "v_mfma_f32_16x16x32_bf16 v[104:107], a[244:247], v[148:151], v[104:107]\n\t"
    "v_exp_f32 v138, v138\n\t"
    "v_mfma_f32_16x16x32_bf16 v[120:123], a[252:255], v[148:151], v[120:123]\n\t"
    "ds_read_b64_tr_b16 a[140:141], v198 offset:40960\n\t"
    "ds_read_b64_tr_b16 a[142:143], v198 offset:45056\n\t"
    "v_mfma_f32_16x16x32_bf16 v[120:123], a[248:251], v[144:147], v[120:123]\n\t"
    "v_exp_f32 v139, v139\n\t"
    "v_smfmac_f32_16x16x64_bf16 v[184:187], v[192:195], v[144:151], v245\n\t"
    "s_waitcnt lgkmcnt(8)\n\t"
    "v_cvt_pk_bf16_f32 v157, v138, v139\n\t"
    "s_add_u32 s66, s66, 1\n\t"
    "s_branch 4b\n\t"
    "5:\n\t"
    "s_cmp_eq_u32 s87, 0\n\t"
    "s_cbranch_scc1 TB_%=\n\t"
    "v_mfma_f32_16x16x32_bf16 v[128:131], a[64:67], a[0:3], v[160:163]\n\t"
    "ds_read_b64_tr_b16 a[208:209], v197 offset:16640\n\t"
    "ds_read_b64_tr_b16 a[210:211], v197 offset:20736\n\t"
    "v_mfma_f32_16x16x32_bf16 v[128:131], a[68:71], a[4:7], v[128:131]\n\t"
    "v_exp_f32 v140, v140\n\t"
    "v_mfma_f32_16x16x32_bf16 v[128:131], a[72:75], a[8:11], v[128:131]\n\t"
    "ds_read_b64_tr_b16 a[212:213], v197 offset:24832\n\t"
    "ds_read_b64_tr_b16 a[214:215], v197 offset:28928\n\t"
    "v_mfma_f32_16x16x32_bf16 v[128:131], a[76:79], a[12:15], v[128:131]\n\t"
    "v_exp_f32 v141, v141\n\t"
    "v_mfma_f32_16x16x32_bf16 v[132:135], a[92:95], a[12:15], v[160:163]\n\t"
    "ds_read_b64_tr_b16 a[216:217], v198 offset:16640\n\t"
    "v_cvt_pk_bf16_f32 v158, v140, v141\n\t"
    "v_mfma_f32_16x16x32_bf16 v[132:135], a[88:91], a[8:11], v[132:135]\n\t"
    "v_exp_f32 v142, v142\n\t"
    "v_mfma_f32_16x16x32_bf16 v[132:135], a[84:87], a[4:7], v[132:135]\n\t"
    "ds_read_b64_tr_b16 a[218:219], v198 offset:20736\n\t"
    "ds_read_b64_tr_b16 a[220:221], v198 offset:24832\n\t"
    "v_mfma_f32_16x16x32_bf16 v[132:135], a[80:83], a[0:3], v[132:135]\n\t"
    "v_exp_f32 v143, v143\n\t"
    "v_mfma_f32_16x16x32_bf16 v[136:139], a[96:99], a[0:3], v[160:163]\n\t"
    "ds_read_b64_tr_b16 a[222:223], v198 offset:28928\n\t"
    "v_cvt_pk_bf16_f32 v159, v142, v143\n\t"
    "v_mfma_f32_16x16x32_bf16 v[136:139], a[100:103], a[4:7], v[136:139]\n\t"
    "v_exp_f32 v128, v128\n\t"
    "v_mfma_f32_16x16x32_bf16 v[136:139], a[104:107], a[8:11], v[136:139]\n\t"
    "ds_read_b64_tr_b16 a[224:225], v197 offset:16896\n\t"
    "ds_read_b64_tr_b16 a[226:227], v197 offset:20992\n\t"
    "v_mfma_f32_16x16x32_bf16 v[136:139], a[108:111], a[12:15], v[136:139]\n\t"
    "v_exp_f32 v129, v129\n\t"
    "v_mfma_f32_16x16x32_bf16 v[140:143], a[124:127], a[12:15], v[160:163]\n\t"
    "ds_read_b64_tr_b16 a[228:229], v197 offset:25088\n\t"
    "v_cvt_pk_bf16_f32 v144, v128, v129\n\t"
    "v_mfma_f32_16x16x32_bf16 v[140:143], a[120:123], a[8:11], v[140:143]\n\t"
    "v_exp_f32 v130, v130\n\t"
    "v_mfma_f32_16x16x32_bf16 v[140:143], a[116:119], a[4:7], v[140:143]\n\t"
    "ds_read_b64_tr_b16 a[230:231], v197 offset:29184\n\t"
    "ds_read_b64_tr_b16 a[232:233], v198 offset:16896\n\t"
    "v_mfma_f32_16x16x32_bf16 v[140:143], a[112:115], a[0:3], v[140:143]\n\t"
    "v_exp_f32 v131, v131\n\t"
    "v_mfma_f32_16x16x32_bf16 v[12:15], a[128:131], v[152:155], v[12:15]\n\t"
    "ds_read_b64_tr_b16 a[234:235], v198 offset:20992\n\t"
    "v_cvt_pk_bf16_f32 v145, v130, v131\n\t"
    "v_mfma_f32_16x16x32_bf16 v[12:15], a[132:135], v[156:159], v[12:15]\n\t"
    "v_exp_f32 v132, v132\n\t"
    "v_mfma_f32_16x16x32_bf16 v[28:31], a[140:143], v[156:159], v[28:31]\n\t"
    "ds_read_b64_tr_b16 a[236:237], v198 offset:25088\n\t"
    "ds_read_b64_tr_b16 a[238:239], v198 offset:29184\n\t"
    "v_mfma_f32_16x16x32_bf16 v[28:31], a[136:139], v[152:155], v[28:31]\n\t"
    "v_exp_f32 v133, v133\n\t"
    "v_mfma_f32_16x16x32_bf16 v[44:47], a[144:147], v[152:155], v[44:47]\n\t"
    "ds_read_b64_tr_b16 a[240:241], v197 offset:17152\n\t"
    "v_cvt_pk_bf16_f32 v146, v132, v133\n\t"
    "v_mfma_f32_16x16x32_bf16 v[44:47], a[148:151], v[156:159], v[44:47]\n\t"
    "v_exp_f32 v134, v134\n\t"
    "v_mfma_f32_16x16x32_bf16 v[60:63], a[156:159], v[156:159], v[60:63]\n\t"
    "ds_read_b64_tr_b16 a[242:243], v197 offset:21248\n\t"
    "ds_read_b64_tr_b16 a[244:245], v197 offset:25344\n\t"
    "v_mfma_f32_16x16x32_bf16 v[60:63], a[152:155], v[152:155], v[60:63]\n\t"
    "v_exp_f32 v135, v135\n\t"
    "v_mfma_f32_16x16x32_bf16 v[76:79], a[160:163], v[152:155], v[76:79]\n\t"
    "ds_read_b64_tr_b16 a[246:247], v197 offset:29440\n\t"
    "v_cvt_pk_bf16_f32 v147, v134, v135\n\t"
    "v_mfma_f32_16x16x32_bf16 v[76:79], a[164:167], v[156:159], v[76:79]\n\t"
    "v_exp_f32 v136, v136\n\t"
    "v_mfma_f32_16x16x32_bf16 v[92:95], a[172:175], v[156:159], v[92:95]\n\t"
    "ds_read_b64_tr_b16 a[248:249], v198 offset:17152\n\t"
    "ds_read_b64_tr_b16 a[250:251], v198 offset:21248\n\t"
    "v_mfma_f32_16x16x32_bf16 v[92:95], a[168:171], v[152:155], v[92:95]\n\t"
    "v_exp_f32 v137, v137\n\t"
    "v_mfma_f32_16x16x32_bf16 v[108:111], a[176:179], v[152:155], v[108:111]\n\t"
    "ds_read_b64_tr_b16 a[252:253], v198 offset:25344\n\t"
    "v_cvt_pk_bf16_f32 v148, v136, v137\n\t"
    "v_mfma_f32_16x16x32_bf16 v[108:111], a[180:183], v[156:159], v[108:111]\n\t"
    "v_exp_f32 v138, v138\n\t"
    "v_mfma_f32_16x16x32_bf16 v[124:127], a[188:191], v[156:159], v[124:127]\n\t"
    "s_mov_b64 s[36:37], s[60:61]\n\t"
    "s_mov_b64 s[40:41], s[62:63]\n\t"
    "s_mov_b32 s38, s79\n\t"
    "s_mov_b32 s42, s79\n\t"
    "ds_read_b64_tr_b16 a[254:255], v198 offset:29440\n\t"
    "v_mfma_f32_16x16x32_bf16 v[124:127], a[184:187], v[152:155], v[124:127]\n\t"
    "v_exp_f32 v139, v139\n\t"
    "v_smfmac_f32_16x16x64_bf16 v[188:191], v[192:195], v[152:159], v245\n\t"
    "s_waitcnt vmcnt(8)\n\t"
    "s_barrier\n\t"
    "v_cvt_pk_bf16_f32 v149, v138, v139\n\t"
    "v_mfma_f32_16x16x32_bf16 v[128:131], a[64:67], a[16:19], v[164:167]\n\t"
    "ds_read_b128 a[64:67], v196 offset:32768\n\t"
    "v_mfma_f32_16x16x32_bf16 v[128:131], a[68:71], a[20:23], v[128:131]\n\t"
    "v_exp_f32 v140, v140\n\t"
    "v_mfma_f32_16x16x32_bf16 v[128:131], a[72:75], a[24:27], v[128:131]\n\t"
    "ds_read_b128 a[68:71], v196 offset:33024\n\t"
    "ds_read_b128 a[72:75], v196 offset:33280\n\t"
    "v_mfma_f32_16x16x32_bf16 v[128:131], a[76:79], a[28:31], v[128:131]\n\t"
    "v_exp_f32 v141, v141\n\t"
    "v_mfma_f32_16x16x32_bf16 v[132:135], a[92:95], a[28:31], v[164:167]\n\t"
    "ds_read_b128 a[76:79], v196 offset:33536\n\t"
    "v_cvt_pk_bf16_f32 v150, v140, v141\n\t"
    "v_mfma_f32_16x16x32_bf16 v[132:135], a[88:91], a[24:27], v[132:135]\n\t"
    "v_exp_f32 v142, v142\n\t"
    "v_mfma_f32_16x16x32_bf16 v[132:135], a[84:87], a[20:23], v[132:135]\n\t"
    "ds_read_b128 a[92:95], v196 offset:37632\n\t"
    "ds_read_b128 a[88:91], v196 offset:37376\n\t"
    "v_mfma_f32_16x16x32_bf16 v[132:135], a[80:83], a[16:19], v[132:135]\n\t"
    "v_exp_f32 v143, v143\n\t"
    "v_mfma_f32_16x16x32_bf16 v[136:139], a[96:99], a[16:19], v[164:167]\n\t"
    "ds_read_b128 a[84:87], v196 offset:37120\n\t"
    "v_cvt_pk_bf16_f32 v151, v142, v143\n\t"
    "v_mfma_f32_16x16x32_bf16 v[136:139], a[100:103], a[20:23], v[136:139]\n\t"
    "v_exp_f32 v128, v128\n\t"
    "v_mfma_f32_16x16x32_bf16 v[136:139], a[104:107], a[24:27], v[136:139]\n\t"
    "ds_read_b128 a[80:83], v196 offset:36864\n\t"
    "ds_read_b128 a[96:99], v196 offset:40960\n\t"
    "v_mfma_f32_16x16x32_bf16 v[136:139], a[108:111], a[28:31], v[136:139]\n\t"
    "v_exp_f32 v129, v129\n\t"
    "v_mfma_f32_16x16x32_bf16 v[140:143], a[124:127], a[28:31], v[164:167]\n\t"
    "ds_read_b128 a[100:103], v196 offset:41216\n\t"
    "v_cvt_pk_bf16_f32 v152, v128, v129\n\t"
    "v_mfma_f32_16x16x32_bf16 v[140:143], a[120:123], a[24:27], v[140:143]\n\t"
    "s_waitcnt lgkmcnt(10)\n\t"
    "v_exp_f32 v130, v130\n\t"
    "v_mfma_f32_16x16x32_bf16 v[140:143], a[116:119], a[20:23], v[140:143]\n\t"
    "ds_read_b128 a[104:107], v196 offset:41472\n\t"
    "ds_read_b128 a[108:111], v196 offset:41728\n\t"
    "v_mfma_f32_16x16x32_bf16 v[140:143], a[112:115], a[16:19], v[140:143]\n\t"
    "v_exp_f32 v131, v131\n\t"
    "v_mfma_f32_16x16x32_bf16 v[0:3], a[192:195], v[144:147], v[0:3]\n\t"
    "ds_read_b128 a[124:127], v196 offset:45824\n\t"
    "v_cvt_pk_bf16_f32 v153, v130, v131\n\t"
    "v_mfma_f32_16x16x32_bf16 v[0:3], a[196:199], v[148:151], v[0:3]\n\t"
    "v_exp_f32 v132, v132\n\t"
    "v_mfma_f32_16x16x32_bf16 v[16:19], a[204:207], v[148:151], v[16:19]\n\t"
    "ds_read_b128 a[120:123], v196 offset:45568\n\t"
    "ds_read_b128 a[116:119], v196 offset:45312\n\t"
    "v_mfma_f32_16x16x32_bf16 v[16:19], a[200:203], v[144:147], v[16:19]\n\t"
    "v_exp_f32 v133, v133\n\t"
    "v_mfma_f32_16x16x32_bf16 v[32:35], a[208:211], v[144:147], v[32:35]\n\t"
    "ds_read_b128 a[112:115], v196 offset:45056\n\t"
    "v_cvt_pk_bf16_f32 v154, v132, v133\n\t"
    "v_mfma_f32_16x16x32_bf16 v[32:35], a[212:215], v[148:151], v[32:35]\n\t"
    "v_exp_f32 v134, v134\n\t"
    "v_mfma_f32_16x16x32_bf16 v[48:51], a[220:223], v[148:151], v[48:51]\n\t"
    "ds_read_b64_tr_b16 a[128:129], v197 offset:32768\n\t"
    "ds_read_b64_tr_b16 a[130:131], v197 offset:36864\n\t"
    "v_mfma_f32_16x16x32_bf16 v[48:51], a[216:219], v[144:147], v[48:51]\n\t"
    "v_exp_f32 v135, v135\n\t"
    "v_mfma_f32_16x16x32_bf16 v[64:67], a[224:227], v[144:147], v[64:67]\n\t"
    "ds_read_b64_tr_b16 a[132:133], v197 offset:40960\n\t"
    "v_cvt_pk_bf16_f32 v155, v134, v135\n\t"
    "v_mfma_f32_16x16x32_bf16 v[64:67], a[228:231], v[148:151], v[64:67]\n\t"
    "v_exp_f32 v136, v136\n\t"
    "v_mfma_f32_16x16x32_bf16 v[80:83], a[236:239], v[148:151], v[80:83]\n\t"
    "ds_read_b64_tr_b16 a[134:135], v197 offset:45056\n\t"
    "ds_read_b64_tr_b16 a[136:137], v198 offset:32768\n\t"
    "v_mfma_f32_16x16x32_bf16 v[80:83], a[232:235], v[144:147], v[80:83]\n\t"
    "v_exp_f32 v137, v137\n\t"
    "v_mfma_f32_16x16x32_bf16 v[96:99], a[240:243], v[144:147], v[96:99]\n\t"
    "ds_read_b64_tr_b16 a[138:139], v198 offset:36864\n\t"
    "v_cvt_pk_bf16_f32 v156, v136, v137\n\t"
    "v_mfma_f32_16x16x32_bf16 v[96:99], a[244:247], v[148:151], v[96:99]\n\t"
    "v_exp_f32 v138, v138\n\t"
    "v_mfma_f32_16x16x32_bf16 v[112:115], a[252:255], v[148:151], v[112:115]\n\t"
    "ds_read_b64_tr_b16 a[140:141], v198 offset:40960\n\t"
    "ds_read_b64_tr_b16 a[142:143], v198 offset:45056\n\t"
    "v_mfma_f32_16x16x32_bf16 v[112:115], a[248:251], v[144:147], v[112:115]\n\t"
    "v_exp_f32 v139, v139\n\t"
    "v_smfmac_f32_16x16x64_bf16 v[176:179], v[192:195], v[144:151], v245\n\t"
    "s_waitcnt lgkmcnt(8)\n\t"
    "v_cvt_pk_bf16_f32 v157, v138, v139\n\t"
    "v_mfma_f32_16x16x32_bf16 v[128:131], a[64:67], a[0:3], v[160:163]\n\t"
    "ds_read_b64_tr_b16 a[144:145], v197 offset:33024\n\t"
    "ds_read_b64_tr_b16 a[146:147], v197 offset:37120\n\t"
    "v_mfma_f32_16x16x32_bf16 v[128:131], a[68:71], a[4:7], v[128:131]\n\t"
    "s_add_u32 m0, s64, 0\n\t"
    "v_exp_f32 v140, v140\n\t"
    "v_mfma_f32_16x16x32_bf16 v[128:131], a[72:75], a[8:11], v[128:131]\n\t"
    "buffer_load_dwordx4 v199, s[36:39], s64 offen offset:0 nt lds\n\t"
    "ds_read_b64_tr_b16 a[148:149], v197 offset:41216\n\t"
    "v_mfma_f32_16x16x32_bf16 v[128:131], a[76:79], a[12:15], v[128:131]\n\t"
    "v_exp_f32 v141, v141\n\t"
    "v_mfma_f32_16x16x32_bf16 v[132:135], a[92:95], a[12:15], v[160:163]\n\t"
    "ds_read_b64_tr_b16 a[150:151], v197 offset:45312\n\t"
    "v_cvt_pk_bf16_f32 v158, v140, v141\n\t"
    "v_mfma_f32_16x16x32_bf16 v[132:135], a[88:91], a[8:11], v[132:135]\n\t"
    "v_exp_f32 v142, v142\n\t"
    "v_mfma_f32_16x16x32_bf16 v[132:135], a[84:87], a[4:7], v[132:135]\n\t"
    "ds_read_b64_tr_b16 a[152:153], v198 offset:33024\n\t"
    "ds_read_b64_tr_b16 a[154:155], v198 offset:37120\n\t"
    "v_mfma_f32_16x16x32_bf16 v[132:135], a[80:83], a[0:3], v[132:135]\n\t"
    "v_exp_f32 v143, v143\n\t"
    "v_mfma_f32_16x16x32_bf16 v[136:139], a[96:99], a[0:3], v[160:163]\n\t"
    "ds_read_b64_tr_b16 a[156:157], v198 offset:41216\n\t"
    "v_cvt_pk_bf16_f32 v159, v142, v143\n\t"
    "v_mfma_f32_16x16x32_bf16 v[136:139], a[100:103], a[4:7], v[136:139]\n\t"
    "v_exp_f32 v128, v128\n\t"
    "v_mfma_f32_16x16x32_bf16 v[136:139], a[104:107], a[8:11], v[136:139]\n\t"
    "buffer_load_dwordx4 v200, s[36:39], s64 offen offset:1024 nt lds\n\t"
    "ds_read_b64_tr_b16 a[158:159], v198 offset:45312\n\t"
    "v_mfma_f32_16x16x32_bf16 v[136:139], a[108:111], a[12:15], v[136:139]\n\t"
    "v_exp_f32 v129, v129\n\t"
    "v_mfma_f32_16x16x32_bf16 v[140:143], a[124:127], a[12:15], v[160:163]\n\t"
    "ds_read_b64_tr_b16 a[160:161], v197 offset:33280\n\t"
    "v_cvt_pk_bf16_f32 v144, v128, v129\n\t"
    "v_mfma_f32_16x16x32_bf16 v[140:143], a[120:123], a[8:11], v[140:143]\n\t"
    "v_exp_f32 v130, v130\n\t"
    "v_mfma_f32_16x16x32_bf16 v[140:143], a[116:119], a[4:7], v[140:143]\n\t"
    "ds_read_b64_tr_b16 a[162:163], v197 offset:37376\n\t"
    "ds_read_b64_tr_b16 a[164:165], v197 offset:41472\n\t"
    "v_mfma_f32_16x16x32_bf16 v[140:143], a[112:115], a[0:3], v[140:143]\n\t"
    "v_exp_f32 v131, v131\n\t"
    "v_mfma_f32_16x16x32_bf16 v[4:7], a[192:195], v[152:155], v[4:7]\n\t"
    "ds_read_b64_tr_b16 a[166:167], v197 offset:45568\n\t"
    "v_cvt_pk_bf16_f32 v145, v130, v131\n\t"
    "v_mfma_f32_16x16x32_bf16 v[4:7], a[196:199], v[156:159], v[4:7]\n\t"
    "v_exp_f32 v132, v132\n\t"
    "v_mfma_f32_16x16x32_bf16 v[20:23], a[204:207], v[156:159], v[20:23]\n\t"
    "buffer_load_dwordx4 v201, s[36:39], s64 offen offset:2048 nt lds\n\t"
    "ds_read_b64_tr_b16 a[168:169], v198 offset:33280\n\t"
    "v_mfma_f32_16x16x32_bf16 v[20:23], a[200:203], v[152:155], v[20:23]\n\t"
    "v_exp_f32 v133, v133\n\t"
    "v_mfma_f32_16x16x32_bf16 v[36:39], a[208:211], v[152:155], v[36:39]\n\t"
    "ds_read_b64_tr_b16 a[170:171], v198 offset:37376\n\t"
    "v_cvt_pk_bf16_f32 v146, v132, v133\n\t"
    "v_mfma_f32_16x16x32_bf16 v[36:39], a[212:215], v[156:159], v[36:39]\n\t"
    "v_exp_f32 v134, v134\n\t"
    "v_mfma_f32_16x16x32_bf16 v[52:55], a[220:223], v[156:159], v[52:55]\n\t"
    "ds_read_b64_tr_b16 a[172:173], v198 offset:41472\n\t"
    "ds_read_b64_tr_b16 a[174:175], v198 offset:45568\n\t"
    "v_mfma_f32_16x16x32_bf16 v[52:55], a[216:219], v[152:155], v[52:55]\n\t"
    "v_exp_f32 v135, v135\n\t"
    "v_mfma_f32_16x16x32_bf16 v[68:71], a[224:227], v[152:155], v[68:71]\n\t"
    "ds_read_b64_tr_b16 a[176:177], v197 offset:33536\n\t"
    "v_cvt_pk_bf16_f32 v147, v134, v135\n\t"
    "v_mfma_f32_16x16x32_bf16 v[68:71], a[228:231], v[156:159], v[68:71]\n\t"
    "v_exp_f32 v136, v136\n\t"
    "v_mfma_f32_16x16x32_bf16 v[84:87], a[236:239], v[156:159], v[84:87]\n\t"
    "buffer_load_dwordx4 v202, s[36:39], s64 offen offset:3072 nt lds\n\t"
    "ds_read_b64_tr_b16 a[178:179], v197 offset:37632\n\t"
    "v_mfma_f32_16x16x32_bf16 v[84:87], a[232:235], v[152:155], v[84:87]\n\t"
    "v_exp_f32 v137, v137\n\t"
    "v_mfma_f32_16x16x32_bf16 v[100:103], a[240:243], v[152:155], v[100:103]\n\t"
    "ds_read_b64_tr_b16 a[180:181], v197 offset:41728\n\t"
    "v_cvt_pk_bf16_f32 v148, v136, v137\n\t"
    "v_mfma_f32_16x16x32_bf16 v[100:103], a[244:247], v[156:159], v[100:103]\n\t"
    "v_exp_f32 v138, v138\n\t"
    "v_mfma_f32_16x16x32_bf16 v[116:119], a[252:255], v[156:159], v[116:119]\n\t"
    "ds_read_b64_tr_b16 a[182:183], v197 offset:45824\n\t"
    "ds_read_b64_tr_b16 a[184:185], v198 offset:33536\n\t"
    "ds_read_b64_tr_b16 a[188:189], v198 offset:41728\n\t"
    "ds_read_b64_tr_b16 a[190:191], v198 offset:45824\n\t"
    "v_mfma_f32_16x16x32_bf16 v[116:119], a[248:251], v[152:155], v[116:119]\n\t"
    "v_exp_f32 v139, v139\n\t"
    "v_smfmac_f32_16x16x64_bf16 v[180:183], v[192:195], v[152:159], v245\n\t"
    "ds_read_b64_tr_b16 a[186:187], v198 offset:37632\n\t"
    "s_waitcnt vmcnt(4)\n\t"
    "s_barrier\n\t"
    "v_cvt_pk_bf16_f32 v149, v138, v139\n\t"
    "v_mfma_f32_16x16x32_bf16 v[128:131], a[64:67], a[16:19], v[164:167]\n\t"
    "ds_read_b128 a[64:67], v196 offset:49152\n\t"
    "v_mfma_f32_16x16x32_bf16 v[128:131], a[68:71], a[20:23], v[128:131]\n\t"
    "v_exp_f32 v140, v140\n\t"
    "v_mfma_f32_16x16x32_bf16 v[128:131], a[72:75], a[24:27], v[128:131]\n\t"
    "ds_read_b128 a[68:71], v196 offset:49408\n\t"
    "ds_read_b128 a[72:75], v196 offset:49664\n\t"
    "v_mfma_f32_16x16x32_bf16 v[128:131], a[76:79], a[28:31], v[128:131]\n\t"
    "v_exp_f32 v141, v141\n\t"
    "v_mfma_f32_16x16x32_bf16 v[132:135], a[92:95], a[28:31], v[164:167]\n\t"
    "ds_read_b128 a[76:79], v196 offset:49920\n\t"
    "v_cvt_pk_bf16_f32 v150, v140, v141\n\t"
    "v_mfma_f32_16x16x32_bf16 v[132:135], a[88:91], a[24:27], v[132:135]\n\t"
    "v_exp_f32 v142, v142\n\t"
    "v_mfma_f32_16x16x32_bf16 v[132:135], a[84:87], a[20:23], v[132:135]\n\t"
    "ds_read_b128 a[92:95], v196 offset:54016\n\t"
    "ds_read_b128 a[88:91], v196 offset:53760\n\t"
    "v_mfma_f32_16x16x32_bf16 v[132:135], a[80:83], a[16:19], v[132:135]\n\t"
    "v_exp_f32 v143, v143\n\t"
    "v_mfma_f32_16x16x32_bf16 v[136:139], a[96:99], a[16:19], v[164:167]\n\t"
    "ds_read_b128 a[84:87], v196 offset:53504\n\t"
    "v_cvt_pk_bf16_f32 v151, v142, v143\n\t"
    "v_mfma_f32_16x16x32_bf16 v[136:139], a[100:103], a[20:23], v[136:139]\n\t"
    "v_exp_f32 v128, v128\n\t"
    "v_mfma_f32_16x16x32_bf16 v[136:139], a[104:107], a[24:27], v[136:139]\n\t"
    "ds_read_b128 a[80:83], v196 offset:53248\n\t"
    "ds_read_b128 a[96:99], v196 offset:57344\n\t"
    "v_mfma_f32_16x16x32_bf16 v[136:139], a[108:111], a[28:31], v[136:139]\n\t"
    "v_exp_f32 v129, v129\n\t"
    "v_mfma_f32_16x16x32_bf16 v[140:143], a[124:127], a[28:31], v[164:167]\n\t"
    "ds_read_b128 a[100:103], v196 offset:57600\n\t"
    "v_cvt_pk_bf16_f32 v152, v128, v129\n\t"
    "v_mfma_f32_16x16x32_bf16 v[140:143], a[120:123], a[24:27], v[140:143]\n\t"
    "s_waitcnt lgkmcnt(10)\n\t"
    "v_exp_f32 v130, v130\n\t"
    "v_mfma_f32_16x16x32_bf16 v[140:143], a[116:119], a[20:23], v[140:143]\n\t"
    "ds_read_b128 a[104:107], v196 offset:57856\n\t"
    "ds_read_b128 a[108:111], v196 offset:58112\n\t"
    "v_mfma_f32_16x16x32_bf16 v[140:143], a[112:115], a[16:19], v[140:143]\n\t"
    "v_exp_f32 v131, v131\n\t"
    "v_mfma_f32_16x16x32_bf16 v[0:3], a[128:131], v[144:147], v[0:3]\n\t"
    "ds_read_b128 a[124:127], v196 offset:62208\n\t"
    "v_cvt_pk_bf16_f32 v153, v130, v131\n\t"
    "v_mfma_f32_16x16x32_bf16 v[0:3], a[132:135], v[148:151], v[0:3]\n\t"
    "v_exp_f32 v132, v132\n\t"
    "v_mfma_f32_16x16x32_bf16 v[16:19], a[140:143], v[148:151], v[16:19]\n\t"
    "ds_read_b128 a[120:123], v196 offset:61952\n\t"
    "ds_read_b128 a[116:119], v196 offset:61696\n\t"
    "v_mfma_f32_16x16x32_bf16 v[16:19], a[136:139], v[144:147], v[16:19]\n\t"
    "v_exp_f32 v133, v133\n\t"
    "v_mfma_f32_16x16x32_bf16 v[32:35], a[144:147], v[144:147], v[32:35]\n\t"
    "ds_read_b128 a[112:115], v196 offset:61440\n\t"
    "v_cvt_pk_bf16_f32 v154, v132, v133\n\t"
    "v_mfma_f32_16x16x32_bf16 v[32:35], a[148:151], v[148:151], v[32:35]\n\t"
    "v_exp_f32 v134, v134\n\t"
    "v_mfma_f32_16x16x32_bf16 v[48:51], a[156:159], v[148:151], v[48:51]\n\t"
    "ds_read_b64_tr_b16 a[192:193], v197 offset:49152\n\t"
    "ds_read_b64_tr_b16 a[194:195], v197 offset:53248\n\t"
    "v_mfma_f32_16x16x32_bf16 v[48:51], a[152:155], v[144:147], v[48:51]\n\t"
    "v_exp_f32 v135, v135\n\t"
    "v_mfma_f32_16x16x32_bf16 v[64:67], a[160:163], v[144:147], v[64:67]\n\t"
    "ds_read_b64_tr_b16 a[196:197], v197 offset:57344\n\t"
    "v_cvt_pk_bf16_f32 v155, v134, v135\n\t"
    "v_mfma_f32_16x16x32_bf16 v[64:67], a[164:167], v[148:151], v[64:67]\n\t"
    "v_exp_f32 v136, v136\n\t"
    "v_mfma_f32_16x16x32_bf16 v[80:83], a[172:175], v[148:151], v[80:83]\n\t"
    "ds_read_b64_tr_b16 a[198:199], v197 offset:61440\n\t"
    "ds_read_b64_tr_b16 a[200:201], v198 offset:49152\n\t"
    "v_mfma_f32_16x16x32_bf16 v[80:83], a[168:171], v[144:147], v[80:83]\n\t"
    "v_exp_f32 v137, v137\n\t"
    "v_mfma_f32_16x16x32_bf16 v[96:99], a[176:179], v[144:147], v[96:99]\n\t"
    "ds_read_b64_tr_b16 a[202:203], v198 offset:53248\n\t"
    "v_cvt_pk_bf16_f32 v156, v136, v137\n\t"
    "v_mfma_f32_16x16x32_bf16 v[96:99], a[180:183], v[148:151], v[96:99]\n\t"
    "v_exp_f32 v138, v138\n\t"
    "v_mfma_f32_16x16x32_bf16 v[112:115], a[188:191], v[148:151], v[112:115]\n\t"
    "ds_read_b64_tr_b16 a[204:205], v198 offset:57344\n\t"
    "ds_read_b64_tr_b16 a[206:207], v198 offset:61440\n\t"
    "v_mfma_f32_16x16x32_bf16 v[112:115], a[184:187], v[144:147], v[112:115]\n\t"
    "v_exp_f32 v139, v139\n\t"
    "v_smfmac_f32_16x16x64_bf16 v[176:179], v[192:195], v[144:151], v245\n\t"
    "s_waitcnt lgkmcnt(8)\n\t"
    "v_cvt_pk_bf16_f32 v157, v138, v139\n\t"
    "v_mfma_f32_16x16x32_bf16 v[128:131], a[64:67], a[0:3], v[160:163]\n\t"
    "ds_read_b64_tr_b16 a[208:209], v197 offset:49408\n\t"
    "ds_read_b64_tr_b16 a[210:211], v197 offset:53504\n\t"
    "v_mfma_f32_16x16x32_bf16 v[128:131], a[68:71], a[4:7], v[128:131]\n\t"
    "s_add_u32 m0, s64, 65536\n\t"
    "v_exp_f32 v140, v140\n\t"
    "v_mfma_f32_16x16x32_bf16 v[128:131], a[72:75], a[8:11], v[128:131]\n\t"
    "buffer_load_dwordx4 v199, s[40:43], s64 offen offset:0 nt lds\n\t"
    "ds_read_b64_tr_b16 a[212:213], v197 offset:57600\n\t"
    "v_mfma_f32_16x16x32_bf16 v[128:131], a[76:79], a[12:15], v[128:131]\n\t"
    "v_exp_f32 v141, v141\n\t"
    "v_mfma_f32_16x16x32_bf16 v[132:135], a[92:95], a[12:15], v[160:163]\n\t"
    "ds_read_b64_tr_b16 a[214:215], v197 offset:61696\n\t"
    "v_cvt_pk_bf16_f32 v158, v140, v141\n\t"
    "v_mfma_f32_16x16x32_bf16 v[132:135], a[88:91], a[8:11], v[132:135]\n\t"
    "v_exp_f32 v142, v142\n\t"
    "v_mfma_f32_16x16x32_bf16 v[132:135], a[84:87], a[4:7], v[132:135]\n\t"
    "ds_read_b64_tr_b16 a[216:217], v198 offset:49408\n\t"
    "ds_read_b64_tr_b16 a[218:219], v198 offset:53504\n\t"
    "v_mfma_f32_16x16x32_bf16 v[132:135], a[80:83], a[0:3], v[132:135]\n\t"
    "v_exp_f32 v143, v143\n\t"
    "v_mfma_f32_16x16x32_bf16 v[136:139], a[96:99], a[0:3], v[160:163]\n\t"
    "ds_read_b64_tr_b16 a[220:221], v198 offset:57600\n\t"
    "v_cvt_pk_bf16_f32 v159, v142, v143\n\t"
    "v_mfma_f32_16x16x32_bf16 v[136:139], a[100:103], a[4:7], v[136:139]\n\t"
    "v_exp_f32 v128, v128\n\t"
    "v_mfma_f32_16x16x32_bf16 v[136:139], a[104:107], a[8:11], v[136:139]\n\t"
    "buffer_load_dwordx4 v200, s[40:43], s64 offen offset:1024 nt lds\n\t"
    "ds_read_b64_tr_b16 a[222:223], v198 offset:61696\n\t"
    "v_mfma_f32_16x16x32_bf16 v[136:139], a[108:111], a[12:15], v[136:139]\n\t"
    "v_exp_f32 v129, v129\n\t"
    "v_mfma_f32_16x16x32_bf16 v[140:143], a[124:127], a[12:15], v[160:163]\n\t"
    "ds_read_b64_tr_b16 a[224:225], v197 offset:49664\n\t"
    "v_cvt_pk_bf16_f32 v144, v128, v129\n\t"
    "v_mfma_f32_16x16x32_bf16 v[140:143], a[120:123], a[8:11], v[140:143]\n\t"
    "v_exp_f32 v130, v130\n\t"
    "v_mfma_f32_16x16x32_bf16 v[140:143], a[116:119], a[4:7], v[140:143]\n\t"
    "ds_read_b64_tr_b16 a[226:227], v197 offset:53760\n\t"
    "ds_read_b64_tr_b16 a[228:229], v197 offset:57856\n\t"
    "v_mfma_f32_16x16x32_bf16 v[140:143], a[112:115], a[0:3], v[140:143]\n\t"
    "v_exp_f32 v131, v131\n\t"
    "v_mfma_f32_16x16x32_bf16 v[4:7], a[128:131], v[152:155], v[4:7]\n\t"
    "ds_read_b64_tr_b16 a[230:231], v197 offset:61952\n\t"
    "v_cvt_pk_bf16_f32 v145, v130, v131\n\t"
    "v_mfma_f32_16x16x32_bf16 v[4:7], a[132:135], v[156:159], v[4:7]\n\t"
    "v_exp_f32 v132, v132\n\t"
    "v_mfma_f32_16x16x32_bf16 v[20:23], a[140:143], v[156:159], v[20:23]\n\t"
    "buffer_load_dwordx4 v201, s[40:43], s64 offen offset:2048 nt lds\n\t"
    "ds_read_b64_tr_b16 a[232:233], v198 offset:49664\n\t"
    "v_mfma_f32_16x16x32_bf16 v[20:23], a[136:139], v[152:155], v[20:23]\n\t"
    "v_exp_f32 v133, v133\n\t"
    "v_mfma_f32_16x16x32_bf16 v[36:39], a[144:147], v[152:155], v[36:39]\n\t"
    "ds_read_b64_tr_b16 a[234:235], v198 offset:53760\n\t"
    "v_cvt_pk_bf16_f32 v146, v132, v133\n\t"
    "v_mfma_f32_16x16x32_bf16 v[36:39], a[148:151], v[156:159], v[36:39]\n\t"
    "v_exp_f32 v134, v134\n\t"
    "v_mfma_f32_16x16x32_bf16 v[52:55], a[156:159], v[156:159], v[52:55]\n\t"
    "ds_read_b64_tr_b16 a[236:237], v198 offset:57856\n\t"
    "ds_read_b64_tr_b16 a[238:239], v198 offset:61952\n\t"
    "v_mfma_f32_16x16x32_bf16 v[52:55], a[152:155], v[152:155], v[52:55]\n\t"
    "v_exp_f32 v135, v135\n\t"
    "v_mfma_f32_16x16x32_bf16 v[68:71], a[160:163], v[152:155], v[68:71]\n\t"
    "ds_read_b64_tr_b16 a[240:241], v197 offset:49920\n\t"
    "v_cvt_pk_bf16_f32 v147, v134, v135\n\t"
    "v_mfma_f32_16x16x32_bf16 v[68:71], a[164:167], v[156:159], v[68:71]\n\t"
    "v_exp_f32 v136, v136\n\t"
    "v_mfma_f32_16x16x32_bf16 v[84:87], a[172:175], v[156:159], v[84:87]\n\t"
    "buffer_load_dwordx4 v202, s[40:43], s64 offen offset:3072 nt lds\n\t"
    "ds_read_b64_tr_b16 a[242:243], v197 offset:54016\n\t"
    "v_mfma_f32_16x16x32_bf16 v[84:87], a[168:171], v[152:155], v[84:87]\n\t"
    "v_exp_f32 v137, v137\n\t"
    "v_mfma_f32_16x16x32_bf16 v[100:103], a[176:179], v[152:155], v[100:103]\n\t"
    "ds_read_b64_tr_b16 a[244:245], v197 offset:58112\n\t"
    "v_cvt_pk_bf16_f32 v148, v136, v137\n\t"
    "v_mfma_f32_16x16x32_bf16 v[100:103], a[180:183], v[156:159], v[100:103]\n\t"
    "v_exp_f32 v138, v138\n\t"
    "v_mfma_f32_16x16x32_bf16 v[116:119], a[188:191], v[156:159], v[116:119]\n\t"
    "ds_read_b64_tr_b16 a[246:247], v197 offset:62208\n\t"
    "ds_read_b64_tr_b16 a[248:249], v198 offset:49920\n\t"
    "ds_read_b64_tr_b16 a[252:253], v198 offset:58112\n\t"
    "ds_read_b64_tr_b16 a[254:255], v198 offset:62208\n\t"
    "v_mfma_f32_16x16x32_bf16 v[116:119], a[184:187], v[152:155], v[116:119]\n\t"
    "s_mov_b64 s[44:45], s[48:49]\n\t"
    "s_cmp_eq_u32 s91, 0\n\t"
    "s_cselect_b32 s46, s50, 0\n\t"
    "s_add_u32 s70, s75, 4096\n\t"
    "s_add_u32 s69, s75, 8192\n\t"
    "v_exp_f32 v139, v139\n\t"
    "v_smfmac_f32_16x16x64_bf16 v[180:183], v[192:195], v[152:159], v245\n\t"
    "ds_read_b64_tr_b16 a[250:251], v198 offset:54016\n\t"
    "v_cvt_pk_bf16_f32 v149, v138, v139\n\t"
    "v_mfma_f32_16x16x32_bf16 v[128:131], a[64:67], a[16:19], v[164:167]\n\t"
    "v_lshlrev_b32 v204, 16, v220\n\t"
    "v_and_b32 v205, 0xffff0000, v220\n\t"
    "v_mfma_f32_16x16x32_bf16 v[128:131], a[68:71], a[20:23], v[128:131]\n\t"
    "v_exp_f32 v140, v140\n\t"
    "v_mfma_f32_16x16x32_bf16 v[128:131], a[72:75], a[24:27], v[128:131]\n\t"
    "v_lshlrev_b32 v206, 16, v221\n\t"
    "v_and_b32 v207, 0xffff0000, v221\n\t"
    "v_mfma_f32_16x16x32_bf16 v[128:131], a[76:79], a[28:31], v[128:131]\n\t"
    "v_exp_f32 v141, v141\n\t"
    "v_mfma_f32_16x16x32_bf16 v[132:135], a[92:95], a[28:31], v[164:167]\n\t"
    "v_cvt_pk_bf16_f32 v150, v140, v141\n\t"
    "v_lshlrev_b32 v208, 16, v222\n\t"
    "v_mfma_f32_16x16x32_bf16 v[132:135], a[88:91], a[24:27], v[132:135]\n\t"
    "v_exp_f32 v142, v142\n\t"
    "v_mfma_f32_16x16x32_bf16 v[132:135], a[84:87], a[20:23], v[132:135]\n\t"
    "v_and_b32 v209, 0xffff0000, v222\n\t"
    "v_lshlrev_b32 v210, 16, v223\n\t"
    "v_mfma_f32_16x16x32_bf16 v[132:135], a[80:83], a[16:19], v[132:135]\n\t"
    "v_exp_f32 v143, v143\n\t"
    "v_mfma_f32_16x16x32_bf16 v[136:139], a[96:99], a[16:19], v[164:167]\n\t"
    "v_cvt_pk_bf16_f32 v151, v142, v143\n\t"
    "v_and_b32 v211, 0xffff0000, v223\n\t"
    "v_mfma_f32_16x16x32_bf16 v[136:139], a[100:103], a[20:23], v[136:139]\n\t"
    "v_exp_f32 v128, v128\n\t"
    "v_mfma_f32_16x16x32_bf16 v[136:139], a[104:107], a[24:27], v[136:139]\n\t"
    "v_lshlrev_b32 v212, 16, v224\n\t"
    "v_and_b32 v213, 0xffff0000, v224\n\t"
    "v_mfma_f32_16x16x32_bf16 v[136:139], a[108:111], a[28:31], v[136:139]\n\t"
    "v_exp_f32 v129, v129\n\t"
    "v_mfma_f32_16x16x32_bf16 v[140:143], a[124:127], a[28:31], v[164:167]\n\t"
    "v_cvt_pk_bf16_f32 v152, v128, v129\n\t"
    "v_lshlrev_b32 v214, 16, v225\n\t"
    "v_mfma_f32_16x16x32_bf16 v[140:143], a[120:123], a[24:27], v[140:143]\n\t"
    "s_waitcnt lgkmcnt(0)\n\t"
    "v_exp_f32 v130, v130\n\t"
    "v_mfma_f32_16x16x32_bf16 v[140:143], a[116:119], a[20:23], v[140:143]\n\t"
    "v_and_b32 v215, 0xffff0000, v225\n\t"
    "v_lshlrev_b32 v216, 16, v226\n\t"
    "v_mfma_f32_16x16x32_bf16 v[140:143], a[112:115], a[16:19], v[140:143]\n\t"
    "v_exp_f32 v131, v131\n\t"
    "v_mfma_f32_16x16x32_bf16 v[0:3], a[192:195], v[144:147], v[0:3]\n\t"
    "v_cvt_pk_bf16_f32 v153, v130, v131\n\t"
    "v_and_b32 v217, 0xffff0000, v226\n\t"
    "v_mfma_f32_16x16x32_bf16 v[0:3], a[196:199], v[148:151], v[0:3]\n\t"
    "v_exp_f32 v132, v132\n\t"
    "v_mfma_f32_16x16x32_bf16 v[16:19], a[204:207], v[148:151], v[16:19]\n\t"
    "v_lshlrev_b32 v218, 16, v227\n\t"
    "v_and_b32 v219, 0xffff0000, v227\n\t"
    "v_mfma_f32_16x16x32_bf16 v[16:19], a[200:203], v[144:147], v[16:19]\n\t"
    "v_exp_f32 v133, v133\n\t"
    "v_mfma_f32_16x16x32_bf16 v[32:35], a[208:211], v[144:147], v[32:35]\n\t"
    "v_cvt_pk_bf16_f32 v154, v132, v133\n\t"
    "v_fma_f32 v204, -v204, s74, s74\n\t"
    "v_mfma_f32_16x16x32_bf16 v[32:35], a[212:215], v[148:151], v[32:35]\n\t"
    "v_exp_f32 v134, v134\n\t"
    "v_mfma_f32_16x16x32_bf16 v[48:51], a[220:223], v[148:151], v[48:51]\n\t"
    "v_fma_f32 v205, -v205, s74, s74\n\t"
    "v_fma_f32 v206, -v206, s74, s74\n\t"
    "v_mfma_f32_16x16x32_bf16 v[48:51], a[216:219], v[144:147], v[48:51]\n\t"
    "v_exp_f32 v135, v135\n\t"
    "v_mfma_f32_16x16x32_bf16 v[64:67], a[224:227], v[144:147], v[64:67]\n\t"
    "v_cvt_pk_bf16_f32 v155, v134, v135\n\t"
    "v_fma_f32 v207, -v207, s74, s74\n\t"
    "v_mfma_f32_16x16x32_bf16 v[64:67], a[228:231], v[148:151], v[64:67]\n\t"
    "v_exp_f32 v136, v136\n\t"
    "v_mfma_f32_16x16x32_bf16 v[80:83], a[236:239], v[148:151], v[80:83]\n\t"
    "v_fma_f32 v208, -v208, s74, s74\n\t"
    "v_fma_f32 v209, -v209, s74, s74\n\t"
    "v_mfma_f32_16x16x32_bf16 v[80:83], a[232:235], v[144:147], v[80:83]\n\t"
    "v_exp_f32 v137, v137\n\t"
    "v_mfma_f32_16x16x32_bf16 v[96:99], a[240:243], v[144:147], v[96:99]\n\t"
    "v_cvt_pk_bf16_f32 v156, v136, v137\n\t"
    "v_fma_f32 v210, -v210, s74, s74\n\t"
    "v_mfma_f32_16x16x32_bf16 v[96:99], a[244:247], v[148:151], v[96:99]\n\t"
    "v_exp_f32 v138, v138\n\t"
    "v_mfma_f32_16x16x32_bf16 v[112:115], a[252:255], v[148:151], v[112:115]\n\t"
    "v_fma_f32 v211, -v211, s74, s74\n\t"
    "v_fma_f32 v212, -v212, s74, s74\n\t"
    "v_mfma_f32_16x16x32_bf16 v[112:115], a[248:251], v[144:147], v[112:115]\n\t"
    "v_exp_f32 v139, v139\n\t"
    "v_smfmac_f32_16x16x64_bf16 v[176:179], v[192:195], v[144:151], v245\n\t"
    "v_cvt_pk_bf16_f32 v157, v138, v139\n\t"
    "v_fma_f32 v213, -v213, s74, s74\n\t"
    "s_mov_b64 exec, s[92:93]\n\tv_mov_b32 v254, v252\n\tv_mov_b32 v255, v253\n\ts_mov_b64 exec, -1\n\ts_nop 3\n\t"
    "v_mfma_f32_16x16x32_bf16 v[128:131], a[64:67], a[32:35], v[168:171]\n\t"
    "v_mfma_f32_16x16x32_bf16 v[128:131], a[68:71], a[36:39], v[128:131]\n\t"
    "v_exp_f32 v140, v140\n\t"
    "v_mfma_f32_16x16x32_bf16 v[128:131], a[72:75], a[40:43], v[128:131]\n\t"
    "v_mfma_f32_16x16x32_bf16 v[128:131], a[76:79], a[44:47], v[128:131]\n\t"
    "v_exp_f32 v141, v141\n\t"
    "v_mfma_f32_16x16x32_bf16 v[132:135], a[92:95], a[44:47], v[168:171]\n\t"
    "v_cvt_pk_bf16_f32 v158, v140, v141\n\t"
    "v_cvt_pk_bf16_f32 v0, v0, v1\n\t"
    "v_mfma_f32_16x16x32_bf16 v[132:135], a[88:91], a[40:43], v[132:135]\n\t"
    "v_exp_f32 v142, v142\n\t"
    "v_mfma_f32_16x16x32_bf16 v[132:135], a[84:87], a[36:39], v[132:135]\n\t"
    "v_cvt_pk_bf16_f32 v1, v2, v3\n\t"
    "v_cvt_pk_bf16_f32 v2, v16, v17\n\t"
    "v_mfma_f32_16x16x32_bf16 v[132:135], a[80:83], a[32:35], v[132:135]\n\t"
    "v_exp_f32 v143, v143\n\t"
    "v_mfma_f32_16x16x32_bf16 v[136:139], a[96:99], a[32:35], v[168:171]\n\t"
    "v_cvt_pk_bf16_f32 v159, v142, v143\n\t"
    "v_cvt_pk_bf16_f32 v3, v18, v19\n\t"
    "v_mfma_f32_16x16x32_bf16 v[136:139], a[100:103], a[36:39], v[136:139]\n\t"
    "v_exp_f32 v128, v128\n\t"
    "v_mfma_f32_16x16x32_bf16 v[136:139], a[104:107], a[40:43], v[136:139]\n\t"
    "buffer_store_dwordx4 v[0:3], v248, s[44:47], s75 offen offset:0 sc1\n\t"
    "v_cvt_pk_bf16_f32 v32, v32, v33\n\t"
    "v_mfma_f32_16x16x32_bf16 v[136:139], a[108:111], a[44:47], v[136:139]\n\t"
    "v_exp_f32 v129, v129\n\t"
    "v_mfma_f32_16x16x32_bf16 v[140:143], a[124:127], a[44:47], v[168:171]\n\t"
    "v_cvt_pk_bf16_f32 v144, v128, v129\n\t"
    "v_cvt_pk_bf16_f32 v33, v34, v35\n\t"
    "v_mfma_f32_16x16x32_bf16 v[140:143], a[120:123], a[40:43], v[140:143]\n\t"
    "v_exp_f32 v130, v130\n\t"
    "v_mfma_f32_16x16x32_bf16 v[140:143], a[116:119], a[36:39], v[140:143]\n\t"
    "v_cvt_pk_bf16_f32 v34, v48, v49\n\t"
    "v_cvt_pk_bf16_f32 v35, v50, v51\n\t"
    "v_mfma_f32_16x16x32_bf16 v[140:143], a[112:115], a[32:35], v[140:143]\n\t"
    "v_exp_f32 v131, v131\n\t"
    "v_mfma_f32_16x16x32_bf16 v[4:7], a[192:195], v[152:155], v[4:7]\n\t"
    "buffer_store_dwordx4 v[32:35], v248, s[44:47], s75 offen offset:1024 sc1\n\t"
    "v_cvt_pk_bf16_f32 v145, v130, v131\n\t"
    "v_mfma_f32_16x16x32_bf16 v[4:7], a[196:199], v[156:159], v[4:7]\n\t"
    "v_exp_f32 v132, v132\n\t"
    "v_mfma_f32_16x16x32_bf16 v[20:23], a[204:207], v[156:159], v[20:23]\n\t"
    "v_cvt_pk_bf16_f32 v64, v64, v65\n\t"
    "v_cvt_pk_bf16_f32 v65, v66, v67\n\t"
    "v_mfma_f32_16x16x32_bf16 v[20:23], a[200:203], v[152:155], v[20:23]\n\t"
    "v_exp_f32 v133, v133\n\t"
    "v_mfma_f32_16x16x32_bf16 v[36:39], a[208:211], v[152:155], v[36:39]\n\t"
    "v_cvt_pk_bf16_f32 v146, v132, v133\n\t"
    "v_cvt_pk_bf16_f32 v66, v80, v81\n\t"
    "v_mfma_f32_16x16x32_bf16 v[36:39], a[212:215], v[156:159], v[36:39]\n\t"
    "v_exp_f32 v134, v134\n\t"
    "v_mfma_f32_16x16x32_bf16 v[52:55], a[220:223], v[156:159], v[52:55]\n\t"
    "v_cvt_pk_bf16_f32 v67, v82, v83\n\t"
    "v_mfma_f32_16x16x32_bf16 v[52:55], a[216:219], v[152:155], v[52:55]\n\t"
    "v_exp_f32 v135, v135\n\t"
    "v_mfma_f32_16x16x32_bf16 v[68:71], a[224:227], v[152:155], v[68:71]\n\t"
    "buffer_store_dwordx4 v[64:67], v248, s[44:47], s75 offen offset:2048 sc1\n\t"
    "v_cvt_pk_bf16_f32 v147, v134, v135\n\t"
    "v_mfma_f32_16x16x32_bf16 v[68:71], a[228:231], v[156:159], v[68:71]\n\t"
    "v_exp_f32 v136, v136\n\t"
    "v_mfma_f32_16x16x32_bf16 v[84:87], a[236:239], v[156:159], v[84:87]\n\t"
    "v_cvt_pk_bf16_f32 v96, v96, v97\n\t"
    "v_cvt_pk_bf16_f32 v97, v98, v99\n\t"
    "v_mfma_f32_16x16x32_bf16 v[84:87], a[232:235], v[152:155], v[84:87]\n\t"
    "v_exp_f32 v137, v137\n\t"
    "v_mfma_f32_16x16x32_bf16 v[100:103], a[240:243], v[152:155], v[100:103]\n\t"
    "v_cvt_pk_bf16_f32 v148, v136, v137\n\t"
    "v_cvt_pk_bf16_f32 v98, v112, v113\n\t"
    "v_mfma_f32_16x16x32_bf16 v[100:103], a[244:247], v[156:159], v[100:103]\n\t"
    "v_exp_f32 v138, v138\n\t"
    "v_mfma_f32_16x16x32_bf16 v[116:119], a[252:255], v[156:159], v[116:119]\n\t"
    "v_cvt_pk_bf16_f32 v99, v114, v115\n\t"
    "v_mfma_f32_16x16x32_bf16 v[116:119], a[248:251], v[152:155], v[116:119]\n\t"
    "v_exp_f32 v139, v139\n\t"
    "v_smfmac_f32_16x16x64_bf16 v[180:183], v[192:195], v[152:159], v245\n\t"
    "buffer_store_dwordx4 v[96:99], v248, s[44:47], s75 offen offset:3072 sc1\n\t"
    "v_cvt_pk_bf16_f32 v149, v138, v139\n\t"
    "v_fma_f32 v214, -v214, s74, s74\n\t"
    "v_fma_f32 v215, -v215, s74, s74\n\t"
    "v_fma_f32 v216, -v216, s74, s74\n\t"
    "v_fma_f32 v217, -v217, s74, s74\n\t"
    "v_fma_f32 v218, -v218, s74, s74\n\t"
    "v_fma_f32 v219, -v219, s74, s74\n\t"
    "v_mfma_f32_16x16x32_bf16 v[128:131], a[64:67], a[48:51], v[172:175]\n\t"
    "ds_read_b128 a[64:67], v196 offset:32768\n\t"
    "v_mfma_f32_16x16x32_bf16 v[128:131], a[68:71], a[52:55], v[128:131]\n\t"
    "v_exp_f32 v140, v140\n\t"
    "v_mfma_f32_16x16x32_bf16 v[128:131], a[72:75], a[56:59], v[128:131]\n\t"
    "ds_read_b128 a[68:71], v196 offset:33024\n\t"
    "ds_read_b128 a[72:75], v196 offset:33280\n\t"
    "v_mfma_f32_16x16x32_bf16 v[128:131], a[76:79], a[60:63], v[128:131]\n\t"
    "v_exp_f32 v141, v141\n\t"
    "v_mfma_f32_16x16x32_bf16 v[132:135], a[92:95], a[60:63], v[172:175]\n\t"
    "ds_read_b128 a[76:79], v196 offset:33536\n\t"
    "v_cvt_pk_bf16_f32 v150, v140, v141\n\t"
    "v_mfma_f32_16x16x32_bf16 v[132:135], a[88:91], a[56:59], v[132:135]\n\t"
    "v_exp_f32 v142, v142\n\t"
    "v_mfma_f32_16x16x32_bf16 v[132:135], a[84:87], a[52:55], v[132:135]\n\t"
    "ds_read_b128 a[92:95], v196 offset:37632\n\t"
    "ds_read_b128 a[88:91], v196 offset:37376\n\t"
    "v_mfma_f32_16x16x32_bf16 v[132:135], a[80:83], a[48:51], v[132:135]\n\t"
    "v_exp_f32 v143, v143\n\t"
    "v_mfma_f32_16x16x32_bf16 v[136:139], a[96:99], a[48:51], v[172:175]\n\t"
    "ds_read_b128 a[84:87], v196 offset:37120\n\t"
    "v_cvt_pk_bf16_f32 v151, v142, v143\n\t"
    "v_mfma_f32_16x16x32_bf16 v[136:139], a[100:103], a[52:55], v[136:139]\n\t"
    "v_exp_f32 v128, v128\n\t"
    "v_mfma_f32_16x16x32_bf16 v[136:139], a[104:107], a[56:59], v[136:139]\n\t"
    "ds_read_b128 a[80:83], v196 offset:36864\n\t"
    "ds_read_b128 a[96:99], v196 offset:40960\n\t"
    "v_mfma_f32_16x16x32_bf16 v[136:139], a[108:111], a[60:63], v[136:139]\n\t"
    "v_exp_f32 v129, v129\n\t"
    "v_mfma_f32_16x16x32_bf16 v[140:143], a[124:127], a[60:63], v[172:175]\n\t"
    "ds_read_b128 a[100:103], v196 offset:41216\n\t"
    "v_cvt_pk_bf16_f32 v152, v128, v129\n\t"
    "v_mfma_f32_16x16x32_bf16 v[140:143], a[120:123], a[56:59], v[140:143]\n\t"
    "v_exp_f32 v130, v130\n\t"
    "v_mfma_f32_16x16x32_bf16 v[140:143], a[116:119], a[52:55], v[140:143]\n\t"
    "ds_read_b128 a[104:107], v196 offset:41472\n\t"
    "ds_read_b128 a[108:111], v196 offset:41728\n\t"
    "v_mfma_f32_16x16x32_bf16 v[140:143], a[112:115], a[48:51], v[140:143]\n\t"
    "v_exp_f32 v131, v131\n\t"
    "v_mfma_f32_16x16x32_bf16 v[8:11], a[192:195], v[144:147], v[8:11]\n\t"
    "ds_read_b128 a[124:127], v196 offset:45824\n\t"
    "v_cvt_pk_bf16_f32 v153, v130, v131\n\t"
    "v_mfma_f32_16x16x32_bf16 v[8:11], a[196:199], v[148:151], v[8:11]\n\t"
    "v_exp_f32 v132, v132\n\t"
    "v_mfma_f32_16x16x32_bf16 v[24:27], a[204:207], v[148:151], v[24:27]\n\t"
    "ds_read_b128 a[120:123], v196 offset:45568\n\t"
    "ds_read_b128 a[116:119], v196 offset:45312\n\t"
    "v_mfma_f32_16x16x32_bf16 v[24:27], a[200:203], v[144:147], v[24:27]\n\t"
    "v_exp_f32 v133, v133\n\t"
    "v_mfma_f32_16x16x32_bf16 v[40:43], a[208:211], v[144:147], v[40:43]\n\t"
    "ds_read_b128 a[112:115], v196 offset:45056\n\t"
    "v_cvt_pk_bf16_f32 v154, v132, v133\n\t"
    "v_mfma_f32_16x16x32_bf16 v[40:43], a[212:215], v[148:151], v[40:43]\n\t"
    "v_exp_f32 v134, v134\n\t"
    "v_mfma_f32_16x16x32_bf16 v[56:59], a[220:223], v[148:151], v[56:59]\n\t"
    "v_cvt_pk_bf16_f32 v4, v4, v5\n\t"
    "v_cvt_pk_bf16_f32 v5, v6, v7\n\t"
    "v_mfma_f32_16x16x32_bf16 v[56:59], a[216:219], v[144:147], v[56:59]\n\t"
    "v_exp_f32 v135, v135\n\t"
    "v_mfma_f32_16x16x32_bf16 v[72:75], a[224:227], v[144:147], v[72:75]\n\t"
    "v_cvt_pk_bf16_f32 v155, v134, v135\n\t"
    "v_cvt_pk_bf16_f32 v6, v20, v21\n\t"
    "v_mfma_f32_16x16x32_bf16 v[72:75], a[228:231], v[148:151], v[72:75]\n\t"
    "v_exp_f32 v136, v136\n\t"
    "v_mfma_f32_16x16x32_bf16 v[88:91], a[236:239], v[148:151], v[88:91]\n\t"
    "v_cvt_pk_bf16_f32 v7, v22, v23\n\t"
    "v_mfma_f32_16x16x32_bf16 v[88:91], a[232:235], v[144:147], v[88:91]\n\t"
    "v_exp_f32 v137, v137\n\t"
    "v_mfma_f32_16x16x32_bf16 v[104:107], a[240:243], v[144:147], v[104:107]\n\t"
    "buffer_store_dwordx4 v[4:7], v248, s[44:47], s70 offen offset:0 sc1\n\t"
    "v_cvt_pk_bf16_f32 v156, v136, v137\n\t"
    "v_mfma_f32_16x16x32_bf16 v[104:107], a[244:247], v[148:151], v[104:107]\n\t"
    "v_exp_f32 v138, v138\n\t"
    "v_mfma_f32_16x16x32_bf16 v[120:123], a[252:255], v[148:151], v[120:123]\n\t"
    "v_cvt_pk_bf16_f32 v36, v36, v37\n\t"
    "v_cvt_pk_bf16_f32 v37, v38, v39\n\t"
    "v_mfma_f32_16x16x32_bf16 v[120:123], a[248:251], v[144:147], v[120:123]\n\t"
    "v_exp_f32 v139, v139\n\t"
    "v_smfmac_f32_16x16x64_bf16 v[184:187], v[192:195], v[144:151], v245\n\t"
    "s_waitcnt lgkmcnt(0)\n\t"
    "v_cvt_pk_bf16_f32 v157, v138, v139\n\t"
    "v_cvt_pk_bf16_f32 v38, v52, v53\n\t"
    "v_cvt_pk_bf16_f32 v39, v54, v55\n\t"
    "buffer_store_dwordx4 v[36:39], v248, s[44:47], s70 offen offset:1024 sc1\n\t"
    "v_mfma_f32_16x16x32_bf16 v[128:131], a[64:67], a[32:35], v[168:171]\n\t"
    "v_cvt_pk_bf16_f32 v68, v68, v69\n\t"
    "v_cvt_pk_bf16_f32 v69, v70, v71\n\t"
    "v_mfma_f32_16x16x32_bf16 v[128:131], a[68:71], a[36:39], v[128:131]\n\t"
    "v_exp_f32 v140, v140\n\t"
    "v_mfma_f32_16x16x32_bf16 v[128:131], a[72:75], a[40:43], v[128:131]\n\t"
    "v_cvt_pk_bf16_f32 v70, v84, v85\n\t"
    "v_cvt_pk_bf16_f32 v71, v86, v87\n\t"
    "v_mfma_f32_16x16x32_bf16 v[128:131], a[76:79], a[44:47], v[128:131]\n\t"
    "v_exp_f32 v141, v141\n\t"
    "v_mfma_f32_16x16x32_bf16 v[132:135], a[92:95], a[44:47], v[168:171]\n\t"
    "buffer_store_dwordx4 v[68:71], v248, s[44:47], s70 offen offset:2048 sc1\n\t"
    "v_cvt_pk_bf16_f32 v158, v140, v141\n\t"
    "v_mfma_f32_16x16x32_bf16 v[132:135], a[88:91], a[40:43], v[132:135]\n\t"
    "v_exp_f32 v142, v142\n\t"
    "v_mfma_f32_16x16x32_bf16 v[132:135], a[84:87], a[36:39], v[132:135]\n\t"
    "v_cvt_pk_bf16_f32 v100, v100, v101\n\t"
    "v_cvt_pk_bf16_f32 v101, v102, v103\n\t"
    "v_mfma_f32_16x16x32_bf16 v[132:135], a[80:83], a[32:35], v[132:135]\n\t"
    "v_exp_f32 v143, v143\n\t"
    "v_mfma_f32_16x16x32_bf16 v[136:139], a[96:99], a[32:35], v[168:171]\n\t"
    "v_cvt_pk_bf16_f32 v159, v142, v143\n\t"
    "v_cvt_pk_bf16_f32 v102, v116, v117\n\t"
    "v_mfma_f32_16x16x32_bf16 v[136:139], a[100:103], a[36:39], v[136:139]\n\t"
    "v_exp_f32 v128, v128\n\t"
    "v_mfma_f32_16x16x32_bf16 v[136:139], a[104:107], a[40:43], v[136:139]\n\t"
    "v_cvt_pk_bf16_f32 v103, v118, v119\n\t"
    "v_mfma_f32_16x16x32_bf16 v[136:139], a[108:111], a[44:47], v[136:139]\n\t"
    "v_exp_f32 v129, v129\n\t"
    "v_mfma_f32_16x16x32_bf16 v[140:143], a[124:127], a[44:47], v[168:171]\n\t"
    "buffer_store_dwordx4 v[100:103], v248, s[44:47], s70 offen offset:3072 sc1\n\t"
    "v_cvt_pk_bf16_f32 v144, v128, v129\n\t"
    "v_mfma_f32_16x16x32_bf16 v[140:143], a[120:123], a[40:43], v[140:143]\n\t"
    "v_exp_f32 v130, v130\n\t"
    "v_mfma_f32_16x16x32_bf16 v[140:143], a[116:119], a[36:39], v[140:143]\n\t"
    "v_mov_b32 v236, v160\n\t"
    "v_add_f32 v237, v176, v177\n\t"
    "v_mfma_f32_16x16x32_bf16 v[140:143], a[112:115], a[32:35], v[140:143]\n\t"
    "v_exp_f32 v131, v131\n\t"
    "v_mfma_f32_16x16x32_bf16 v[12:15], a[192:195], v[152:155], v[12:15]\n\t"
    "v_cvt_pk_bf16_f32 v145, v130, v131\n\t"
    "v_mov_b32 v238, v164\n\t"
    "v_mfma_f32_16x16x32_bf16 v[12:15], a[196:199], v[156:159], v[12:15]\n\t"
    "v_exp_f32 v132, v132\n\t"
    "v_mfma_f32_16x16x32_bf16 v[28:31], a[204:207], v[156:159], v[28:31]\n\t"
    "v_add_f32 v239, v180, v181\n\t"
    "v_mfma_f32_16x16x32_bf16 v[28:31], a[200:203], v[152:155], v[28:31]\n\t"
    "v_exp_f32 v133, v133\n\t"
    "v_mfma_f32_16x16x32_bf16 v[44:47], a[208:211], v[152:155], v[44:47]\n\t"
    "buffer_store_dwordx4 v[236:239], v248, s[44:47], s69 offen sc1\n\t"
    "v_cvt_pk_bf16_f32 v146, v132, v133\n\t"
    "v_mfma_f32_16x16x32_bf16 v[44:47], a[212:215], v[156:159], v[44:47]\n\t"
    "v_exp_f32 v134, v134\n\t"
    "v_mfma_f32_16x16x32_bf16 v[60:63], a[220:223], v[156:159], v[60:63]\n\t"
    "v_add_f32 v240, v237, v239\n\t"
    "v_cmp_ngt_f32 vcc, 0x53800000, v240\n\t"
    "v_mfma_f32_16x16x32_bf16 v[60:63], a[216:219], v[152:155], v[60:63]\n\t"
    "v_exp_f32 v135, v135\n\t"
    "v_mfma_f32_16x16x32_bf16 v[76:79], a[224:227], v[152:155], v[76:79]\n\t"
    "v_cvt_pk_bf16_f32 v147, v134, v135\n\t"
    "v_add_f32 v220, v204, v168\n\t"
    "v_mfma_f32_16x16x32_bf16 v[76:79], a[228:231], v[156:159], v[76:79]\n\t"
    "v_exp_f32 v136, v136\n\t"
    "v_mfma_f32_16x16x32_bf16 v[92:95], a[236:239], v[156:159], v[92:95]\n\t"
    "v_add_f32 v221, v205, v168\n\t"
    "v_add_f32 v222, v206, v168\n\t"
    "v_mfma_f32_16x16x32_bf16 v[92:95], a[232:235], v[152:155], v[92:95]\n\t"
    "v_exp_f32 v137, v137\n\t"
    "v_mfma_f32_16x16x32_bf16 v[108:111], a[240:243], v[152:155], v[108:111]\n\t"
    "v_cvt_pk_bf16_f32 v148, v136, v137\n\t"
    "v_add_f32 v223, v207, v168\n\t"
    "v_mfma_f32_16x16x32_bf16 v[108:111], a[244:247], v[156:159], v[108:111]\n\t"
    "v_exp_f32 v138, v138\n\t"
    "v_mfma_f32_16x16x32_bf16 v[124:127], a[252:255], v[156:159], v[124:127]\n\t"
    "v_add_f32 v224, v208, v168\n\t"
    "v_add_f32 v225, v209, v168\n\t"
    "v_mfma_f32_16x16x32_bf16 v[124:127], a[248:251], v[152:155], v[124:127]\n\t"
    "v_exp_f32 v139, v139\n\t"
    "v_smfmac_f32_16x16x64_bf16 v[188:191], v[192:195], v[152:159], v245\n\t"
    "s_cmp_lg_u64 vcc, 0\n\t"
    "s_cselect_b32 s90, 1, 0\n\t"
    "s_andn2_b32 s90, s90, s67\n\t"
    "v_cvt_pk_bf16_f32 v149, v138, v139\n\t"
    "v_add_f32 v226, v210, v168\n\t"
    "v_add_f32 v227, v211, v168\n\t"
    "v_mfma_f32_16x16x32_bf16 v[128:131], a[64:67], a[48:51], v[172:175]\n\t"
    "ds_read_b128 a[64:67], v196 offset:16384\n\t"
    "v_mfma_f32_16x16x32_bf16 v[128:131], a[68:71], a[52:55], v[128:131]\n\t"
    "v_exp_f32 v140, v140\n\t"
    "v_mfma_f32_16x16x32_bf16 v[128:131], a[72:75], a[56:59], v[128:131]\n\t"
    "ds_read_b128 a[68:71], v196 offset:16640\n\t"
    "ds_read_b128 a[72:75], v196 offset:16896\n\t"
    "v_mfma_f32_16x16x32_bf16 v[128:131], a[76:79], a[60:63], v[128:131]\n\t"
    "v_exp_f32 v141, v141\n\t"
    "v_mfma_f32_16x16x32_bf16 v[132:135], a[92:95], a[60:63], v[172:175]\n\t"
    "ds_read_b128 a[76:79], v196 offset:17152\n\t"
    "v_cvt_pk_bf16_f32 v150, v140, v141\n\t"
    "v_mfma_f32_16x16x32_bf16 v[132:135], a[88:91], a[56:59], v[132:135]\n\t"
    "v_exp_f32 v142, v142\n\t"
    "v_mfma_f32_16x16x32_bf16 v[132:135], a[84:87], a[52:55], v[132:135]\n\t"
    "ds_read_b128 a[92:95], v196 offset:21248\n\t"
    "ds_read_b128 a[88:91], v196 offset:20992\n\t"
    "v_mfma_f32_16x16x32_bf16 v[132:135], a[80:83], a[48:51], v[132:135]\n\t"
    "v_exp_f32 v143, v143\n\t"
    "v_mfma_f32_16x16x32_bf16 v[136:139], a[96:99], a[48:51], v[172:175]\n\t"
    "ds_read_b128 a[84:87], v196 offset:20736\n\t"
    "v_cvt_pk_bf16_f32 v151, v142, v143\n\t"
    "v_mfma_f32_16x16x32_bf16 v[136:139], a[100:103], a[52:55], v[136:139]\n\t"
    "v_exp_f32 v128, v128\n\t"
    "v_mfma_f32_16x16x32_bf16 v[136:139], a[104:107], a[56:59], v[136:139]\n\t"
    "ds_read_b128 a[80:83], v196 offset:20480\n\t"
    "ds_read_b128 a[96:99], v196 offset:24576\n\t"
    "v_mfma_f32_16x16x32_bf16 v[136:139], a[108:111], a[60:63], v[136:139]\n\t"
    "v_exp_f32 v129, v129\n\t"
    "v_mfma_f32_16x16x32_bf16 v[140:143], a[124:127], a[60:63], v[172:175]\n\t"
    "ds_read_b128 a[100:103], v196 offset:24832\n\t"
    "v_cvt_pk_bf16_f32 v152, v128, v129\n\t"
    "v_mfma_f32_16x16x32_bf16 v[140:143], a[120:123], a[56:59], v[140:143]\n\t"
    "v_exp_f32 v130, v130\n\t"
    "v_mfma_f32_16x16x32_bf16 v[140:143], a[116:119], a[52:55], v[140:143]\n\t"
    "ds_read_b128 a[104:107], v196 offset:25088\n\t"
    "ds_read_b128 a[108:111], v196 offset:25344\n\t"
    "v_mfma_f32_16x16x32_bf16 v[140:143], a[112:115], a[48:51], v[140:143]\n\t"
    "v_exp_f32 v131, v131\n\t"
    "v_mfma_f32_16x16x32_bf16 v[8:11], a[128:131], v[144:147], v[8:11]\n\t"
    "ds_read_b128 a[124:127], v196 offset:29440\n\t"
    "v_cvt_pk_bf16_f32 v153, v130, v131\n\t"
    "v_mfma_f32_16x16x32_bf16 v[8:11], a[132:135], v[148:151], v[8:11]\n\t"
    "v_exp_f32 v132, v132\n\t"
    "v_mfma_f32_16x16x32_bf16 v[24:27], a[140:143], v[148:151], v[24:27]\n\t"
    "ds_read_b128 a[120:123], v196 offset:29184\n\t"
    "ds_read_b128 a[116:119], v196 offset:28928\n\t"
    "v_mfma_f32_16x16x32_bf16 v[24:27], a[136:139], v[144:147], v[24:27]\n\t"
    "v_exp_f32 v133, v133\n\t"
    "v_mfma_f32_16x16x32_bf16 v[40:43], a[144:147], v[144:147], v[40:43]\n\t"
    "ds_read_b128 a[112:115], v196 offset:28672\n\t"
    "v_cvt_pk_bf16_f32 v154, v132, v133\n\t"
    "v_mfma_f32_16x16x32_bf16 v[40:43], a[148:151], v[148:151], v[40:43]\n\t"
    "v_exp_f32 v134, v134\n\t"
    "v_mfma_f32_16x16x32_bf16 v[56:59], a[156:159], v[148:151], v[56:59]\n\t"
    "ds_read_b64_tr_b16 a[192:193], v197 offset:16384\n\t"
    "ds_read_b64_tr_b16 a[194:195], v197 offset:20480\n\t"
    "v_mfma_f32_16x16x32_bf16 v[56:59], a[152:155], v[144:147], v[56:59]\n\t"
    "v_exp_f32 v135, v135\n\t"
    "v_mfma_f32_16x16x32_bf16 v[72:75], a[160:163], v[144:147], v[72:75]\n\t"
    "ds_read_b64_tr_b16 a[196:197], v197 offset:24576\n\t"
    "v_cvt_pk_bf16_f32 v155, v134, v135\n\t"
    "v_mfma_f32_16x16x32_bf16 v[72:75], a[164:167], v[148:151], v[72:75]\n\t"
    "v_exp_f32 v136, v136\n\t"
    "v_mfma_f32_16x16x32_bf16 v[88:91], a[172:175], v[148:151], v[88:91]\n\t"
    "ds_read_b64_tr_b16 a[198:199], v197 offset:28672\n\t"
    "ds_read_b64_tr_b16 a[200:201], v198 offset:16384\n\t"
    "v_mfma_f32_16x16x32_bf16 v[88:91], a[168:171], v[144:147], v[88:91]\n\t"
    "v_exp_f32 v137, v137\n\t"
    "v_mfma_f32_16x16x32_bf16 v[104:107], a[176:179], v[144:147], v[104:107]\n\t"
    "ds_read_b64_tr_b16 a[202:203], v198 offset:20480\n\t"
    "v_cvt_pk_bf16_f32 v156, v136, v137\n\t"
    "v_mfma_f32_16x16x32_bf16 v[104:107], a[180:183], v[148:151], v[104:107]\n\t"
    "v_exp_f32 v138, v138\n\t"
    "v_mfma_f32_16x16x32_bf16 v[120:123], a[188:191], v[148:151], v[120:123]\n\t"
    "ds_read_b64_tr_b16 a[204:205], v198 offset:24576\n\t"
    "ds_read_b64_tr_b16 a[206:207], v198 offset:28672\n\t"
    "v_mfma_f32_16x16x32_bf16 v[120:123], a[184:187], v[144:147], v[120:123]\n\t"
    "v_exp_f32 v139, v139\n\t"
    "v_smfmac_f32_16x16x64_bf16 v[184:187], v[192:195], v[144:151], v245\n\t"
    "s_waitcnt lgkmcnt(8)\n\t"
    "v_cvt_pk_bf16_f32 v157, v138, v139\n\t"
    "s_waitcnt vmcnt(0)\n\t"
    "s_cmp_eq_u32 s90, 0\n\t"
    "s_cselect_b32 s46, s46, 0\n\t"
    "s_cselect_b32 s91, 1, s91\n\t"
    "v_add_u32 v252, 1, v255\n\t"
    "buffer_store_dword v252, off, s[44:47], s77 sc1\n\t"
    "v_mfma_f32_16x16x32_bf16 v[128:131], a[64:67], a[32:35], v[168:171]\n\t"
    "ds_read_b64_tr_b16 a[208:209], v197 offset:16640\n\t"
    "ds_read_b64_tr_b16 a[210:211], v197 offset:20736\n\t"
    "v_mfma_f32_16x16x32_bf16 v[128:131], a[68:71], a[36:39], v[128:131]\n\t"
    "v_exp_f32 v140, v140\n\t"
    "v_mfma_f32_16x16x32_bf16 v[128:131], a[72:75], a[40:43], v[128:131]\n\t"
    "ds_read_b64_tr_b16 a[212:213], v197 offset:24832\n\t"
    "ds_read_b64_tr_b16 a[214:215], v197 offset:28928\n\t"
    "v_mfma_f32_16x16x32_bf16 v[128:131], a[76:79], a[44:47], v[128:131]\n\t"
    "v_exp_f32 v141, v141\n\t"
    "v_mfma_f32_16x16x32_bf16 v[132:135], a[92:95], a[44:47], v[168:171]\n\t"
    "ds_read_b64_tr_b16 a[216:217], v198 offset:16640\n\t"
    "v_cvt_pk_bf16_f32 v158, v140, v141\n\t"
    "v_mfma_f32_16x16x32_bf16 v[132:135], a[88:91], a[40:43], v[132:135]\n\t"
    "v_exp_f32 v142, v142\n\t"
    "v_mfma_f32_16x16x32_bf16 v[132:135], a[84:87], a[36:39], v[132:135]\n\t"
    "ds_read_b64_tr_b16 a[218:219], v198 offset:20736\n\t"
    "ds_read_b64_tr_b16 a[220:221], v198 offset:24832\n\t"
    "v_mfma_f32_16x16x32_bf16 v[132:135], a[80:83], a[32:35], v[132:135]\n\t"
    "v_exp_f32 v143, v143\n\t"
    "v_mfma_f32_16x16x32_bf16 v[136:139], a[96:99], a[32:35], v[168:171]\n\t"
    "ds_read_b64_tr_b16 a[222:223], v198 offset:28928\n\t"
    "v_cvt_pk_bf16_f32 v159, v142, v143\n\t"
    "v_mfma_f32_16x16x32_bf16 v[136:139], a[100:103], a[36:39], v[136:139]\n\t"
    "v_exp_f32 v128, v128\n\t"
    "v_mfma_f32_16x16x32_bf16 v[136:139], a[104:107], a[40:43], v[136:139]\n\t"
    "ds_read_b64_tr_b16 a[224:225], v197 offset:16896\n\t"
    "ds_read_b64_tr_b16 a[226:227], v197 offset:20992\n\t"
    "v_mfma_f32_16x16x32_bf16 v[136:139], a[108:111], a[44:47], v[136:139]\n\t"
    "v_exp_f32 v129, v129\n\t"
    "v_mfma_f32_16x16x32_bf16 v[140:143], a[124:127], a[44:47], v[168:171]\n\t"
    "ds_read_b64_tr_b16 a[228:229], v197 offset:25088\n\t"
    "v_cvt_pk_bf16_f32 v144, v128, v129\n\t"
    "v_mfma_f32_16x16x32_bf16 v[140:143], a[120:123], a[40:43], v[140:143]\n\t"
    "v_exp_f32 v130, v130\n\t"
    "v_mfma_f32_16x16x32_bf16 v[140:143], a[116:119], a[36:39], v[140:143]\n\t"
    "ds_read_b64_tr_b16 a[230:231], v197 offset:29184\n\t"
    "ds_read_b64_tr_b16 a[232:233], v198 offset:16896\n\t"
    "v_mfma_f32_16x16x32_bf16 v[140:143], a[112:115], a[32:35], v[140:143]\n\t"
    "v_exp_f32 v131, v131\n\t"
    "v_mfma_f32_16x16x32_bf16 v[12:15], a[128:131], v[152:155], v[12:15]\n\t"
    "ds_read_b64_tr_b16 a[234:235], v198 offset:20992\n\t"
    "v_cvt_pk_bf16_f32 v145, v130, v131\n\t"
    "v_mfma_f32_16x16x32_bf16 v[12:15], a[132:135], v[156:159], v[12:15]\n\t"
    "v_exp_f32 v132, v132\n\t"
    "v_mfma_f32_16x16x32_bf16 v[28:31], a[140:143], v[156:159], v[28:31]\n\t"
    "ds_read_b64_tr_b16 a[236:237], v198 offset:25088\n\t"
    "ds_read_b64_tr_b16 a[238:239], v198 offset:29184\n\t"
    "v_mfma_f32_16x16x32_bf16 v[28:31], a[136:139], v[152:155], v[28:31]\n\t"
    "v_exp_f32 v133, v133\n\t"
    "v_mfma_f32_16x16x32_bf16 v[44:47], a[144:147], v[152:155], v[44:47]\n\t"
    "ds_read_b64_tr_b16 a[240:241], v197 offset:17152\n\t"
    "v_cvt_pk_bf16_f32 v146, v132, v133\n\t"
    "v_mfma_f32_16x16x32_bf16 v[44:47], a[148:151], v[156:159], v[44:47]\n\t"
    "v_exp_f32 v134, v134\n\t"
    "v_mfma_f32_16x16x32_bf16 v[60:63], a[156:159], v[156:159], v[60:63]\n\t"
    "ds_read_b64_tr_b16 a[242:243], v197 offset:21248\n\t"
    "ds_read_b64_tr_b16 a[244:245], v197 offset:25344\n\t"
    "v_mfma_f32_16x16x32_bf16 v[60:63], a[152:155], v[152:155], v[60:63]\n\t"
    "v_exp_f32 v135, v135\n\t"
    "v_mfma_f32_16x16x32_bf16 v[76:79], a[160:163], v[152:155], v[76:79]\n\t"
    "ds_read_b64_tr_b16 a[246:247], v197 offset:29440\n\t"
    "v_cvt_pk_bf16_f32 v147, v134, v135\n\t"
    "v_add_f32 v228, v212, v172\n\t"
    "v_mfma_f32_16x16x32_bf16 v[76:79], a[164:167], v[156:159], v[76:79]\n\t"
    "v_exp_f32 v136, v136\n\t"
    "v_add_f32 v229, v213, v172\n\t"
    "v_mfma_f32_16x16x32_bf16 v[92:95], a[172:175], v[156:159], v[92:95]\n\t"
    "ds_read_b64_tr_b16 a[248:249], v198 offset:17152\n\t"
    "ds_read_b64_tr_b16 a[250:251], v198 offset:21248\n\t"
    "v_add_f32 v230, v214, v172\n\t"
    "v_mfma_f32_16x16x32_bf16 v[92:95], a[168:171], v[152:155], v[92:95]\n\t"
    "v_exp_f32 v137, v137\n\t"
    "v_add_f32 v231, v215, v172\n\t"
    "v_mfma_f32_16x16x32_bf16 v[108:111], a[176:179], v[152:155], v[108:111]\n\t"
    "ds_read_b64_tr_b16 a[252:253], v198 offset:25344\n\t"
    "v_cvt_pk_bf16_f32 v148, v136, v137\n\t"
    "v_add_f32 v232, v216, v172\n\t"
    "v_mfma_f32_16x16x32_bf16 v[108:111], a[180:183], v[156:159], v[108:111]\n\t"
    "s_add_u32 s69, s76, 8192\n\t"
    "v_exp_f32 v138, v138\n\t"
    "v_add_f32 v233, v217, v172\n\t"
    "v_mfma_f32_16x16x32_bf16 v[124:127], a[188:191], v[156:159], v[124:127]\n\t"
    "ds_read_b64_tr_b16 a[254:255], v198 offset:29440\n\t"
    "v_add_f32 v234, v218, v172\n\t"
    "v_mfma_f32_16x16x32_bf16 v[124:127], a[184:187], v[152:155], v[124:127]\n\t"
    "s_add_u32 s70, s76, 4096\n\t"
    "v_exp_f32 v139, v139\n\t"
    "v_add_f32 v235, v219, v172\n\t"
    "v_smfmac_f32_16x16x64_bf16 v[188:191], v[192:195], v[152:159], v245\n\t"
    "buffer_load_dword v249, off, s[48:51], s78 sc1\n\t"
    "s_barrier\n\t"
    "v_cvt_pk_bf16_f32 v149, v138, v139\n\t"
    "v_mfma_f32_16x16x32_bf16 v[128:131], a[64:67], a[48:51], v[172:175]\n\t"
    "ds_read_b128 a[64:67], v196 offset:0\n\t"
    "v_mfma_f32_16x16x32_bf16 v[128:131], a[68:71], a[52:55], v[128:131]\n\t"
    "v_exp_f32 v140, v140\n\t"
    "v_mfma_f32_16x16x32_bf16 v[128:131], a[72:75], a[56:59], v[128:131]\n\t"
    "ds_read_b128 a[68:71], v196 offset:256\n\t"
    "ds_read_b128 a[72:75], v196 offset:512\n\t"
    "v_mfma_f32_16x16x32_bf16 v[128:131], a[76:79], a[60:63], v[128:131]\n\t"
    "v_exp_f32 v141, v141\n\t"
    "v_mfma_f32_16x16x32_bf16 v[132:135], a[92:95], a[60:63], v[172:175]\n\t"
    "ds_read_b128 a[76:79], v196 offset:768\n\t"
    "v_cvt_pk_bf16_f32 v150, v140, v141\n\t"
    "v_mfma_f32_16x16x32_bf16 v[132:135], a[88:91], a[56:59], v[132:135]\n\t"
    "v_exp_f32 v142, v142\n\t"
    "v_mfma_f32_16x16x32_bf16 v[132:135], a[84:87], a[52:55], v[132:135]\n\t"
    "ds_read_b128 a[92:95], v196 offset:4864\n\t"
    "ds_read_b128 a[88:91], v196 offset:4608\n\t"
    "v_mfma_f32_16x16x32_bf16 v[132:135], a[80:83], a[48:51], v[132:135]\n\t"
    "v_exp_f32 v143, v143\n\t"
    "v_mfma_f32_16x16x32_bf16 v[136:139], a[96:99], a[48:51], v[172:175]\n\t"
    "ds_read_b128 a[84:87], v196 offset:4352\n\t"
    "v_cvt_pk_bf16_f32 v151, v142, v143\n\t"
    "v_mfma_f32_16x16x32_bf16 v[136:139], a[100:103], a[52:55], v[136:139]\n\t"
    "v_exp_f32 v128, v128\n\t"
    "v_mfma_f32_16x16x32_bf16 v[136:139], a[104:107], a[56:59], v[136:139]\n\t"
    "ds_read_b128 a[80:83], v196 offset:4096\n\t"
    "ds_read_b64_tr_b16 a[128:129], v197 offset:0\n\t"
    "v_mfma_f32_16x16x32_bf16 v[136:139], a[108:111], a[60:63], v[136:139]\n\t"
    "v_exp_f32 v129, v129\n\t"
    "v_mfma_f32_16x16x32_bf16 v[140:143], a[124:127], a[60:63], v[172:175]\n\t"
    "ds_read_b64_tr_b16 a[130:131], v197 offset:4096\n\t"
    "v_cvt_pk_bf16_f32 v152, v128, v129\n\t"
    "v_mfma_f32_16x16x32_bf16 v[140:143], a[120:123], a[56:59], v[140:143]\n\t"
    "s_waitcnt lgkmcnt(10)\n\t"
    "v_exp_f32 v130, v130\n\t"
    "v_mfma_f32_16x16x32_bf16 v[140:143], a[116:119], a[52:55], v[140:143]\n\t"
    "ds_read_b64_tr_b16 a[136:137], v198 offset:0\n\t"
    "ds_read_b64_tr_b16 a[138:139], v198 offset:4096\n\t"
    "v_mfma_f32_16x16x32_bf16 v[140:143], a[112:115], a[48:51], v[140:143]\n\t"
    "v_exp_f32 v131, v131\n\t"
    "v_mfma_f32_16x16x32_bf16 v[8:11], a[192:195], v[144:147], v[8:11]\n\t"
    "ds_read_b64_tr_b16 a[144:145], v197 offset:256\n\t"
    "v_cvt_pk_bf16_f32 v153, v130, v131\n\t"
    "v_mfma_f32_16x16x32_bf16 v[8:11], a[196:199], v[148:151], v[8:11]\n\t"
    "v_exp_f32 v132, v132\n\t"
    "v_mfma_f32_16x16x32_bf16 v[24:27], a[204:207], v[148:151], v[24:27]\n\t"
    "ds_read_b64_tr_b16 a[146:147], v197 offset:4352\n\t"
    "ds_read_b64_tr_b16 a[152:153], v198 offset:256\n\t"
    "v_mfma_f32_16x16x32_bf16 v[24:27], a[200:203], v[144:147], v[24:27]\n\t"
    "v_exp_f32 v133, v133\n\t"
    "v_mfma_f32_16x16x32_bf16 v[40:43], a[208:211], v[144:147], v[40:43]\n\t"
    "ds_read_b64_tr_b16 a[154:155], v198 offset:4352\n\t"
    "v_cvt_pk_bf16_f32 v154, v132, v133\n\t"
    "v_mfma_f32_16x16x32_bf16 v[40:43], a[212:215], v[148:151], v[40:43]\n\t"
    "v_exp_f32 v134, v134\n\t"
    "v_mfma_f32_16x16x32_bf16 v[56:59], a[220:223], v[148:151], v[56:59]\n\t"
    "ds_read_b64_tr_b16 a[160:161], v197 offset:512\n\t"
    "ds_read_b64_tr_b16 a[162:163], v197 offset:4608\n\t"
    "v_mfma_f32_16x16x32_bf16 v[56:59], a[216:219], v[144:147], v[56:59]\n\t"
    "v_exp_f32 v135, v135\n\t"
    "v_mfma_f32_16x16x32_bf16 v[72:75], a[224:227], v[144:147], v[72:75]\n\t"
    "ds_read_b64_tr_b16 a[168:169], v198 offset:512\n\t"
    "v_cvt_pk_bf16_f32 v155, v134, v135\n\t"
    "v_mfma_f32_16x16x32_bf16 v[72:75], a[228:231], v[148:151], v[72:75]\n\t"
    "v_exp_f32 v136, v136\n\t"
    "v_mfma_f32_16x16x32_bf16 v[88:91], a[236:239], v[148:151], v[88:91]\n\t"
    "ds_read_b64_tr_b16 a[170:171], v198 offset:4608\n\t"
    "ds_read_b64_tr_b16 a[176:177], v197 offset:768\n\t"
    "v_mfma_f32_16x16x32_bf16 v[88:91], a[232:235], v[144:147], v[88:91]\n\t"
    "v_exp_f32 v137, v137\n\t"
    "v_mfma_f32_16x16x32_bf16 v[104:107], a[240:243], v[144:147], v[104:107]\n\t"
    "ds_read_b64_tr_b16 a[178:179], v197 offset:4864\n\t"
    "v_cvt_pk_bf16_f32 v156, v136, v137\n\t"
    "v_mfma_f32_16x16x32_bf16 v[104:107], a[244:247], v[148:151], v[104:107]\n\t"
    "v_exp_f32 v138, v138\n\t"
    "v_mfma_f32_16x16x32_bf16 v[120:123], a[252:255], v[148:151], v[120:123]\n\t"
    "ds_read_b64_tr_b16 a[184:185], v198 offset:768\n\t"
    "ds_read_b64_tr_b16 a[186:187], v198 offset:4864\n\t"
    "v_mfma_f32_16x16x32_bf16 v[120:123], a[248:251], v[144:147], v[120:123]\n\t"
    "v_exp_f32 v139, v139\n\t"
    "v_smfmac_f32_16x16x64_bf16 v[184:187], v[192:195], v[144:151], v245\n\t"
    "s_waitcnt lgkmcnt(0)\n\t"
    "v_cvt_pk_bf16_f32 v157, v138, v139\n\t"
    "v_mfma_f32_16x16x32_bf16 v[128:131], a[64:67], a[32:35], v[220:223]\n\t"
    "v_mfma_f32_16x16x32_bf16 v[128:131], a[68:71], a[36:39], v[128:131]\n\t"
    "v_exp_f32 v140, v140\n\t"
    "v_mfma_f32_16x16x32_bf16 v[128:131], a[72:75], a[40:43], v[128:131]\n\t"
    "v_mfma_f32_16x16x32_bf16 v[128:131], a[76:79], a[44:47], v[128:131]\n\t"
    "v_exp_f32 v141, v141\n\t"
    "v_mfma_f32_16x16x32_bf16 v[132:135], a[80:83], a[32:35], v[224:227]\n\t"
    "v_cvt_pk_bf16_f32 v158, v140, v141\n\t"
    "v_mfma_f32_16x16x32_bf16 v[132:135], a[84:87], a[36:39], v[132:135]\n\t"
    "v_exp_f32 v142, v142\n\t"
    "v_mfma_f32_16x16x32_bf16 v[132:135], a[88:91], a[40:43], v[132:135]\n\t"
    "v_mfma_f32_16x16x32_bf16 v[132:135], a[92:95], a[44:47], v[132:135]\n\t"
    "v_exp_f32 v143, v143\n\t"
    "v_mfma_f32_16x16x32_bf16 v[136:139], a[64:67], a[48:51], v[228:231]\n\t"
    "v_cvt_pk_bf16_f32 v159, v142, v143\n\t"
    "v_mfma_f32_16x16x32_bf16 v[136:139], a[68:71], a[52:55], v[136:139]\n\t"
    "v_exp_f32 v128, v128\n\t"
    "v_mfma_f32_16x16x32_bf16 v[136:139], a[72:75], a[56:59], v[136:139]\n\t"
    "v_mfma_f32_16x16x32_bf16 v[136:139], a[76:79], a[60:63], v[136:139]\n\t"
    "v_exp_f32 v129, v129\n\t"
    "v_mfma_f32_16x16x32_bf16 v[140:143], a[80:83], a[48:51], v[232:235]\n\t"
    "v_cvt_pk_bf16_f32 v144, v128, v129\n\t"
    "v_mfma_f32_16x16x32_bf16 v[140:143], a[84:87], a[52:55], v[140:143]\n\t"
    "v_exp_f32 v130, v130\n\t"
    "v_mfma_f32_16x16x32_bf16 v[140:143], a[88:91], a[56:59], v[140:143]\n\t"
    "v_mfma_f32_16x16x32_bf16 v[140:143], a[92:95], a[60:63], v[140:143]\n\t"
    "v_exp_f32 v131, v131\n\t"
    "v_mfma_f32_16x16x32_bf16 v[12:15], a[192:195], v[152:155], v[12:15]\n\t"
    "v_cvt_pk_bf16_f32 v145, v130, v131\n\t"
    "v_mfma_f32_16x16x32_bf16 v[12:15], a[196:199], v[156:159], v[12:15]\n\t"
    "v_exp_f32 v132, v132\n\t"
    "v_mfma_f32_16x16x32_bf16 v[28:31], a[204:207], v[156:159], v[28:31]\n\t"
    "v_mov_b32 v220, 0x3f003f00\n\t"
    "v_mov_b32 v221, 0x3f003f00\n\t"
    "v_mfma_f32_16x16x32_bf16 v[28:31], a[200:203], v[152:155], v[28:31]\n\t"
    "v_exp_f32 v133, v133\n\t"
    "v_mfma_f32_16x16x32_bf16 v[44:47], a[208:211], v[152:155], v[44:47]\n\t"
    "v_cvt_pk_bf16_f32 v146, v132, v133\n\t"
    "v_mov_b32 v222, 0x3f003f00\n\t"
    "v_mfma_f32_16x16x32_bf16 v[44:47], a[212:215], v[156:159], v[44:47]\n\t"
    "v_exp_f32 v134, v134\n\t"
    "v_mfma_f32_16x16x32_bf16 v[60:63], a[220:223], v[156:159], v[60:63]\n\t"
    "v_mov_b32 v223, 0x3f003f00\n\t"
    "v_mfma_f32_16x16x32_bf16 v[60:63], a[216:219], v[152:155], v[60:63]\n\t"
    "v_exp_f32 v135, v135\n\t"
    "v_mfma_f32_16x16x32_bf16 v[76:79], a[224:227], v[152:155], v[76:79]\n\t"
    "v_cvt_pk_bf16_f32 v147, v134, v135\n\t"
    "s_waitcnt vmcnt(0)\n\t"
    "v_cmp_ne_u32 vcc, v249, v254\n\t"
    "s_mov_b32 s66, 0\n\t"
    "s_cbranch_vccz NEA_%=\n\t"
    "buffer_load_dwordx4 v[236:239], v248, s[48:51], s69 offen sc1\n\t"
    "buffer_load_dwordx4 v[0:3], v248, s[48:51], s76 offen offset:0 sc1\n\t"
    "buffer_load_dwordx4 v[32:35], v248, s[48:51], s76 offen offset:1024 sc1\n\t"
    "buffer_load_dwordx4 v[64:67], v248, s[48:51], s76 offen offset:2048 sc1\n\t"
    "buffer_load_dwordx4 v[96:99], v248, s[48:51], s76 offen offset:3072 sc1\n\t"
    "buffer_load_dwordx4 v[4:7], v248, s[48:51], s70 offen offset:0 sc1\n\t"
    "buffer_load_dwordx4 v[36:39], v248, s[48:51], s70 offen offset:1024 sc1\n\t"
    "buffer_load_dwordx4 v[68:71], v248, s[48:51], s70 offen offset:2048 sc1\n\t"
    "buffer_load_dwordx4 v[100:103], v248, s[48:51], s70 offen offset:3072 sc1\n\t"
    "s_mov_b32 s66, 1\n\t"
    "NEA_%=:\n\t"
    "v_mfma_f32_16x16x32_bf16 v[76:79], a[228:231], v[156:159], v[76:79]\n\t"
    "v_exp_f32 v136, v136\n\t"
    "v_mfma_f32_16x16x32_bf16 v[92:95], a[236:239], v[156:159], v[92:95]\n\t"
    "v_mfma_f32_16x16x32_bf16 v[92:95], a[232:235], v[152:155], v[92:95]\n\t"
    "v_exp_f32 v137, v137\n\t"
    "v_mfma_f32_16x16x32_bf16 v[108:111], a[240:243], v[152:155], v[108:111]\n\t"
    "v_cvt_pk_bf16_f32 v148, v136, v137\n\t"
    "v_mfma_f32_16x16x32_bf16 v[108:111], a[244:247], v[156:159], v[108:111]\n\t"
    "v_exp_f32 v138, v138\n\t"
    "v_mfma_f32_16x16x32_bf16 v[124:127], a[252:255], v[156:159], v[124:127]\n\t"
    "v_mfma_f32_16x16x32_bf16 v[124:127], a[248:251], v[152:155], v[124:127]\n\t"
    "v_exp_f32 v139, v139\n\t"
    "v_smfmac_f32_16x16x64_bf16 v[188:191], v[192:195], v[152:159], v245\n\t"
    "v_cvt_pk_bf16_f32 v149, v138, v139\n\t"
    "v_mfma_f32_16x16x32_bf16 v[184:187], v[220:223], v[144:147], v[184:187]\n\t"
    "v_exp_f32 v140, v140\n\t"
    "v_mfma_f32_16x16x32_bf16 v[8:11], a[128:131], v[144:147], v[8:11]\n\t"
    "v_exp_f32 v141, v141\n\t"
    "v_mfma_f32_16x16x32_bf16 v[24:27], a[136:139], v[144:147], v[24:27]\n\t"
    "v_exp_f32 v142, v142\n\t"
    "v_mfma_f32_16x16x32_bf16 v[40:43], a[144:147], v[144:147], v[40:43]\n\t"
    "v_exp_f32 v143, v143\n\t"
    "v_mfma_f32_16x16x32_bf16 v[56:59], a[152:155], v[144:147], v[56:59]\n\t"
    "v_cvt_pk_bf16_f32 v150, v140, v141\n\t"
    "v_cvt_pk_bf16_f32 v151, v142, v143\n\t"
    "v_mfma_f32_16x16x32_bf16 v[72:75], a[160:163], v[144:147], v[72:75]\n\t"
    "s_and_b32 s68, %1, 1\n\t"
    "v_and_b32 v240, 15, v203\n\t"
    "v_lshrrev_b32 v241, 4, v203\n\t"
    "v_mfma_f32_16x16x32_bf16 v[88:91], a[168:171], v[144:147], v[88:91]\n\t"
    "s_lshl_b32 s68, s68, 14\n\t"
    "v_mul_u32_u24 v196, 272, v240\n\t"
    "v_mul_u32_u24 v246, 272, v241\n\t"
    "v_mfma_f32_16x16x32_bf16 v[104:107], a[176:179], v[144:147], v[104:107]\n\t"
    "s_lshr_b32 s71, %1, 1\n\t"
    "v_lshl_add_u32 v196, v241, 3, v196\n\t"
    "v_lshl_add_u32 v246, v240, 4, v246\n\t"
    "v_mfma_f32_16x16x32_bf16 v[120:123], a[184:187], v[144:147], v[120:123]\n\t"
    "s_lshl_b32 s71, s71, 16\n\t"
    "v_mfma_f32_16x16x32_bf16 v[188:191], v[220:223], v[148:151], v[188:191]\n\t"
    "s_add_u32 s68, s68, s71\n\t"
    "v_mfma_f32_16x16x32_bf16 v[12:15], a[128:131], v[148:151], v[12:15]\n\t"
    "s_add_u32 s68, s68, 0x8000\n\t"
    "v_add_u32 v196, s68, v196\n\t"
    "v_add_u32 v246, s68, v246\n\t"
    "v_mfma_f32_16x16x32_bf16 v[28:31], a[136:139], v[148:151], v[28:31]\n\t"
    "s_lshl_b32 s68, %1, 2\n\t"
    "v_mfma_f32_16x16x32_bf16 v[44:47], a[144:147], v[148:151], v[44:47]\n\t"
    "s_add_u32 s68, s68, 0x20000\n\t"
    "v_mov_b32 v242, s68\n\t"
    "v_mov_b32 v243, 0x20000\n\t"
    "v_mfma_f32_16x16x32_bf16 v[60:63], a[152:155], v[148:151], v[60:63]\n\t"
    "s_add_u32 s71, s89, 4096\n\t"
    "v_add_u32 v252, 2, v255\n\t"
    "v_mfma_f32_16x16x32_bf16 v[76:79], a[160:163], v[148:151], v[76:79]\n\t"
    "v_add_f32 v184, v184, v185\n\t"
    "v_mfma_f32_16x16x32_bf16 v[92:95], a[168:171], v[148:151], v[92:95]\n\t"
    "v_add_f32 v188, v188, v189\n\t"
    "v_add_f32 v240, v184, v188\n\t"
    "v_cmp_ngt_f32 vcc, 0x53800000, v240\n\t"
    "v_mfma_f32_16x16x32_bf16 v[108:111], a[176:179], v[148:151], v[108:111]\n\t"
    "s_cmp_lg_u64 vcc, 0\n\t"
    "s_cselect_b32 s68, 1, s90\n\t"
    "v_mov_b32 v241, s68\n\t"
    "v_mfma_f32_16x16x32_bf16 v[124:127], a[184:187], v[148:151], v[124:127]\n\t"
    "ds_write_b32 v242, v241\n\t"
    "s_waitcnt lgkmcnt(0)\n\t"
    "s_barrier\n\t"
    "ds_read_b128 v[240:243], v243\n\t"
    "s_cmp_lg_u32 s66, 0\n\t"
    "s_cbranch_scc1 GOTA_%=\n\t"
    "s_waitcnt lgkmcnt(0)\n\t"
    "v_or3_b32 v240, v240, v241, v242\n\t"
    "v_or_b32 v240, v240, v243\n\t"
    "s_nop 0\n\t"
    "v_readfirstlane_b32 s68, v240\n\t"
    "s_nop 1\n\t"
    "s_andn2_b32 s68, s68, s67\n\t"
    "s_cbranch_scc1 FB_%=\n\t"
    "s_mov_b32 s68, 0\n\t"
    "PLA_%=:\n\t"
    "buffer_load_dword v249, off, s[48:51], s78 sc1\n\t"
    "s_add_u32 s68, s68, 1\n\t"
    "s_waitcnt vmcnt(0)\n\t"
    "v_cmp_ne_u32 vcc, v249, v254\n\t"
    "s_cmp_gt_u32 s68, 0x400000\n\t"
    "s_cbranch_scc1 PXA_%=\n\t"
    "s_cbranch_vccz PLA_%=\n\t"
    "PXA_%=:\n\t"
    "buffer_load_dwordx4 v[236:239], v248, s[48:51], s69 offen sc1\n\t"
    "buffer_load_dwordx4 v[0:3], v248, s[48:51], s76 offen offset:0 sc1\n\t"
    "buffer_load_dwordx4 v[32:35], v248, s[48:51], s76 offen offset:1024 sc1\n\t"
    "buffer_load_dwordx4 v[64:67], v248, s[48:51], s76 offen offset:2048 sc1\n\t"
    "buffer_load_dwordx4 v[96:99], v248, s[48:51], s76 offen offset:3072 sc1\n\t"
    "buffer_load_dwordx4 v[4:7], v248, s[48:51], s70 offen offset:0 sc1\n\t"
    "buffer_load_dwordx4 v[36:39], v248, s[48:51], s70 offen offset:1024 sc1\n\t"
    "buffer_load_dwordx4 v[68:71], v248, s[48:51], s70 offen offset:2048 sc1\n\t"
    "buffer_load_dwordx4 v[100:103], v248, s[48:51], s70 offen offset:3072 sc1\n\t"
    "GOTA_%=:\n\t"
    "s_waitcnt vmcnt(8)\n\t"
    "v_min_f32 v136, v168, v236\n\t"
    "v_sub_f32 v128, v136, v168\n\t"
    "v_sub_f32 v130, v136, v236\n\t"
    "v_exp_f32 v128, v128\n\t"
    "v_exp_f32 v130, v130\n\t"
    "s_nop 0\n\t"
    "v_mul_f32 v136, v128, v184\n\t"
    "v_fmac_f32 v136, v130, v237\n\t"
    "v_rcp_f32 v136, v136\n\t"
    "s_nop 0\n\t"
    "v_mul_f32 v128, v128, v136\n\t"
    "v_mul_f32 v130, v130, v136\n\t"
    "v_min_f32 v136, v172, v238\n\t"
    "v_sub_f32 v132, v136, v172\n\t"
    "v_sub_f32 v134, v136, v238\n\t"
    "v_exp_f32 v132, v132\n\t"
    "v_exp_f32 v134, v134\n\t"
    "s_nop 0\n\t"
    "v_mul_f32 v136, v132, v188\n\t"
    "v_fmac_f32 v136, v134, v239\n\t"
    "v_rcp_f32 v136, v136\n\t"
    "s_nop 0\n\t"
    "v_mul_f32 v132, v132, v136\n\t"
    "v_mul_f32 v134, v134, v136\n\t"
    "s_waitcnt lgkmcnt(0)\n\t"
    "v_or3_b32 v240, v240, v241, v242\n\t"
    "v_or_b32 v240, v240, v243\n\t"
    "s_nop 0\n\t"
    "v_readfirstlane_b32 s68, v240\n\t"
    "s_nop 1\n\t"
    "s_andn2_b32 s68, s68, s67\n\t"
    "s_cbranch_scc1 FB_%=\n\t"
    "v_pk_mul_f32 v[8:9], v[8:9], v[128:129] op_sel_hi:[1,0]\n\t"
    "v_pk_mul_f32 v[10:11], v[10:11], v[128:129] op_sel_hi:[1,0]\n\t"
    "v_pk_mul_f32 v[24:25], v[24:25], v[128:129] op_sel_hi:[1,0]\n\t"
    "v_pk_mul_f32 v[26:27], v[26:27], v[128:129] op_sel_hi:[1,0]\n\t"
    "s_waitcnt vmcnt(7)\n\t"
    "v_lshlrev_b32 v16, 16, v0\n\t"
    "v_and_b32 v17, 0xffff0000, v0\n\t"
    "v_lshlrev_b32 v18, 16, v1\n\t"
    "v_and_b32 v19, 0xffff0000, v1\n\t"
    "v_pk_fma_f32 v[8:9], v[16:17], v[130:131], v[8:9] op_sel_hi:[1,0,1]\n\t"
    "v_pk_fma_f32 v[10:11], v[18:19], v[130:131], v[10:11] op_sel_hi:[1,0,1]\n\t"
    "v_cvt_pk_bf16_f32 v0, v8, v9\n\t"
    "v_cvt_pk_bf16_f32 v1, v10, v11\n\t"
    "v_lshlrev_b32 v16, 16, v2\n\t"
    "v_and_b32 v17, 0xffff0000, v2\n\t"
    "v_lshlrev_b32 v18, 16, v3\n\t"
    "v_and_b32 v19, 0xffff0000, v3\n\t"
    "v_pk_fma_f32 v[24:25], v[16:17], v[130:131], v[24:25] op_sel_hi:[1,0,1]\n\t"
    "v_pk_fma_f32 v[26:27], v[18:19], v[130:131], v[26:27] op_sel_hi:[1,0,1]\n\t"
    "v_cvt_pk_bf16_f32 v2, v24, v25\n\t"
    "v_cvt_pk_bf16_f32 v3, v26, v27\n\t"
    "ds_write_b64 v196, v[0:1] offset:0\n\t"
    "ds_write_b64 v196, v[2:3] offset:32\n\t"
    "v_pk_mul_f32 v[40:41], v[40:41], v[128:129] op_sel_hi:[1,0]\n\t"
    "v_pk_mul_f32 v[42:43], v[42:43], v[128:129] op_sel_hi:[1,0]\n\t"
    "v_pk_mul_f32 v[56:57], v[56:57], v[128:129] op_sel_hi:[1,0]\n\t"
    "v_pk_mul_f32 v[58:59], v[58:59], v[128:129] op_sel_hi:[1,0]\n\t"
    "s_waitcnt vmcnt(6)\n\t"
    "v_lshlrev_b32 v48, 16, v32\n\t"
    "v_and_b32 v49, 0xffff0000, v32\n\t"
    "v_lshlrev_b32 v50, 16, v33\n\t"
    "v_and_b32 v51, 0xffff0000, v33\n\t"
    "v_pk_fma_f32 v[40:41], v[48:49], v[130:131], v[40:41] op_sel_hi:[1,0,1]\n\t"
    "v_pk_fma_f32 v[42:43], v[50:51], v[130:131], v[42:43] op_sel_hi:[1,0,1]\n\t"
    "v_cvt_pk_bf16_f32 v32, v40, v41\n\t"
    "v_cvt_pk_bf16_f32 v33, v42, v43\n\t"
    "v_lshlrev_b32 v48, 16, v34\n\t"
    "v_and_b32 v49, 0xffff0000, v34\n\t"
    "v_lshlrev_b32 v50, 16, v35\n\t"
    "v_and_b32 v51, 0xffff0000, v35\n\t"
    "v_pk_fma_f32 v[56:57], v[48:49], v[130:131], v[56:57] op_sel_hi:[1,0,1]\n\t"
    "v_pk_fma_f32 v[58:59], v[50:51], v[130:131], v[58:59] op_sel_hi:[1,0,1]\n\t"
    "v_cvt_pk_bf16_f32 v34, v56, v57\n\t"
    "v_cvt_pk_bf16_f32 v35, v58, v59\n\t"
    "ds_write_b64 v196, v[32:33] offset:64\n\t"
    "ds_write_b64 v196, v[34:35] offset:96\n\t"
    "v_pk_mul_f32 v[72:73], v[72:73], v[128:129] op_sel_hi:[1,0]\n\t"
    "v_pk_mul_f32 v[74:75], v[74:75], v[128:129] op_sel_hi:[1,0]\n\t"
    "v_pk_mul_f32 v[88:89], v[88:89], v[128:129] op_sel_hi:[1,0]\n\t"
    "v_pk_mul_f32 v[90:91], v[90:91], v[128:129] op_sel_hi:[1,0]\n\t"
    "s_waitcnt vmcnt(5)\n\t"
    "v_lshlrev_b32 v80, 16, v64\n\t"
    "v_and_b32 v81, 0xffff0000, v64\n\t"
    "v_lshlrev_b32 v82, 16, v65\n\t"
    "v_and_b32 v83, 0xffff0000, v65\n\t"
    "v_pk_fma_f32 v[72:73], v[80:81], v[130:131], v[72:73] op_sel_hi:[1,0,1]\n\t"
    "v_pk_fma_f32 v[74:75], v[82:83], v[130:131], v[74:75] op_sel_hi:[1,0,1]\n\t"
    "v_cvt_pk_bf16_f32 v64, v72, v73\n\t"
    "v_cvt_pk_bf16_f32 v65, v74, v75\n\t"
    "v_lshlrev_b32 v80, 16, v66\n\t"
    "v_and_b32 v81, 0xffff0000, v66\n\t"
    "v_lshlrev_b32 v82, 16, v67\n\t"
    "v_and_b32 v83, 0xffff0000, v67\n\t"
    "v_pk_fma_f32 v[88:89], v[80:81], v[130:131], v[88:89] op_sel_hi:[1,0,1]\n\t"
    "v_pk_fma_f32 v[90:91], v[82:83], v[130:131], v[90:91] op_sel_hi:[1,0,1]\n\t"
    "v_cvt_pk_bf16_f32 v66, v88, v89\n\t"
    "v_cvt_pk_bf16_f32 v67, v90, v91\n\t"
    "ds_write_b64 v196, v[64:65] offset:128\n\t"
    "ds_write_b64 v196, v[66:67] offset:160\n\t"
    "v_pk_mul_f32 v[104:105], v[104:105], v[128:129] op_sel_hi:[1,0]\n\t"
    "v_pk_mul_f32 v[106:107], v[106:107], v[128:129] op_sel_hi:[1,0]\n\t"
    "v_pk_mul_f32 v[120:121], v[120:121], v[128:129] op_sel_hi:[1,0]\n\t"
    "v_pk_mul_f32 v[122:123], v[122:123], v[128:129] op_sel_hi:[1,0]\n\t"
    "s_waitcnt vmcnt(4)\n\t"
    "v_lshlrev_b32 v112, 16, v96\n\t"
    "v_and_b32 v113, 0xffff0000, v96\n\t"
    "v_lshlrev_b32 v114, 16, v97\n\t"
    "v_and_b32 v115, 0xffff0000, v97\n\t"
    "v_pk_fma_f32 v[104:105], v[112:113], v[130:131], v[104:105] op_sel_hi:[1,0,1]\n\t"
    "v_pk_fma_f32 v[106:107], v[114:115], v[130:131], v[106:107] op_sel_hi:[1,0,1]\n\t"
    "v_cvt_pk_bf16_f32 v96, v104, v105\n\t"
    "v_cvt_pk_bf16_f32 v97, v106, v107\n\t"
    "v_lshlrev_b32 v112, 16, v98\n\t"
    "v_and_b32 v113, 0xffff0000, v98\n\t"
    "v_lshlrev_b32 v114, 16, v99\n\t"
    "v_and_b32 v115, 0xffff0000, v99\n\t"
    "v_pk_fma_f32 v[120:121], v[112:113], v[130:131], v[120:121] op_sel_hi:[1,0,1]\n\t"
    "v_pk_fma_f32 v[122:123], v[114:115], v[130:131], v[122:123] op_sel_hi:[1,0,1]\n\t"
    "v_cvt_pk_bf16_f32 v98, v120, v121\n\t"
    "v_cvt_pk_bf16_f32 v99, v122, v123\n\t"
    "ds_write_b64 v196, v[96:97] offset:192\n\t"
    "ds_write_b64 v196, v[98:99] offset:224\n\t"
    "ds_read_b128 v[8:11], v246 offset:0\n\t"
    "ds_read_b128 v[40:43], v246 offset:1088\n\t"
    "ds_read_b128 v[72:75], v246 offset:2176\n\t"
    "ds_read_b128 v[104:107], v246 offset:3264\n\t"
    "v_pk_mul_f32 v[12:13], v[12:13], v[132:133] op_sel_hi:[1,0]\n\t"
    "v_pk_mul_f32 v[14:15], v[14:15], v[132:133] op_sel_hi:[1,0]\n\t"
    "v_pk_mul_f32 v[28:29], v[28:29], v[132:133] op_sel_hi:[1,0]\n\t"
    "v_pk_mul_f32 v[30:31], v[30:31], v[132:133] op_sel_hi:[1,0]\n\t"
    "s_waitcnt vmcnt(3)\n\t"
    "v_lshlrev_b32 v20, 16, v4\n\t"
    "v_and_b32 v21, 0xffff0000, v4\n\t"
    "v_lshlrev_b32 v22, 16, v5\n\t"
    "v_and_b32 v23, 0xffff0000, v5\n\t"
    "v_pk_fma_f32 v[12:13], v[20:21], v[134:135], v[12:13] op_sel_hi:[1,0,1]\n\t"
    "v_pk_fma_f32 v[14:15], v[22:23], v[134:135], v[14:15] op_sel_hi:[1,0,1]\n\t"
    "v_cvt_pk_bf16_f32 v4, v12, v13\n\t"
    "v_cvt_pk_bf16_f32 v5, v14, v15\n\t"
    "v_lshlrev_b32 v20, 16, v6\n\t"
    "v_and_b32 v21, 0xffff0000, v6\n\t"
    "v_lshlrev_b32 v22, 16, v7\n\t"
    "v_and_b32 v23, 0xffff0000, v7\n\t"
    "v_pk_fma_f32 v[28:29], v[20:21], v[134:135], v[28:29] op_sel_hi:[1,0,1]\n\t"
    "v_pk_fma_f32 v[30:31], v[22:23], v[134:135], v[30:31] op_sel_hi:[1,0,1]\n\t"
    "v_cvt_pk_bf16_f32 v6, v28, v29\n\t"
    "v_cvt_pk_bf16_f32 v7, v30, v31\n\t"
    "ds_write_b64 v196, v[4:5] offset:4352\n\t"
    "ds_write_b64 v196, v[6:7] offset:4384\n\t"
    "s_waitcnt lgkmcnt(2)\n\t"
    "buffer_store_dwordx4 v[8:11], v248, s[56:59], s89 offen offset:0 sc1\n\t"
    "buffer_store_dwordx4 v[40:43], v248, s[56:59], s89 offen offset:1024 sc1\n\t"
    "buffer_store_dwordx4 v[72:75], v248, s[56:59], s89 offen offset:2048 sc1\n\t"
    "buffer_store_dwordx4 v[104:107], v248, s[56:59], s89 offen offset:3072 sc1\n\t"
    "v_pk_mul_f32 v[44:45], v[44:45], v[132:133] op_sel_hi:[1,0]\n\t"
    "v_pk_mul_f32 v[46:47], v[46:47], v[132:133] op_sel_hi:[1,0]\n\t"
    "v_pk_mul_f32 v[60:61], v[60:61], v[132:133] op_sel_hi:[1,0]\n\t"
    "v_pk_mul_f32 v[62:63], v[62:63], v[132:133] op_sel_hi:[1,0]\n\t"
    "s_waitcnt vmcnt(6)\n\t"
    "v_lshlrev_b32 v52, 16, v36\n\t"
    "v_and_b32 v53, 0xffff0000, v36\n\t"
    "v_lshlrev_b32 v54, 16, v37\n\t"
    "v_and_b32 v55, 0xffff0000, v37\n\t"
    "v_pk_fma_f32 v[44:45], v[52:53], v[134:135], v[44:45] op_sel_hi:[1,0,1]\n\t"
    "v_pk_fma_f32 v[46:47], v[54:55], v[134:135], v[46:47] op_sel_hi:[1,0,1]\n\t"
    "v_cvt_pk_bf16_f32 v36, v44, v45\n\t"
    "v_cvt_pk_bf16_f32 v37, v46, v47\n\t"
    "v_lshlrev_b32 v52, 16, v38\n\t"
    "v_and_b32 v53, 0xffff0000, v38\n\t"
    "v_lshlrev_b32 v54, 16, v39\n\t"
    "v_and_b32 v55, 0xffff0000, v39\n\t"
    "v_pk_fma_f32 v[60:61], v[52:53], v[134:135], v[60:61] op_sel_hi:[1,0,1]\n\t"
    "v_pk_fma_f32 v[62:63], v[54:55], v[134:135], v[62:63] op_sel_hi:[1,0,1]\n\t"
    "v_cvt_pk_bf16_f32 v38, v60, v61\n\t"
    "v_cvt_pk_bf16_f32 v39, v62, v63\n\t"
    "ds_write_b64 v196, v[36:37] offset:4416\n\t"
    "ds_write_b64 v196, v[38:39] offset:4448\n\t"
    "v_pk_mul_f32 v[76:77], v[76:77], v[132:133] op_sel_hi:[1,0]\n\t"
    "v_pk_mul_f32 v[78:79], v[78:79], v[132:133] op_sel_hi:[1,0]\n\t"
    "v_pk_mul_f32 v[92:93], v[92:93], v[132:133] op_sel_hi:[1,0]\n\t"
    "v_pk_mul_f32 v[94:95], v[94:95], v[132:133] op_sel_hi:[1,0]\n\t"
    "s_waitcnt vmcnt(5)\n\t"
    "v_lshlrev_b32 v84, 16, v68\n\t"
    "v_and_b32 v85, 0xffff0000, v68\n\t"
    "v_lshlrev_b32 v86, 16, v69\n\t"
    "v_and_b32 v87, 0xffff0000, v69\n\t"
    "v_pk_fma_f32 v[76:77], v[84:85], v[134:135], v[76:77] op_sel_hi:[1,0,1]\n\t"
    "v_pk_fma_f32 v[78:79], v[86:87], v[134:135], v[78:79] op_sel_hi:[1,0,1]\n\t"
    "v_cvt_pk_bf16_f32 v68, v76, v77\n\t"
    "v_cvt_pk_bf16_f32 v69, v78, v79\n\t"
    "v_lshlrev_b32 v84, 16, v70\n\t"
    "v_and_b32 v85, 0xffff0000, v70\n\t"
    "v_lshlrev_b32 v86, 16, v71\n\t"
    "v_and_b32 v87, 0xffff0000, v71\n\t"
    "v_pk_fma_f32 v[92:93], v[84:85], v[134:135], v[92:93] op_sel_hi:[1,0,1]\n\t"
    "v_pk_fma_f32 v[94:95], v[86:87], v[134:135], v[94:95] op_sel_hi:[1,0,1]\n\t"
    "v_cvt_pk_bf16_f32 v70, v92, v93\n\t"
    "v_cvt_pk_bf16_f32 v71, v94, v95\n\t"
    "ds_write_b64 v196, v[68:69] offset:4480\n\t"
    "ds_write_b64 v196, v[70:71] offset:4512\n\t"
    "v_pk_mul_f32 v[108:109], v[108:109], v[132:133] op_sel_hi:[1,0]\n\t"
    "v_pk_mul_f32 v[110:111], v[110:111], v[132:133] op_sel_hi:[1,0]\n\t"
    "v_pk_mul_f32 v[124:125], v[124:125], v[132:133] op_sel_hi:[1,0]\n\t"
    "v_pk_mul_f32 v[126:127], v[126:127], v[132:133] op_sel_hi:[1,0]\n\t"
    "s_waitcnt vmcnt(4)\n\t"
    "v_lshlrev_b32 v116, 16, v100\n\t"
    "v_and_b32 v117, 0xffff0000, v100\n\t"
    "v_lshlrev_b32 v118, 16, v101\n\t"
    "v_and_b32 v119, 0xffff0000, v101\n\t"
    "v_pk_fma_f32 v[108:109], v[116:117], v[134:135], v[108:109] op_sel_hi:[1,0,1]\n\t"
    "v_pk_fma_f32 v[110:111], v[118:119], v[134:135], v[110:111] op_sel_hi:[1,0,1]\n\t"
    "v_cvt_pk_bf16_f32 v100, v108, v109\n\t"
    "v_cvt_pk_bf16_f32 v101, v110, v111\n\t"
    "v_lshlrev_b32 v116, 16, v102\n\t"
    "v_and_b32 v117, 0xffff0000, v102\n\t"
    "v_lshlrev_b32 v118, 16, v103\n\t"
    "v_and_b32 v119, 0xffff0000, v103\n\t"
    "v_pk_fma_f32 v[124:125], v[116:117], v[134:135], v[124:125] op_sel_hi:[1,0,1]\n\t"
    "v_pk_fma_f32 v[126:127], v[118:119], v[134:135], v[126:127] op_sel_hi:[1,0,1]\n\t"
    "v_cvt_pk_bf16_f32 v102, v124, v125\n\t"
    "v_cvt_pk_bf16_f32 v103, v126, v127\n\t"
    "ds_write_b64 v196, v[100:101] offset:4544\n\t"
    "ds_write_b64 v196, v[102:103] offset:4576\n\t"
    "ds_read_b128 v[12:15], v246 offset:4352\n\t"
    "ds_read_b128 v[44:47], v246 offset:5440\n\t"
    "ds_read_b128 v[76:79], v246 offset:6528\n\t"
    "ds_read_b128 v[108:111], v246 offset:7616\n\t"
    "s_waitcnt lgkmcnt(3)\n\t"
    "buffer_store_dwordx4 v[12:15], v248, s[56:59], s71 offen offset:0 sc1\n\t"
    "s_waitcnt lgkmcnt(2)\n\t"
    "buffer_store_dwordx4 v[44:47], v248, s[56:59], s71 offen offset:1024 sc1\n\t"
    "s_waitcnt lgkmcnt(1)\n\t"
    "buffer_store_dwordx4 v[76:79], v248, s[56:59], s71 offen offset:2048 sc1\n\t"
    "s_waitcnt lgkmcnt(0)\n\t"
    "buffer_store_dwordx4 v[108:111], v248, s[56:59], s71 offen offset:3072 sc1\n\t"
    "buffer_store_dword v252, off, s[48:51], s77 sc1\n\t"
    "s_endpgm\n\t"
    "TB_%=:\n\t"
    "v_mfma_f32_16x16x32_bf16 v[128:131], a[64:67], a[32:35], v[168:171]\n\t"
    "ds_read_b64_tr_b16 a[208:209], v197 offset:16640\n\t"
    "ds_read_b64_tr_b16 a[210:211], v197 offset:20736\n\t"
    "v_mfma_f32_16x16x32_bf16 v[128:131], a[68:71], a[36:39], v[128:131]\n\t"
    "v_exp_f32 v140, v140\n\t"
    "v_mfma_f32_16x16x32_bf16 v[128:131], a[72:75], a[40:43], v[128:131]\n\t"
    "ds_read_b64_tr_b16 a[212:213], v197 offset:24832\n\t"
    "ds_read_b64_tr_b16 a[214:215], v197 offset:28928\n\t"
    "v_mfma_f32_16x16x32_bf16 v[128:131], a[76:79], a[44:47], v[128:131]\n\t"
    "v_exp_f32 v141, v141\n\t"
    "v_mfma_f32_16x16x32_bf16 v[132:135], a[92:95], a[44:47], v[168:171]\n\t"
    "ds_read_b64_tr_b16 a[216:217], v198 offset:16640\n\t"
    "v_cvt_pk_bf16_f32 v158, v140, v141\n\t"
    "v_mfma_f32_16x16x32_bf16 v[132:135], a[88:91], a[40:43], v[132:135]\n\t"
    "v_exp_f32 v142, v142\n\t"
    "v_mfma_f32_16x16x32_bf16 v[132:135], a[84:87], a[36:39], v[132:135]\n\t"
    "ds_read_b64_tr_b16 a[218:219], v198 offset:20736\n\t"
    "ds_read_b64_tr_b16 a[220:221], v198 offset:24832\n\t"
    "v_mfma_f32_16x16x32_bf16 v[132:135], a[80:83], a[32:35], v[132:135]\n\t"
    "v_exp_f32 v143, v143\n\t"
    "v_mfma_f32_16x16x32_bf16 v[136:139], a[96:99], a[32:35], v[168:171]\n\t"
    "ds_read_b64_tr_b16 a[222:223], v198 offset:28928\n\t"
    "v_cvt_pk_bf16_f32 v159, v142, v143\n\t"
    "v_mfma_f32_16x16x32_bf16 v[136:139], a[100:103], a[36:39], v[136:139]\n\t"
    "v_exp_f32 v128, v128\n\t"
    "v_mfma_f32_16x16x32_bf16 v[136:139], a[104:107], a[40:43], v[136:139]\n\t"
    "ds_read_b64_tr_b16 a[224:225], v197 offset:16896\n\t"
    "ds_read_b64_tr_b16 a[226:227], v197 offset:20992\n\t"
    "v_mfma_f32_16x16x32_bf16 v[136:139], a[108:111], a[44:47], v[136:139]\n\t"
    "v_exp_f32 v129, v129\n\t"
    "v_mfma_f32_16x16x32_bf16 v[140:143], a[124:127], a[44:47], v[168:171]\n\t"
    "ds_read_b64_tr_b16 a[228:229], v197 offset:25088\n\t"
    "v_cvt_pk_bf16_f32 v144, v128, v129\n\t"
    "v_mfma_f32_16x16x32_bf16 v[140:143], a[120:123], a[40:43], v[140:143]\n\t"
    "v_exp_f32 v130, v130\n\t"
    "v_mfma_f32_16x16x32_bf16 v[140:143], a[116:119], a[36:39], v[140:143]\n\t"
    "ds_read_b64_tr_b16 a[230:231], v197 offset:29184\n\t"
    "ds_read_b64_tr_b16 a[232:233], v198 offset:16896\n\t"
    "v_mfma_f32_16x16x32_bf16 v[140:143], a[112:115], a[32:35], v[140:143]\n\t"
    "v_exp_f32 v131, v131\n\t"
    "v_mfma_f32_16x16x32_bf16 v[12:15], a[128:131], v[152:155], v[12:15]\n\t"
    "ds_read_b64_tr_b16 a[234:235], v198 offset:20992\n\t"
    "v_cvt_pk_bf16_f32 v145, v130, v131\n\t"
    "v_mfma_f32_16x16x32_bf16 v[12:15], a[132:135], v[156:159], v[12:15]\n\t"
    "v_exp_f32 v132, v132\n\t"
    "v_mfma_f32_16x16x32_bf16 v[28:31], a[140:143], v[156:159], v[28:31]\n\t"
    "ds_read_b64_tr_b16 a[236:237], v198 offset:25088\n\t"
    "ds_read_b64_tr_b16 a[238:239], v198 offset:29184\n\t"
    "v_mfma_f32_16x16x32_bf16 v[28:31], a[136:139], v[152:155], v[28:31]\n\t"
    "v_exp_f32 v133, v133\n\t"
    "v_mfma_f32_16x16x32_bf16 v[44:47], a[144:147], v[152:155], v[44:47]\n\t"
    "ds_read_b64_tr_b16 a[240:241], v197 offset:17152\n\t"
    "v_cvt_pk_bf16_f32 v146, v132, v133\n\t"
    "v_mfma_f32_16x16x32_bf16 v[44:47], a[148:151], v[156:159], v[44:47]\n\t"
    "v_exp_f32 v134, v134\n\t"
    "v_mfma_f32_16x16x32_bf16 v[60:63], a[156:159], v[156:159], v[60:63]\n\t"
    "ds_read_b64_tr_b16 a[242:243], v197 offset:21248\n\t"
    "ds_read_b64_tr_b16 a[244:245], v197 offset:25344\n\t"
    "v_mfma_f32_16x16x32_bf16 v[60:63], a[152:155], v[152:155], v[60:63]\n\t"
    "v_exp_f32 v135, v135\n\t"
    "v_mfma_f32_16x16x32_bf16 v[76:79], a[160:163], v[152:155], v[76:79]\n\t"
    "ds_read_b64_tr_b16 a[246:247], v197 offset:29440\n\t"
    "v_cvt_pk_bf16_f32 v147, v134, v135\n\t"
    "v_mfma_f32_16x16x32_bf16 v[76:79], a[164:167], v[156:159], v[76:79]\n\t"
    "v_exp_f32 v136, v136\n\t"
    "v_mfma_f32_16x16x32_bf16 v[92:95], a[172:175], v[156:159], v[92:95]\n\t"
    "ds_read_b64_tr_b16 a[248:249], v198 offset:17152\n\t"
    "ds_read_b64_tr_b16 a[250:251], v198 offset:21248\n\t"
    "v_mfma_f32_16x16x32_bf16 v[92:95], a[168:171], v[152:155], v[92:95]\n\t"
    "v_exp_f32 v137, v137\n\t"
    "v_mfma_f32_16x16x32_bf16 v[108:111], a[176:179], v[152:155], v[108:111]\n\t"
    "ds_read_b64_tr_b16 a[252:253], v198 offset:25344\n\t"
    "v_cvt_pk_bf16_f32 v148, v136, v137\n\t"
    "v_mfma_f32_16x16x32_bf16 v[108:111], a[180:183], v[156:159], v[108:111]\n\t"
    "v_exp_f32 v138, v138\n\t"
    "v_mfma_f32_16x16x32_bf16 v[124:127], a[188:191], v[156:159], v[124:127]\n\t"
    "s_mov_b64 s[36:37], s[60:61]\n\t"
    "s_mov_b64 s[40:41], s[62:63]\n\t"
    "s_mov_b32 s38, s79\n\t"
    "s_mov_b32 s42, s79\n\t"
    "ds_read_b64_tr_b16 a[254:255], v198 offset:29440\n\t"
    "v_mfma_f32_16x16x32_bf16 v[124:127], a[184:187], v[152:155], v[124:127]\n\t"
    "v_exp_f32 v139, v139\n\t"
    "v_smfmac_f32_16x16x64_bf16 v[188:191], v[192:195], v[152:159], v245\n\t"
    "s_waitcnt vmcnt(8)\n\t"
    "s_barrier\n\t"
    "v_cvt_pk_bf16_f32 v149, v138, v139\n\t"
    "v_mfma_f32_16x16x32_bf16 v[128:131], a[64:67], a[48:51], v[172:175]\n\t"
    "ds_read_b128 a[64:67], v196 offset:32768\n\t"
    "v_mfma_f32_16x16x32_bf16 v[128:131], a[68:71], a[52:55], v[128:131]\n\t"
    "v_exp_f32 v140, v140\n\t"
    "v_mfma_f32_16x16x32_bf16 v[128:131], a[72:75], a[56:59], v[128:131]\n\t"
    "ds_read_b128 a[68:71], v196 offset:33024\n\t"
    "ds_read_b128 a[72:75], v196 offset:33280\n\t"
    "v_mfma_f32_16x16x32_bf16 v[128:131], a[76:79], a[60:63], v[128:131]\n\t"
    "v_exp_f32 v141, v141\n\t"
    "v_mfma_f32_16x16x32_bf16 v[132:135], a[92:95], a[60:63], v[172:175]\n\t"
    "ds_read_b128 a[76:79], v196 offset:33536\n\t"
    "v_cvt_pk_bf16_f32 v150, v140, v141\n\t"
    "v_mfma_f32_16x16x32_bf16 v[132:135], a[88:91], a[56:59], v[132:135]\n\t"
    "v_exp_f32 v142, v142\n\t"
    "v_mfma_f32_16x16x32_bf16 v[132:135], a[84:87], a[52:55], v[132:135]\n\t"
    "ds_read_b128 a[92:95], v196 offset:37632\n\t"
    "ds_read_b128 a[88:91], v196 offset:37376\n\t"
    "v_mfma_f32_16x16x32_bf16 v[132:135], a[80:83], a[48:51], v[132:135]\n\t"
    "v_exp_f32 v143, v143\n\t"
    "v_mfma_f32_16x16x32_bf16 v[136:139], a[96:99], a[48:51], v[172:175]\n\t"
    "ds_read_b128 a[84:87], v196 offset:37120\n\t"
    "v_cvt_pk_bf16_f32 v151, v142, v143\n\t"
    "v_mfma_f32_16x16x32_bf16 v[136:139], a[100:103], a[52:55], v[136:139]\n\t"
    "v_exp_f32 v128, v128\n\t"
    "v_mfma_f32_16x16x32_bf16 v[136:139], a[104:107], a[56:59], v[136:139]\n\t"
    "ds_read_b128 a[80:83], v196 offset:36864\n\t"
    "ds_read_b128 a[96:99], v196 offset:40960\n\t"
    "v_mfma_f32_16x16x32_bf16 v[136:139], a[108:111], a[60:63], v[136:139]\n\t"
    "v_exp_f32 v129, v129\n\t"
    "v_mfma_f32_16x16x32_bf16 v[140:143], a[124:127], a[60:63], v[172:175]\n\t"
    "ds_read_b128 a[100:103], v196 offset:41216\n\t"
    "v_cvt_pk_bf16_f32 v152, v128, v129\n\t"
    "v_mfma_f32_16x16x32_bf16 v[140:143], a[120:123], a[56:59], v[140:143]\n\t"
    "s_waitcnt lgkmcnt(10)\n\t"
    "v_exp_f32 v130, v130\n\t"
    "v_mfma_f32_16x16x32_bf16 v[140:143], a[116:119], a[52:55], v[140:143]\n\t"
    "ds_read_b128 a[104:107], v196 offset:41472\n\t"
    "ds_read_b128 a[108:111], v196 offset:41728\n\t"
    "v_mfma_f32_16x16x32_bf16 v[140:143], a[112:115], a[48:51], v[140:143]\n\t"
    "v_exp_f32 v131, v131\n\t"
    "v_mfma_f32_16x16x32_bf16 v[8:11], a[192:195], v[144:147], v[8:11]\n\t"
    "ds_read_b128 a[124:127], v196 offset:45824\n\t"
    "v_cvt_pk_bf16_f32 v153, v130, v131\n\t"
    "v_mfma_f32_16x16x32_bf16 v[8:11], a[196:199], v[148:151], v[8:11]\n\t"
    "v_exp_f32 v132, v132\n\t"
    "v_mfma_f32_16x16x32_bf16 v[24:27], a[204:207], v[148:151], v[24:27]\n\t"
    "ds_read_b128 a[120:123], v196 offset:45568\n\t"
    "ds_read_b128 a[116:119], v196 offset:45312\n\t"
    "v_mfma_f32_16x16x32_bf16 v[24:27], a[200:203], v[144:147], v[24:27]\n\t"
    "v_exp_f32 v133, v133\n\t"
    "v_mfma_f32_16x16x32_bf16 v[40:43], a[208:211], v[144:147], v[40:43]\n\t"
    "ds_read_b128 a[112:115], v196 offset:45056\n\t"
    "v_cvt_pk_bf16_f32 v154, v132, v133\n\t"
    "v_mfma_f32_16x16x32_bf16 v[40:43], a[212:215], v[148:151], v[40:43]\n\t"
    "v_exp_f32 v134, v134\n\t"
    "v_mfma_f32_16x16x32_bf16 v[56:59], a[220:223], v[148:151], v[56:59]\n\t"
    "ds_read_b64_tr_b16 a[128:129], v197 offset:32768\n\t"
    "ds_read_b64_tr_b16 a[130:131], v197 offset:36864\n\t"
    "v_mfma_f32_16x16x32_bf16 v[56:59], a[216:219], v[144:147], v[56:59]\n\t"
    "v_exp_f32 v135, v135\n\t"
    "v_mfma_f32_16x16x32_bf16 v[72:75], a[224:227], v[144:147], v[72:75]\n\t"
    "ds_read_b64_tr_b16 a[132:133], v197 offset:40960\n\t"
    "v_cvt_pk_bf16_f32 v155, v134, v135\n\t"
    "v_mfma_f32_16x16x32_bf16 v[72:75], a[228:231], v[148:151], v[72:75]\n\t"
    "v_exp_f32 v136, v136\n\t"
    "v_mfma_f32_16x16x32_bf16 v[88:91], a[236:239], v[148:151], v[88:91]\n\t"
    "ds_read_b64_tr_b16 a[134:135], v197 offset:45056\n\t"
    "ds_read_b64_tr_b16 a[136:137], v198 offset:32768\n\t"
    "v_mfma_f32_16x16x32_bf16 v[88:91], a[232:235], v[144:147], v[88:91]\n\t"
    "v_exp_f32 v137, v137\n\t"
    "v_mfma_f32_16x16x32_bf16 v[104:107], a[240:243], v[144:147], v[104:107]\n\t"
    "ds_read_b64_tr_b16 a[138:139], v198 offset:36864\n\t"
    "v_cvt_pk_bf16_f32 v156, v136, v137\n\t"
    "v_mfma_f32_16x16x32_bf16 v[104:107], a[244:247], v[148:151], v[104:107]\n\t"
    "v_exp_f32 v138, v138\n\t"
    "v_mfma_f32_16x16x32_bf16 v[120:123], a[252:255], v[148:151], v[120:123]\n\t"
    "ds_read_b64_tr_b16 a[140:141], v198 offset:40960\n\t"
    "ds_read_b64_tr_b16 a[142:143], v198 offset:45056\n\t"
    "v_mfma_f32_16x16x32_bf16 v[120:123], a[248:251], v[144:147], v[120:123]\n\t"
    "v_exp_f32 v139, v139\n\t"
    "v_smfmac_f32_16x16x64_bf16 v[184:187], v[192:195], v[144:151], v245\n\t"
    "s_waitcnt lgkmcnt(8)\n\t"
    "v_cvt_pk_bf16_f32 v157, v138, v139\n\t"
    "v_mfma_f32_16x16x32_bf16 v[128:131], a[64:67], a[32:35], v[168:171]\n\t"
    "ds_read_b64_tr_b16 a[144:145], v197 offset:33024\n\t"
    "ds_read_b64_tr_b16 a[146:147], v197 offset:37120\n\t"
    "v_mfma_f32_16x16x32_bf16 v[128:131], a[68:71], a[36:39], v[128:131]\n\t"
    "s_add_u32 m0, s64, 0\n\t"
    "v_exp_f32 v140, v140\n\t"
    "v_mfma_f32_16x16x32_bf16 v[128:131], a[72:75], a[40:43], v[128:131]\n\t"
    "buffer_load_dwordx4 v199, s[36:39], s64 offen offset:0 nt lds\n\t"
    "ds_read_b64_tr_b16 a[148:149], v197 offset:41216\n\t"
    "v_mfma_f32_16x16x32_bf16 v[128:131], a[76:79], a[44:47], v[128:131]\n\t"
    "v_exp_f32 v141, v141\n\t"
    "v_mfma_f32_16x16x32_bf16 v[132:135], a[92:95], a[44:47], v[168:171]\n\t"
    "ds_read_b64_tr_b16 a[150:151], v197 offset:45312\n\t"
    "v_cvt_pk_bf16_f32 v158, v140, v141\n\t"
    "v_mfma_f32_16x16x32_bf16 v[132:135], a[88:91], a[40:43], v[132:135]\n\t"
    "v_exp_f32 v142, v142\n\t"
    "v_mfma_f32_16x16x32_bf16 v[132:135], a[84:87], a[36:39], v[132:135]\n\t"
    "ds_read_b64_tr_b16 a[152:153], v198 offset:33024\n\t"
    "ds_read_b64_tr_b16 a[154:155], v198 offset:37120\n\t"
    "v_mfma_f32_16x16x32_bf16 v[132:135], a[80:83], a[32:35], v[132:135]\n\t"
    "v_exp_f32 v143, v143\n\t"
    "v_mfma_f32_16x16x32_bf16 v[136:139], a[96:99], a[32:35], v[168:171]\n\t"
    "ds_read_b64_tr_b16 a[156:157], v198 offset:41216\n\t"
    "v_cvt_pk_bf16_f32 v159, v142, v143\n\t"
    "v_mfma_f32_16x16x32_bf16 v[136:139], a[100:103], a[36:39], v[136:139]\n\t"
    "v_exp_f32 v128, v128\n\t"
    "v_mfma_f32_16x16x32_bf16 v[136:139], a[104:107], a[40:43], v[136:139]\n\t"
    "buffer_load_dwordx4 v200, s[36:39], s64 offen offset:1024 nt lds\n\t"
    "ds_read_b64_tr_b16 a[158:159], v198 offset:45312\n\t"
    "v_mfma_f32_16x16x32_bf16 v[136:139], a[108:111], a[44:47], v[136:139]\n\t"
    "v_exp_f32 v129, v129\n\t"
    "v_mfma_f32_16x16x32_bf16 v[140:143], a[124:127], a[44:47], v[168:171]\n\t"
    "ds_read_b64_tr_b16 a[160:161], v197 offset:33280\n\t"
    "v_cvt_pk_bf16_f32 v144, v128, v129\n\t"
    "v_mfma_f32_16x16x32_bf16 v[140:143], a[120:123], a[40:43], v[140:143]\n\t"
    "v_exp_f32 v130, v130\n\t"
    "v_mfma_f32_16x16x32_bf16 v[140:143], a[116:119], a[36:39], v[140:143]\n\t"
    "ds_read_b64_tr_b16 a[162:163], v197 offset:37376\n\t"
    "ds_read_b64_tr_b16 a[164:165], v197 offset:41472\n\t"
    "v_mfma_f32_16x16x32_bf16 v[140:143], a[112:115], a[32:35], v[140:143]\n\t"
    "v_exp_f32 v131, v131\n\t"
    "v_mfma_f32_16x16x32_bf16 v[12:15], a[192:195], v[152:155], v[12:15]\n\t"
    "ds_read_b64_tr_b16 a[166:167], v197 offset:45568\n\t"
    "v_cvt_pk_bf16_f32 v145, v130, v131\n\t"
    "v_mfma_f32_16x16x32_bf16 v[12:15], a[196:199], v[156:159], v[12:15]\n\t"
    "v_exp_f32 v132, v132\n\t"
    "v_mfma_f32_16x16x32_bf16 v[28:31], a[204:207], v[156:159], v[28:31]\n\t"
    "buffer_load_dwordx4 v201, s[36:39], s64 offen offset:2048 nt lds\n\t"
    "ds_read_b64_tr_b16 a[168:169], v198 offset:33280\n\t"
    "v_mfma_f32_16x16x32_bf16 v[28:31], a[200:203], v[152:155], v[28:31]\n\t"
    "v_exp_f32 v133, v133\n\t"
    "v_mfma_f32_16x16x32_bf16 v[44:47], a[208:211], v[152:155], v[44:47]\n\t"
    "ds_read_b64_tr_b16 a[170:171], v198 offset:37376\n\t"
    "v_cvt_pk_bf16_f32 v146, v132, v133\n\t"
    "v_mfma_f32_16x16x32_bf16 v[44:47], a[212:215], v[156:159], v[44:47]\n\t"
    "v_exp_f32 v134, v134\n\t"
    "v_mfma_f32_16x16x32_bf16 v[60:63], a[220:223], v[156:159], v[60:63]\n\t"
    "ds_read_b64_tr_b16 a[172:173], v198 offset:41472\n\t"
    "ds_read_b64_tr_b16 a[174:175], v198 offset:45568\n\t"
    "v_mfma_f32_16x16x32_bf16 v[60:63], a[216:219], v[152:155], v[60:63]\n\t"
    "v_exp_f32 v135, v135\n\t"
    "v_mfma_f32_16x16x32_bf16 v[76:79], a[224:227], v[152:155], v[76:79]\n\t"
    "ds_read_b64_tr_b16 a[176:177], v197 offset:33536\n\t"
    "v_cvt_pk_bf16_f32 v147, v134, v135\n\t"
    "v_mfma_f32_16x16x32_bf16 v[76:79], a[228:231], v[156:159], v[76:79]\n\t"
    "v_exp_f32 v136, v136\n\t"
    "v_mfma_f32_16x16x32_bf16 v[92:95], a[236:239], v[156:159], v[92:95]\n\t"
    "buffer_load_dwordx4 v202, s[36:39], s64 offen offset:3072 nt lds\n\t"
    "ds_read_b64_tr_b16 a[178:179], v197 offset:37632\n\t"
    "v_mfma_f32_16x16x32_bf16 v[92:95], a[232:235], v[152:155], v[92:95]\n\t"
    "v_exp_f32 v137, v137\n\t"
    "v_mfma_f32_16x16x32_bf16 v[108:111], a[240:243], v[152:155], v[108:111]\n\t"
    "ds_read_b64_tr_b16 a[180:181], v197 offset:41728\n\t"
    "v_cvt_pk_bf16_f32 v148, v136, v137\n\t"
    "v_mfma_f32_16x16x32_bf16 v[108:111], a[244:247], v[156:159], v[108:111]\n\t"
    "v_exp_f32 v138, v138\n\t"
    "v_mfma_f32_16x16x32_bf16 v[124:127], a[252:255], v[156:159], v[124:127]\n\t"
    "ds_read_b64_tr_b16 a[182:183], v197 offset:45824\n\t"
    "ds_read_b64_tr_b16 a[184:185], v198 offset:33536\n\t"
    "ds_read_b64_tr_b16 a[188:189], v198 offset:41728\n\t"
    "ds_read_b64_tr_b16 a[190:191], v198 offset:45824\n\t"
    "v_mfma_f32_16x16x32_bf16 v[124:127], a[248:251], v[152:155], v[124:127]\n\t"
    "v_exp_f32 v139, v139\n\t"
    "v_smfmac_f32_16x16x64_bf16 v[188:191], v[192:195], v[152:159], v245\n\t"
    "ds_read_b64_tr_b16 a[186:187], v198 offset:37632\n\t"
    "s_waitcnt vmcnt(4)\n\t"
    "s_barrier\n\t"
    "v_cvt_pk_bf16_f32 v149, v138, v139\n\t"
    "v_mfma_f32_16x16x32_bf16 v[128:131], a[64:67], a[48:51], v[172:175]\n\t"
    "ds_read_b128 a[64:67], v196 offset:49152\n\t"
    "v_mfma_f32_16x16x32_bf16 v[128:131], a[68:71], a[52:55], v[128:131]\n\t"
    "v_exp_f32 v140, v140\n\t"
    "v_mfma_f32_16x16x32_bf16 v[128:131], a[72:75], a[56:59], v[128:131]\n\t"
    "ds_read_b128 a[68:71], v196 offset:49408\n\t"
    "ds_read_b128 a[72:75], v196 offset:49664\n\t"
    "v_mfma_f32_16x16x32_bf16 v[128:131], a[76:79], a[60:63], v[128:131]\n\t"
    "v_exp_f32 v141, v141\n\t"
    "v_mfma_f32_16x16x32_bf16 v[132:135], a[92:95], a[60:63], v[172:175]\n\t"
    "ds_read_b128 a[76:79], v196 offset:49920\n\t"
    "v_cvt_pk_bf16_f32 v150, v140, v141\n\t"
    "v_mfma_f32_16x16x32_bf16 v[132:135], a[88:91], a[56:59], v[132:135]\n\t"
    "v_exp_f32 v142, v142\n\t"
    "v_mfma_f32_16x16x32_bf16 v[132:135], a[84:87], a[52:55], v[132:135]\n\t"
    "ds_read_b128 a[92:95], v196 offset:54016\n\t"
    "ds_read_b128 a[88:91], v196 offset:53760\n\t"
    "v_mfma_f32_16x16x32_bf16 v[132:135], a[80:83], a[48:51], v[132:135]\n\t"
    "v_exp_f32 v143, v143\n\t"
    "v_mfma_f32_16x16x32_bf16 v[136:139], a[96:99], a[48:51], v[172:175]\n\t"
    "ds_read_b128 a[84:87], v196 offset:53504\n\t"
    "v_cvt_pk_bf16_f32 v151, v142, v143\n\t"
    "v_mfma_f32_16x16x32_bf16 v[136:139], a[100:103], a[52:55], v[136:139]\n\t"
    "v_exp_f32 v128, v128\n\t"
    "v_mfma_f32_16x16x32_bf16 v[136:139], a[104:107], a[56:59], v[136:139]\n\t"
    "ds_read_b128 a[80:83], v196 offset:53248\n\t"
    "ds_read_b128 a[96:99], v196 offset:57344\n\t"
    "v_mfma_f32_16x16x32_bf16 v[136:139], a[108:111], a[60:63], v[136:139]\n\t"
    "v_exp_f32 v129, v129\n\t"
    "v_mfma_f32_16x16x32_bf16 v[140:143], a[124:127], a[60:63], v[172:175]\n\t"
    "ds_read_b128 a[100:103], v196 offset:57600\n\t"
    "v_cvt_pk_bf16_f32 v152, v128, v129\n\t"
    "v_mfma_f32_16x16x32_bf16 v[140:143], a[120:123], a[56:59], v[140:143]\n\t"
    "s_waitcnt lgkmcnt(10)\n\t"
    "v_exp_f32 v130, v130\n\t"
    "v_mfma_f32_16x16x32_bf16 v[140:143], a[116:119], a[52:55], v[140:143]\n\t"
    "ds_read_b128 a[104:107], v196 offset:57856\n\t"
    "ds_read_b128 a[108:111], v196 offset:58112\n\t"
    "v_mfma_f32_16x16x32_bf16 v[140:143], a[112:115], a[48:51], v[140:143]\n\t"
    "v_exp_f32 v131, v131\n\t"
    "v_mfma_f32_16x16x32_bf16 v[8:11], a[128:131], v[144:147], v[8:11]\n\t"
    "ds_read_b128 a[124:127], v196 offset:62208\n\t"
    "v_cvt_pk_bf16_f32 v153, v130, v131\n\t"
    "v_mfma_f32_16x16x32_bf16 v[8:11], a[132:135], v[148:151], v[8:11]\n\t"
    "v_exp_f32 v132, v132\n\t"
    "v_mfma_f32_16x16x32_bf16 v[24:27], a[140:143], v[148:151], v[24:27]\n\t"
    "ds_read_b128 a[120:123], v196 offset:61952\n\t"
    "ds_read_b128 a[116:119], v196 offset:61696\n\t"
    "v_mfma_f32_16x16x32_bf16 v[24:27], a[136:139], v[144:147], v[24:27]\n\t"
    "v_exp_f32 v133, v133\n\t"
    "v_mfma_f32_16x16x32_bf16 v[40:43], a[144:147], v[144:147], v[40:43]\n\t"
    "ds_read_b128 a[112:115], v196 offset:61440\n\t"
    "v_cvt_pk_bf16_f32 v154, v132, v133\n\t"
    "v_mfma_f32_16x16x32_bf16 v[40:43], a[148:151], v[148:151], v[40:43]\n\t"
    "v_exp_f32 v134, v134\n\t"
    "v_mfma_f32_16x16x32_bf16 v[56:59], a[156:159], v[148:151], v[56:59]\n\t"
    "ds_read_b64_tr_b16 a[192:193], v197 offset:49152\n\t"
    "ds_read_b64_tr_b16 a[194:195], v197 offset:53248\n\t"
    "v_mfma_f32_16x16x32_bf16 v[56:59], a[152:155], v[144:147], v[56:59]\n\t"
    "v_exp_f32 v135, v135\n\t"
    "v_mfma_f32_16x16x32_bf16 v[72:75], a[160:163], v[144:147], v[72:75]\n\t"
    "ds_read_b64_tr_b16 a[196:197], v197 offset:57344\n\t"
    "v_cvt_pk_bf16_f32 v155, v134, v135\n\t"
    "v_mfma_f32_16x16x32_bf16 v[72:75], a[164:167], v[148:151], v[72:75]\n\t"
    "v_exp_f32 v136, v136\n\t"
    "v_mfma_f32_16x16x32_bf16 v[88:91], a[172:175], v[148:151], v[88:91]\n\t"
    "ds_read_b64_tr_b16 a[198:199], v197 offset:61440\n\t"
    "ds_read_b64_tr_b16 a[200:201], v198 offset:49152\n\t"
    "v_mfma_f32_16x16x32_bf16 v[88:91], a[168:171], v[144:147], v[88:91]\n\t"
    "v_exp_f32 v137, v137\n\t"
    "v_mfma_f32_16x16x32_bf16 v[104:107], a[176:179], v[144:147], v[104:107]\n\t"
    "ds_read_b64_tr_b16 a[202:203], v198 offset:53248\n\t"
    "v_cvt_pk_bf16_f32 v156, v136, v137\n\t"
    "v_mfma_f32_16x16x32_bf16 v[104:107], a[180:183], v[148:151], v[104:107]\n\t"
    "v_exp_f32 v138, v138\n\t"
    "v_mfma_f32_16x16x32_bf16 v[120:123], a[188:191], v[148:151], v[120:123]\n\t"
    "ds_read_b64_tr_b16 a[204:205], v198 offset:57344\n\t"
    "ds_read_b64_tr_b16 a[206:207], v198 offset:61440\n\t"
    "v_mfma_f32_16x16x32_bf16 v[120:123], a[184:187], v[144:147], v[120:123]\n\t"
    "v_exp_f32 v139, v139\n\t"
    "v_smfmac_f32_16x16x64_bf16 v[184:187], v[192:195], v[144:151], v245\n\t"
    "s_waitcnt lgkmcnt(8)\n\t"
    "v_cvt_pk_bf16_f32 v157, v138, v139\n\t"
    "v_mfma_f32_16x16x32_bf16 v[128:131], a[64:67], a[32:35], v[168:171]\n\t"
    "ds_read_b64_tr_b16 a[208:209], v197 offset:49408\n\t"
    "ds_read_b64_tr_b16 a[210:211], v197 offset:53504\n\t"
    "v_mfma_f32_16x16x32_bf16 v[128:131], a[68:71], a[36:39], v[128:131]\n\t"
    "s_add_u32 m0, s64, 65536\n\t"
    "v_exp_f32 v140, v140\n\t"
    "v_mfma_f32_16x16x32_bf16 v[128:131], a[72:75], a[40:43], v[128:131]\n\t"
    "buffer_load_dwordx4 v199, s[40:43], s64 offen offset:0 nt lds\n\t"
    "ds_read_b64_tr_b16 a[212:213], v197 offset:57600\n\t"
    "v_mfma_f32_16x16x32_bf16 v[128:131], a[76:79], a[44:47], v[128:131]\n\t"
    "v_exp_f32 v141, v141\n\t"
    "v_mfma_f32_16x16x32_bf16 v[132:135], a[92:95], a[44:47], v[168:171]\n\t"
    "ds_read_b64_tr_b16 a[214:215], v197 offset:61696\n\t"
    "v_cvt_pk_bf16_f32 v158, v140, v141\n\t"
    "v_mfma_f32_16x16x32_bf16 v[132:135], a[88:91], a[40:43], v[132:135]\n\t"
    "v_exp_f32 v142, v142\n\t"
    "v_mfma_f32_16x16x32_bf16 v[132:135], a[84:87], a[36:39], v[132:135]\n\t"
    "ds_read_b64_tr_b16 a[216:217], v198 offset:49408\n\t"
    "ds_read_b64_tr_b16 a[218:219], v198 offset:53504\n\t"
    "v_mfma_f32_16x16x32_bf16 v[132:135], a[80:83], a[32:35], v[132:135]\n\t"
    "v_exp_f32 v143, v143\n\t"
    "v_mfma_f32_16x16x32_bf16 v[136:139], a[96:99], a[32:35], v[168:171]\n\t"
    "ds_read_b64_tr_b16 a[220:221], v198 offset:57600\n\t"
    "v_cvt_pk_bf16_f32 v159, v142, v143\n\t"
    "v_mfma_f32_16x16x32_bf16 v[136:139], a[100:103], a[36:39], v[136:139]\n\t"
    "v_exp_f32 v128, v128\n\t"
    "v_mfma_f32_16x16x32_bf16 v[136:139], a[104:107], a[40:43], v[136:139]\n\t"
    "buffer_load_dwordx4 v200, s[40:43], s64 offen offset:1024 nt lds\n\t"
    "ds_read_b64_tr_b16 a[222:223], v198 offset:61696\n\t"
    "v_mfma_f32_16x16x32_bf16 v[136:139], a[108:111], a[44:47], v[136:139]\n\t"
    "v_exp_f32 v129, v129\n\t"
    "v_mfma_f32_16x16x32_bf16 v[140:143], a[124:127], a[44:47], v[168:171]\n\t"
    "ds_read_b64_tr_b16 a[224:225], v197 offset:49664\n\t"
    "v_cvt_pk_bf16_f32 v144, v128, v129\n\t"
    "v_mfma_f32_16x16x32_bf16 v[140:143], a[120:123], a[40:43], v[140:143]\n\t"
    "v_exp_f32 v130, v130\n\t"
    "v_mfma_f32_16x16x32_bf16 v[140:143], a[116:119], a[36:39], v[140:143]\n\t"
    "ds_read_b64_tr_b16 a[226:227], v197 offset:53760\n\t"
    "ds_read_b64_tr_b16 a[228:229], v197 offset:57856\n\t"
    "v_mfma_f32_16x16x32_bf16 v[140:143], a[112:115], a[32:35], v[140:143]\n\t"
    "v_exp_f32 v131, v131\n\t"
    "v_mfma_f32_16x16x32_bf16 v[12:15], a[128:131], v[152:155], v[12:15]\n\t"
    "ds_read_b64_tr_b16 a[230:231], v197 offset:61952\n\t"
    "v_cvt_pk_bf16_f32 v145, v130, v131\n\t"
    "v_mfma_f32_16x16x32_bf16 v[12:15], a[132:135], v[156:159], v[12:15]\n\t"
    "v_exp_f32 v132, v132\n\t"
    "v_mfma_f32_16x16x32_bf16 v[28:31], a[140:143], v[156:159], v[28:31]\n\t"
    "buffer_load_dwordx4 v201, s[40:43], s64 offen offset:2048 nt lds\n\t"
    "ds_read_b64_tr_b16 a[232:233], v198 offset:49664\n\t"
    "v_mfma_f32_16x16x32_bf16 v[28:31], a[136:139], v[152:155], v[28:31]\n\t"
    "v_exp_f32 v133, v133\n\t"
    "v_mfma_f32_16x16x32_bf16 v[44:47], a[144:147], v[152:155], v[44:47]\n\t"
    "ds_read_b64_tr_b16 a[234:235], v198 offset:53760\n\t"
    "v_cvt_pk_bf16_f32 v146, v132, v133\n\t"
    "v_mfma_f32_16x16x32_bf16 v[44:47], a[148:151], v[156:159], v[44:47]\n\t"
    "v_exp_f32 v134, v134\n\t"
    "v_mfma_f32_16x16x32_bf16 v[60:63], a[156:159], v[156:159], v[60:63]\n\t"
    "ds_read_b64_tr_b16 a[236:237], v198 offset:57856\n\t"
    "ds_read_b64_tr_b16 a[238:239], v198 offset:61952\n\t"
    "v_mfma_f32_16x16x32_bf16 v[60:63], a[152:155], v[152:155], v[60:63]\n\t"
    "v_exp_f32 v135, v135\n\t"
    "v_mfma_f32_16x16x32_bf16 v[76:79], a[160:163], v[152:155], v[76:79]\n\t"
    "ds_read_b64_tr_b16 a[240:241], v197 offset:49920\n\t"
    "v_cvt_pk_bf16_f32 v147, v134, v135\n\t"
    "v_mfma_f32_16x16x32_bf16 v[76:79], a[164:167], v[156:159], v[76:79]\n\t"
    "v_exp_f32 v136, v136\n\t"
    "v_mfma_f32_16x16x32_bf16 v[92:95], a[172:175], v[156:159], v[92:95]\n\t"
    "buffer_load_dwordx4 v202, s[40:43], s64 offen offset:3072 nt lds\n\t"
    "ds_read_b64_tr_b16 a[242:243], v197 offset:54016\n\t"
    "v_mfma_f32_16x16x32_bf16 v[92:95], a[168:171], v[152:155], v[92:95]\n\t"
    "v_exp_f32 v137, v137\n\t"
    "v_mfma_f32_16x16x32_bf16 v[108:111], a[176:179], v[152:155], v[108:111]\n\t"
    "ds_read_b64_tr_b16 a[244:245], v197 offset:58112\n\t"
    "v_cvt_pk_bf16_f32 v148, v136, v137\n\t"
    "v_mfma_f32_16x16x32_bf16 v[108:111], a[180:183], v[156:159], v[108:111]\n\t"
    "v_exp_f32 v138, v138\n\t"
    "v_mfma_f32_16x16x32_bf16 v[124:127], a[188:191], v[156:159], v[124:127]\n\t"
    "ds_read_b64_tr_b16 a[246:247], v197 offset:62208\n\t"
    "ds_read_b64_tr_b16 a[248:249], v198 offset:49920\n\t"
    "ds_read_b64_tr_b16 a[252:253], v198 offset:58112\n\t"
    "ds_read_b64_tr_b16 a[254:255], v198 offset:62208\n\t"
    "v_mfma_f32_16x16x32_bf16 v[124:127], a[184:187], v[152:155], v[124:127]\n\t"
    "s_mov_b64 s[44:45], s[48:49]\n\t"
    "s_cmp_eq_u32 s91, 0\n\t"
    "s_cselect_b32 s46, s50, 0\n\t"
    "s_add_u32 s70, s75, 4096\n\t"
    "s_add_u32 s69, s75, 8192\n\t"
    "v_exp_f32 v139, v139\n\t"
    "v_smfmac_f32_16x16x64_bf16 v[188:191], v[192:195], v[152:159], v245\n\t"
    "ds_read_b64_tr_b16 a[250:251], v198 offset:54016\n\t"
    "v_cvt_pk_bf16_f32 v149, v138, v139\n\t"
    "v_mfma_f32_16x16x32_bf16 v[128:131], a[64:67], a[48:51], v[172:175]\n\t"
    "v_lshlrev_b32 v204, 16, v220\n\t"
    "v_and_b32 v205, 0xffff0000, v220\n\t"
    "v_mfma_f32_16x16x32_bf16 v[128:131], a[68:71], a[52:55], v[128:131]\n\t"
    "v_exp_f32 v140, v140\n\t"
    "v_mfma_f32_16x16x32_bf16 v[128:131], a[72:75], a[56:59], v[128:131]\n\t"
    "v_lshlrev_b32 v206, 16, v221\n\t"
    "v_and_b32 v207, 0xffff0000, v221\n\t"
    "v_mfma_f32_16x16x32_bf16 v[128:131], a[76:79], a[60:63], v[128:131]\n\t"
    "v_exp_f32 v141, v141\n\t"
    "v_mfma_f32_16x16x32_bf16 v[132:135], a[92:95], a[60:63], v[172:175]\n\t"
    "v_cvt_pk_bf16_f32 v150, v140, v141\n\t"
    "v_lshlrev_b32 v208, 16, v222\n\t"
    "v_mfma_f32_16x16x32_bf16 v[132:135], a[88:91], a[56:59], v[132:135]\n\t"
    "v_exp_f32 v142, v142\n\t"
    "v_mfma_f32_16x16x32_bf16 v[132:135], a[84:87], a[52:55], v[132:135]\n\t"
    "v_and_b32 v209, 0xffff0000, v222\n\t"
    "v_lshlrev_b32 v210, 16, v223\n\t"
    "v_mfma_f32_16x16x32_bf16 v[132:135], a[80:83], a[48:51], v[132:135]\n\t"
    "v_exp_f32 v143, v143\n\t"
    "v_mfma_f32_16x16x32_bf16 v[136:139], a[96:99], a[48:51], v[172:175]\n\t"
    "v_cvt_pk_bf16_f32 v151, v142, v143\n\t"
    "v_and_b32 v211, 0xffff0000, v223\n\t"
    "v_mfma_f32_16x16x32_bf16 v[136:139], a[100:103], a[52:55], v[136:139]\n\t"
    "v_exp_f32 v128, v128\n\t"
    "v_mfma_f32_16x16x32_bf16 v[136:139], a[104:107], a[56:59], v[136:139]\n\t"
    "v_lshlrev_b32 v212, 16, v224\n\t"
    "v_and_b32 v213, 0xffff0000, v224\n\t"
    "v_mfma_f32_16x16x32_bf16 v[136:139], a[108:111], a[60:63], v[136:139]\n\t"
    "v_exp_f32 v129, v129\n\t"
    "v_mfma_f32_16x16x32_bf16 v[140:143], a[124:127], a[60:63], v[172:175]\n\t"
    "v_cvt_pk_bf16_f32 v152, v128, v129\n\t"
    "v_lshlrev_b32 v214, 16, v225\n\t"
    "v_mfma_f32_16x16x32_bf16 v[140:143], a[120:123], a[56:59], v[140:143]\n\t"
    "s_waitcnt lgkmcnt(0)\n\t"
    "v_exp_f32 v130, v130\n\t"
    "v_mfma_f32_16x16x32_bf16 v[140:143], a[116:119], a[52:55], v[140:143]\n\t"
    "v_and_b32 v215, 0xffff0000, v225\n\t"
    "v_lshlrev_b32 v216, 16, v226\n\t"
    "v_mfma_f32_16x16x32_bf16 v[140:143], a[112:115], a[48:51], v[140:143]\n\t"
    "v_exp_f32 v131, v131\n\t"
    "v_mfma_f32_16x16x32_bf16 v[8:11], a[192:195], v[144:147], v[8:11]\n\t"
    "v_cvt_pk_bf16_f32 v153, v130, v131\n\t"
    "v_and_b32 v217, 0xffff0000, v226\n\t"
    "v_mfma_f32_16x16x32_bf16 v[8:11], a[196:199], v[148:151], v[8:11]\n\t"
    "v_exp_f32 v132, v132\n\t"
    "v_mfma_f32_16x16x32_bf16 v[24:27], a[204:207], v[148:151], v[24:27]\n\t"
    "v_lshlrev_b32 v218, 16, v227\n\t"
    "v_and_b32 v219, 0xffff0000, v227\n\t"
    "v_mfma_f32_16x16x32_bf16 v[24:27], a[200:203], v[144:147], v[24:27]\n\t"
    "v_exp_f32 v133, v133\n\t"
    "v_mfma_f32_16x16x32_bf16 v[40:43], a[208:211], v[144:147], v[40:43]\n\t"
    "v_cvt_pk_bf16_f32 v154, v132, v133\n\t"
    "v_fma_f32 v204, -v204, s74, s74\n\t"
    "v_mfma_f32_16x16x32_bf16 v[40:43], a[212:215], v[148:151], v[40:43]\n\t"
    "v_exp_f32 v134, v134\n\t"
    "v_mfma_f32_16x16x32_bf16 v[56:59], a[220:223], v[148:151], v[56:59]\n\t"
    "v_fma_f32 v205, -v205, s74, s74\n\t"
    "v_fma_f32 v206, -v206, s74, s74\n\t"
    "v_mfma_f32_16x16x32_bf16 v[56:59], a[216:219], v[144:147], v[56:59]\n\t"
    "v_exp_f32 v135, v135\n\t"
    "v_mfma_f32_16x16x32_bf16 v[72:75], a[224:227], v[144:147], v[72:75]\n\t"
    "v_cvt_pk_bf16_f32 v155, v134, v135\n\t"
    "v_fma_f32 v207, -v207, s74, s74\n\t"
    "v_mfma_f32_16x16x32_bf16 v[72:75], a[228:231], v[148:151], v[72:75]\n\t"
    "v_exp_f32 v136, v136\n\t"
    "v_mfma_f32_16x16x32_bf16 v[88:91], a[236:239], v[148:151], v[88:91]\n\t"
    "v_fma_f32 v208, -v208, s74, s74\n\t"
    "v_fma_f32 v209, -v209, s74, s74\n\t"
    "v_mfma_f32_16x16x32_bf16 v[88:91], a[232:235], v[144:147], v[88:91]\n\t"
    "v_exp_f32 v137, v137\n\t"
    "v_mfma_f32_16x16x32_bf16 v[104:107], a[240:243], v[144:147], v[104:107]\n\t"
    "v_cvt_pk_bf16_f32 v156, v136, v137\n\t"
    "v_fma_f32 v210, -v210, s74, s74\n\t"
    "v_mfma_f32_16x16x32_bf16 v[104:107], a[244:247], v[148:151], v[104:107]\n\t"
    "v_exp_f32 v138, v138\n\t"
    "v_mfma_f32_16x16x32_bf16 v[120:123], a[252:255], v[148:151], v[120:123]\n\t"
    "v_fma_f32 v211, -v211, s74, s74\n\t"
    "v_fma_f32 v212, -v212, s74, s74\n\t"
    "v_mfma_f32_16x16x32_bf16 v[120:123], a[248:251], v[144:147], v[120:123]\n\t"
    "v_exp_f32 v139, v139\n\t"
    "v_smfmac_f32_16x16x64_bf16 v[184:187], v[192:195], v[144:151], v245\n\t"
    "v_cvt_pk_bf16_f32 v157, v138, v139\n\t"
    "v_fma_f32 v213, -v213, s74, s74\n\t"
    "s_mov_b64 exec, s[92:93]\n\tv_mov_b32 v254, v252\n\tv_mov_b32 v255, v253\n\ts_mov_b64 exec, -1\n\ts_nop 3\n\t"
    "v_mfma_f32_16x16x32_bf16 v[128:131], a[64:67], a[0:3], v[160:163]\n\t"
    "v_mfma_f32_16x16x32_bf16 v[128:131], a[68:71], a[4:7], v[128:131]\n\t"
    "v_exp_f32 v140, v140\n\t"
    "v_mfma_f32_16x16x32_bf16 v[128:131], a[72:75], a[8:11], v[128:131]\n\t"
    "v_mfma_f32_16x16x32_bf16 v[128:131], a[76:79], a[12:15], v[128:131]\n\t"
    "v_exp_f32 v141, v141\n\t"
    "v_mfma_f32_16x16x32_bf16 v[132:135], a[92:95], a[12:15], v[160:163]\n\t"
    "v_cvt_pk_bf16_f32 v158, v140, v141\n\t"
    "v_cvt_pk_bf16_f32 v8, v8, v9\n\t"
    "v_mfma_f32_16x16x32_bf16 v[132:135], a[88:91], a[8:11], v[132:135]\n\t"
    "v_exp_f32 v142, v142\n\t"
    "v_mfma_f32_16x16x32_bf16 v[132:135], a[84:87], a[4:7], v[132:135]\n\t"
    "v_cvt_pk_bf16_f32 v9, v10, v11\n\t"
    "v_cvt_pk_bf16_f32 v10, v24, v25\n\t"
    "v_mfma_f32_16x16x32_bf16 v[132:135], a[80:83], a[0:3], v[132:135]\n\t"
    "v_exp_f32 v143, v143\n\t"
    "v_mfma_f32_16x16x32_bf16 v[136:139], a[96:99], a[0:3], v[160:163]\n\t"
    "v_cvt_pk_bf16_f32 v159, v142, v143\n\t"
    "v_cvt_pk_bf16_f32 v11, v26, v27\n\t"
    "v_mfma_f32_16x16x32_bf16 v[136:139], a[100:103], a[4:7], v[136:139]\n\t"
    "v_exp_f32 v128, v128\n\t"
    "v_mfma_f32_16x16x32_bf16 v[136:139], a[104:107], a[8:11], v[136:139]\n\t"
    "buffer_store_dwordx4 v[8:11], v248, s[44:47], s75 offen offset:0 sc1\n\t"
    "v_cvt_pk_bf16_f32 v40, v40, v41\n\t"
    "v_mfma_f32_16x16x32_bf16 v[136:139], a[108:111], a[12:15], v[136:139]\n\t"
    "v_exp_f32 v129, v129\n\t"
    "v_mfma_f32_16x16x32_bf16 v[140:143], a[124:127], a[12:15], v[160:163]\n\t"
    "v_cvt_pk_bf16_f32 v144, v128, v129\n\t"
    "v_cvt_pk_bf16_f32 v41, v42, v43\n\t"
    "v_mfma_f32_16x16x32_bf16 v[140:143], a[120:123], a[8:11], v[140:143]\n\t"
    "v_exp_f32 v130, v130\n\t"
    "v_mfma_f32_16x16x32_bf16 v[140:143], a[116:119], a[4:7], v[140:143]\n\t"
    "v_cvt_pk_bf16_f32 v42, v56, v57\n\t"
    "v_cvt_pk_bf16_f32 v43, v58, v59\n\t"
    "v_mfma_f32_16x16x32_bf16 v[140:143], a[112:115], a[0:3], v[140:143]\n\t"
    "v_exp_f32 v131, v131\n\t"
    "v_mfma_f32_16x16x32_bf16 v[12:15], a[192:195], v[152:155], v[12:15]\n\t"
    "buffer_store_dwordx4 v[40:43], v248, s[44:47], s75 offen offset:1024 sc1\n\t"
    "v_cvt_pk_bf16_f32 v145, v130, v131\n\t"
    "v_mfma_f32_16x16x32_bf16 v[12:15], a[196:199], v[156:159], v[12:15]\n\t"
    "v_exp_f32 v132, v132\n\t"
    "v_mfma_f32_16x16x32_bf16 v[28:31], a[204:207], v[156:159], v[28:31]\n\t"
    "v_cvt_pk_bf16_f32 v72, v72, v73\n\t"
    "v_cvt_pk_bf16_f32 v73, v74, v75\n\t"
    "v_mfma_f32_16x16x32_bf16 v[28:31], a[200:203], v[152:155], v[28:31]\n\t"
    "v_exp_f32 v133, v133\n\t"
    "v_mfma_f32_16x16x32_bf16 v[44:47], a[208:211], v[152:155], v[44:47]\n\t"
    "v_cvt_pk_bf16_f32 v146, v132, v133\n\t"
    "v_cvt_pk_bf16_f32 v74, v88, v89\n\t"
    "v_mfma_f32_16x16x32_bf16 v[44:47], a[212:215], v[156:159], v[44:47]\n\t"
    "v_exp_f32 v134, v134\n\t"
    "v_mfma_f32_16x16x32_bf16 v[60:63], a[220:223], v[156:159], v[60:63]\n\t"
    "v_cvt_pk_bf16_f32 v75, v90, v91\n\t"
    "v_mfma_f32_16x16x32_bf16 v[60:63], a[216:219], v[152:155], v[60:63]\n\t"
    "v_exp_f32 v135, v135\n\t"
    "v_mfma_f32_16x16x32_bf16 v[76:79], a[224:227], v[152:155], v[76:79]\n\t"
    "buffer_store_dwordx4 v[72:75], v248, s[44:47], s75 offen offset:2048 sc1\n\t"
    "v_cvt_pk_bf16_f32 v147, v134, v135\n\t"
    "v_mfma_f32_16x16x32_bf16 v[76:79], a[228:231], v[156:159], v[76:79]\n\t"
    "v_exp_f32 v136, v136\n\t"
    "v_mfma_f32_16x16x32_bf16 v[92:95], a[236:239], v[156:159], v[92:95]\n\t"
    "v_cvt_pk_bf16_f32 v104, v104, v105\n\t"
    "v_cvt_pk_bf16_f32 v105, v106, v107\n\t"
    "v_mfma_f32_16x16x32_bf16 v[92:95], a[232:235], v[152:155], v[92:95]\n\t"
    "v_exp_f32 v137, v137\n\t"
    "v_mfma_f32_16x16x32_bf16 v[108:111], a[240:243], v[152:155], v[108:111]\n\t"
    "v_cvt_pk_bf16_f32 v148, v136, v137\n\t"
    "v_cvt_pk_bf16_f32 v106, v120, v121\n\t"
    "v_mfma_f32_16x16x32_bf16 v[108:111], a[244:247], v[156:159], v[108:111]\n\t"
    "v_exp_f32 v138, v138\n\t"
    "v_mfma_f32_16x16x32_bf16 v[124:127], a[252:255], v[156:159], v[124:127]\n\t"
    "v_cvt_pk_bf16_f32 v107, v122, v123\n\t"
    "v_mfma_f32_16x16x32_bf16 v[124:127], a[248:251], v[152:155], v[124:127]\n\t"
    "v_exp_f32 v139, v139\n\t"
    "v_smfmac_f32_16x16x64_bf16 v[188:191], v[192:195], v[152:159], v245\n\t"
    "buffer_store_dwordx4 v[104:107], v248, s[44:47], s75 offen offset:3072 sc1\n\t"
    "v_cvt_pk_bf16_f32 v149, v138, v139\n\t"
    "v_fma_f32 v214, -v214, s74, s74\n\t"
    "v_fma_f32 v215, -v215, s74, s74\n\t"
    "v_fma_f32 v216, -v216, s74, s74\n\t"
    "v_fma_f32 v217, -v217, s74, s74\n\t"
    "v_fma_f32 v218, -v218, s74, s74\n\t"
    "v_fma_f32 v219, -v219, s74, s74\n\t"
    "v_mfma_f32_16x16x32_bf16 v[128:131], a[64:67], a[16:19], v[164:167]\n\t"
    "ds_read_b128 a[64:67], v196 offset:32768\n\t"
    "v_mfma_f32_16x16x32_bf16 v[128:131], a[68:71], a[20:23], v[128:131]\n\t"
    "v_exp_f32 v140, v140\n\t"
    "v_mfma_f32_16x16x32_bf16 v[128:131], a[72:75], a[24:27], v[128:131]\n\t"
    "ds_read_b128 a[68:71], v196 offset:33024\n\t"
    "ds_read_b128 a[72:75], v196 offset:33280\n\t"
    "v_mfma_f32_16x16x32_bf16 v[128:131], a[76:79], a[28:31], v[128:131]\n\t"
    "v_exp_f32 v141, v141\n\t"
    "v_mfma_f32_16x16x32_bf16 v[132:135], a[92:95], a[28:31], v[164:167]\n\t"
    "ds_read_b128 a[76:79], v196 offset:33536\n\t"
    "v_cvt_pk_bf16_f32 v150, v140, v141\n\t"
    "v_mfma_f32_16x16x32_bf16 v[132:135], a[88:91], a[24:27], v[132:135]\n\t"
    "v_exp_f32 v142, v142\n\t"
    "v_mfma_f32_16x16x32_bf16 v[132:135], a[84:87], a[20:23], v[132:135]\n\t"
    "ds_read_b128 a[92:95], v196 offset:37632\n\t"
    "ds_read_b128 a[88:91], v196 offset:37376\n\t"
    "v_mfma_f32_16x16x32_bf16 v[132:135], a[80:83], a[16:19], v[132:135]\n\t"
    "v_exp_f32 v143, v143\n\t"
    "v_mfma_f32_16x16x32_bf16 v[136:139], a[96:99], a[16:19], v[164:167]\n\t"
    "ds_read_b128 a[84:87], v196 offset:37120\n\t"
    "v_cvt_pk_bf16_f32 v151, v142, v143\n\t"
    "v_mfma_f32_16x16x32_bf16 v[136:139], a[100:103], a[20:23], v[136:139]\n\t"
    "v_exp_f32 v128, v128\n\t"
    "v_mfma_f32_16x16x32_bf16 v[136:139], a[104:107], a[24:27], v[136:139]\n\t"
    "ds_read_b128 a[80:83], v196 offset:36864\n\t"
    "ds_read_b128 a[96:99], v196 offset:40960\n\t"
    "v_mfma_f32_16x16x32_bf16 v[136:139], a[108:111], a[28:31], v[136:139]\n\t"
    "v_exp_f32 v129, v129\n\t"
    "v_mfma_f32_16x16x32_bf16 v[140:143], a[124:127], a[28:31], v[164:167]\n\t"
    "ds_read_b128 a[100:103], v196 offset:41216\n\t"
    "v_cvt_pk_bf16_f32 v152, v128, v129\n\t"
    "v_mfma_f32_16x16x32_bf16 v[140:143], a[120:123], a[24:27], v[140:143]\n\t"
    "v_exp_f32 v130, v130\n\t"
    "v_mfma_f32_16x16x32_bf16 v[140:143], a[116:119], a[20:23], v[140:143]\n\t"
    "ds_read_b128 a[104:107], v196 offset:41472\n\t"
    "ds_read_b128 a[108:111], v196 offset:41728\n\t"
    "v_mfma_f32_16x16x32_bf16 v[140:143], a[112:115], a[16:19], v[140:143]\n\t"
    "v_exp_f32 v131, v131\n\t"
    "v_mfma_f32_16x16x32_bf16 v[0:3], a[192:195], v[144:147], v[0:3]\n\t"
    "ds_read_b128 a[124:127], v196 offset:45824\n\t"
    "v_cvt_pk_bf16_f32 v153, v130, v131\n\t"
    "v_mfma_f32_16x16x32_bf16 v[0:3], a[196:199], v[148:151], v[0:3]\n\t"
    "v_exp_f32 v132, v132\n\t"
    "v_mfma_f32_16x16x32_bf16 v[16:19], a[204:207], v[148:151], v[16:19]\n\t"
    "ds_read_b128 a[120:123], v196 offset:45568\n\t"
    "ds_read_b128 a[116:119], v196 offset:45312\n\t"
    "v_mfma_f32_16x16x32_bf16 v[16:19], a[200:203], v[144:147], v[16:19]\n\t"
    "v_exp_f32 v133, v133\n\t"
    "v_mfma_f32_16x16x32_bf16 v[32:35], a[208:211], v[144:147], v[32:35]\n\t"
    "ds_read_b128 a[112:115], v196 offset:45056\n\t"
    "v_cvt_pk_bf16_f32 v154, v132, v133\n\t"
    "v_mfma_f32_16x16x32_bf16 v[32:35], a[212:215], v[148:151], v[32:35]\n\t"
    "v_exp_f32 v134, v134\n\t"
    "v_mfma_f32_16x16x32_bf16 v[48:51], a[220:223], v[148:151], v[48:51]\n\t"
    "v_cvt_pk_bf16_f32 v12, v12, v13\n\t"
    "v_cvt_pk_bf16_f32 v13, v14, v15\n\t"
    "v_mfma_f32_16x16x32_bf16 v[48:51], a[216:219], v[144:147], v[48:51]\n\t"
    "v_exp_f32 v135, v135\n\t"
    "v_mfma_f32_16x16x32_bf16 v[64:67], a[224:227], v[144:147], v[64:67]\n\t"
    "v_cvt_pk_bf16_f32 v155, v134, v135\n\t"
    "v_cvt_pk_bf16_f32 v14, v28, v29\n\t"
    "v_mfma_f32_16x16x32_bf16 v[64:67], a[228:231], v[148:151], v[64:67]\n\t"
    "v_exp_f32 v136, v136\n\t"
    "v_mfma_f32_16x16x32_bf16 v[80:83], a[236:239], v[148:151], v[80:83]\n\t"
    "v_cvt_pk_bf16_f32 v15, v30, v31\n\t"
    "v_mfma_f32_16x16x32_bf16 v[80:83], a[232:235], v[144:147], v[80:83]\n\t"
    "v_exp_f32 v137, v137\n\t"
    "v_mfma_f32_16x16x32_bf16 v[96:99], a[240:243], v[144:147], v[96:99]\n\t"
    "buffer_store_dwordx4 v[12:15], v248, s[44:47], s70 offen offset:0 sc1\n\t"
    "v_cvt_pk_bf16_f32 v156, v136, v137\n\t"
    "v_mfma_f32_16x16x32_bf16 v[96:99], a[244:247], v[148:151], v[96:99]\n\t"
    "v_exp_f32 v138, v138\n\t"
    "v_mfma_f32_16x16x32_bf16 v[112:115], a[252:255], v[148:151], v[112:115]\n\t"
    "v_cvt_pk_bf16_f32 v44, v44, v45\n\t"
    "v_cvt_pk_bf16_f32 v45, v46, v47\n\t"
    "v_mfma_f32_16x16x32_bf16 v[112:115], a[248:251], v[144:147], v[112:115]\n\t"
    "v_exp_f32 v139, v139\n\t"
    "v_smfmac_f32_16x16x64_bf16 v[176:179], v[192:195], v[144:151], v245\n\t"
    "s_waitcnt lgkmcnt(0)\n\t"
    "v_cvt_pk_bf16_f32 v157, v138, v139\n\t"
    "v_cvt_pk_bf16_f32 v46, v60, v61\n\t"
    "v_cvt_pk_bf16_f32 v47, v62, v63\n\t"
    "buffer_store_dwordx4 v[44:47], v248, s[44:47], s70 offen offset:1024 sc1\n\t"
    "v_mfma_f32_16x16x32_bf16 v[128:131], a[64:67], a[0:3], v[160:163]\n\t"
    "v_cvt_pk_bf16_f32 v76, v76, v77\n\t"
    "v_cvt_pk_bf16_f32 v77, v78, v79\n\t"
    "v_mfma_f32_16x16x32_bf16 v[128:131], a[68:71], a[4:7], v[128:131]\n\t"
    "v_exp_f32 v140, v140\n\t"
    "v_mfma_f32_16x16x32_bf16 v[128:131], a[72:75], a[8:11], v[128:131]\n\t"
    "v_cvt_pk_bf16_f32 v78, v92, v93\n\t"
    "v_cvt_pk_bf16_f32 v79, v94, v95\n\t"
    "v_mfma_f32_16x16x32_bf16 v[128:131], a[76:79], a[12:15], v[128:131]\n\t"
    "v_exp_f32 v141, v141\n\t"
    "v_mfma_f32_16x16x32_bf16 v[132:135], a[92:95], a[12:15], v[160:163]\n\t"
    "buffer_store_dwordx4 v[76:79], v248, s[44:47], s70 offen offset:2048 sc1\n\t"
    "v_cvt_pk_bf16_f32 v158, v140, v141\n\t"
    "v_mfma_f32_16x16x32_bf16 v[132:135], a[88:91], a[8:11], v[132:135]\n\t"
    "v_exp_f32 v142, v142\n\t"
    "v_mfma_f32_16x16x32_bf16 v[132:135], a[84:87], a[4:7], v[132:135]\n\t"
    "v_cvt_pk_bf16_f32 v108, v108, v109\n\t"
    "v_cvt_pk_bf16_f32 v109, v110, v111\n\t"
    "v_mfma_f32_16x16x32_bf16 v[132:135], a[80:83], a[0:3], v[132:135]\n\t"
    "v_exp_f32 v143, v143\n\t"
    "v_mfma_f32_16x16x32_bf16 v[136:139], a[96:99], a[0:3], v[160:163]\n\t"
    "v_cvt_pk_bf16_f32 v159, v142, v143\n\t"
    "v_cvt_pk_bf16_f32 v110, v124, v125\n\t"
    "v_mfma_f32_16x16x32_bf16 v[136:139], a[100:103], a[4:7], v[136:139]\n\t"
    "v_exp_f32 v128, v128\n\t"
    "v_mfma_f32_16x16x32_bf16 v[136:139], a[104:107], a[8:11], v[136:139]\n\t"
    "v_cvt_pk_bf16_f32 v111, v126, v127\n\t"
    "v_mfma_f32_16x16x32_bf16 v[136:139], a[108:111], a[12:15], v[136:139]\n\t"
    "v_exp_f32 v129, v129\n\t"
    "v_mfma_f32_16x16x32_bf16 v[140:143], a[124:127], a[12:15], v[160:163]\n\t"
    "buffer_store_dwordx4 v[108:111], v248, s[44:47], s70 offen offset:3072 sc1\n\t"
    "v_cvt_pk_bf16_f32 v144, v128, v129\n\t"
    "v_mfma_f32_16x16x32_bf16 v[140:143], a[120:123], a[8:11], v[140:143]\n\t"
    "v_exp_f32 v130, v130\n\t"
    "v_mfma_f32_16x16x32_bf16 v[140:143], a[116:119], a[4:7], v[140:143]\n\t"
    "v_mov_b32 v236, v168\n\t"
    "v_add_f32 v237, v184, v185\n\t"
    "v_mfma_f32_16x16x32_bf16 v[140:143], a[112:115], a[0:3], v[140:143]\n\t"
    "v_exp_f32 v131, v131\n\t"
    "v_mfma_f32_16x16x32_bf16 v[4:7], a[192:195], v[152:155], v[4:7]\n\t"
    "v_cvt_pk_bf16_f32 v145, v130, v131\n\t"
    "v_mov_b32 v238, v172\n\t"
    "v_mfma_f32_16x16x32_bf16 v[4:7], a[196:199], v[156:159], v[4:7]\n\t"
    "v_exp_f32 v132, v132\n\t"
    "v_mfma_f32_16x16x32_bf16 v[20:23], a[204:207], v[156:159], v[20:23]\n\t"
    "v_add_f32 v239, v188, v189\n\t"
    "v_mfma_f32_16x16x32_bf16 v[20:23], a[200:203], v[152:155], v[20:23]\n\t"
    "v_exp_f32 v133, v133\n\t"
    "v_mfma_f32_16x16x32_bf16 v[36:39], a[208:211], v[152:155], v[36:39]\n\t"
    "buffer_store_dwordx4 v[236:239], v248, s[44:47], s69 offen sc1\n\t"
    "v_cvt_pk_bf16_f32 v146, v132, v133\n\t"
    "v_mfma_f32_16x16x32_bf16 v[36:39], a[212:215], v[156:159], v[36:39]\n\t"
    "v_exp_f32 v134, v134\n\t"
    "v_mfma_f32_16x16x32_bf16 v[52:55], a[220:223], v[156:159], v[52:55]\n\t"
    "v_add_f32 v240, v237, v239\n\t"
    "v_cmp_ngt_f32 vcc, 0x53800000, v240\n\t"
    "v_mfma_f32_16x16x32_bf16 v[52:55], a[216:219], v[152:155], v[52:55]\n\t"
    "v_exp_f32 v135, v135\n\t"
    "v_mfma_f32_16x16x32_bf16 v[68:71], a[224:227], v[152:155], v[68:71]\n\t"
    "v_cvt_pk_bf16_f32 v147, v134, v135\n\t"
    "v_add_f32 v220, v204, v160\n\t"
    "v_mfma_f32_16x16x32_bf16 v[68:71], a[228:231], v[156:159], v[68:71]\n\t"
    "v_exp_f32 v136, v136\n\t"
    "v_mfma_f32_16x16x32_bf16 v[84:87], a[236:239], v[156:159], v[84:87]\n\t"
    "v_add_f32 v221, v205, v160\n\t"
    "v_add_f32 v222, v206, v160\n\t"
    "v_mfma_f32_16x16x32_bf16 v[84:87], a[232:235], v[152:155], v[84:87]\n\t"
    "v_exp_f32 v137, v137\n\t"
    "v_mfma_f32_16x16x32_bf16 v[100:103], a[240:243], v[152:155], v[100:103]\n\t"
    "v_cvt_pk_bf16_f32 v148, v136, v137\n\t"
    "v_add_f32 v223, v207, v160\n\t"
    "v_mfma_f32_16x16x32_bf16 v[100:103], a[244:247], v[156:159], v[100:103]\n\t"
    "v_exp_f32 v138, v138\n\t"
    "v_mfma_f32_16x16x32_bf16 v[116:119], a[252:255], v[156:159], v[116:119]\n\t"
    "v_add_f32 v224, v208, v160\n\t"
    "v_add_f32 v225, v209, v160\n\t"
    "v_mfma_f32_16x16x32_bf16 v[116:119], a[248:251], v[152:155], v[116:119]\n\t"
    "v_exp_f32 v139, v139\n\t"
    "v_smfmac_f32_16x16x64_bf16 v[180:183], v[192:195], v[152:159], v245\n\t"
    "s_cmp_lg_u64 vcc, 0\n\t"
    "s_cselect_b32 s90, 1, 0\n\t"
    "s_andn2_b32 s90, s90, s67\n\t"
    "v_cvt_pk_bf16_f32 v149, v138, v139\n\t"
    "v_add_f32 v226, v210, v160\n\t"
    "v_add_f32 v227, v211, v160\n\t"
    "v_mfma_f32_16x16x32_bf16 v[128:131], a[64:67], a[16:19], v[164:167]\n\t"
    "ds_read_b128 a[64:67], v196 offset:16384\n\t"
    "v_mfma_f32_16x16x32_bf16 v[128:131], a[68:71], a[20:23], v[128:131]\n\t"
    "v_exp_f32 v140, v140\n\t"
    "v_mfma_f32_16x16x32_bf16 v[128:131], a[72:75], a[24:27], v[128:131]\n\t"
    "ds_read_b128 a[68:71], v196 offset:16640\n\t"
    "ds_read_b128 a[72:75], v196 offset:16896\n\t"
    "v_mfma_f32_16x16x32_bf16 v[128:131], a[76:79], a[28:31], v[128:131]\n\t"
    "v_exp_f32 v141, v141\n\t"
    "v_mfma_f32_16x16x32_bf16 v[132:135], a[92:95], a[28:31], v[164:167]\n\t"
    "ds_read_b128 a[76:79], v196 offset:17152\n\t"
    "v_cvt_pk_bf16_f32 v150, v140, v141\n\t"
    "v_mfma_f32_16x16x32_bf16 v[132:135], a[88:91], a[24:27], v[132:135]\n\t"
    "v_exp_f32 v142, v142\n\t"
    "v_mfma_f32_16x16x32_bf16 v[132:135], a[84:87], a[20:23], v[132:135]\n\t"
    "ds_read_b128 a[92:95], v196 offset:21248\n\t"
    "ds_read_b128 a[88:91], v196 offset:20992\n\t"
    "v_mfma_f32_16x16x32_bf16 v[132:135], a[80:83], a[16:19], v[132:135]\n\t"
    "v_exp_f32 v143, v143\n\t"
    "v_mfma_f32_16x16x32_bf16 v[136:139], a[96:99], a[16:19], v[164:167]\n\t"
    "ds_read_b128 a[84:87], v196 offset:20736\n\t"
    "v_cvt_pk_bf16_f32 v151, v142, v143\n\t"
    "v_mfma_f32_16x16x32_bf16 v[136:139], a[100:103], a[20:23], v[136:139]\n\t"
    "v_exp_f32 v128, v128\n\t"
    "v_mfma_f32_16x16x32_bf16 v[136:139], a[104:107], a[24:27], v[136:139]\n\t"
    "ds_read_b128 a[80:83], v196 offset:20480\n\t"
    "ds_read_b128 a[96:99], v196 offset:24576\n\t"
    "v_mfma_f32_16x16x32_bf16 v[136:139], a[108:111], a[28:31], v[136:139]\n\t"
    "v_exp_f32 v129, v129\n\t"
    "v_mfma_f32_16x16x32_bf16 v[140:143], a[124:127], a[28:31], v[164:167]\n\t"
    "ds_read_b128 a[100:103], v196 offset:24832\n\t"
    "v_cvt_pk_bf16_f32 v152, v128, v129\n\t"
    "v_mfma_f32_16x16x32_bf16 v[140:143], a[120:123], a[24:27], v[140:143]\n\t"
    "v_exp_f32 v130, v130\n\t"
    "v_mfma_f32_16x16x32_bf16 v[140:143], a[116:119], a[20:23], v[140:143]\n\t"
    "ds_read_b128 a[104:107], v196 offset:25088\n\t"
    "ds_read_b128 a[108:111], v196 offset:25344\n\t"
    "v_mfma_f32_16x16x32_bf16 v[140:143], a[112:115], a[16:19], v[140:143]\n\t"
    "v_exp_f32 v131, v131\n\t"
    "v_mfma_f32_16x16x32_bf16 v[0:3], a[128:131], v[144:147], v[0:3]\n\t"
    "ds_read_b128 a[124:127], v196 offset:29440\n\t"
    "v_cvt_pk_bf16_f32 v153, v130, v131\n\t"
    "v_mfma_f32_16x16x32_bf16 v[0:3], a[132:135], v[148:151], v[0:3]\n\t"
    "v_exp_f32 v132, v132\n\t"
    "v_mfma_f32_16x16x32_bf16 v[16:19], a[140:143], v[148:151], v[16:19]\n\t"
    "ds_read_b128 a[120:123], v196 offset:29184\n\t"
    "ds_read_b128 a[116:119], v196 offset:28928\n\t"
    "v_mfma_f32_16x16x32_bf16 v[16:19], a[136:139], v[144:147], v[16:19]\n\t"
    "v_exp_f32 v133, v133\n\t"
    "v_mfma_f32_16x16x32_bf16 v[32:35], a[144:147], v[144:147], v[32:35]\n\t"
    "ds_read_b128 a[112:115], v196 offset:28672\n\t"
    "v_cvt_pk_bf16_f32 v154, v132, v133\n\t"
    "v_mfma_f32_16x16x32_bf16 v[32:35], a[148:151], v[148:151], v[32:35]\n\t"
    "v_exp_f32 v134, v134\n\t"
    "v_mfma_f32_16x16x32_bf16 v[48:51], a[156:159], v[148:151], v[48:51]\n\t"
    "ds_read_b64_tr_b16 a[192:193], v197 offset:16384\n\t"
    "ds_read_b64_tr_b16 a[194:195], v197 offset:20480\n\t"
    "v_mfma_f32_16x16x32_bf16 v[48:51], a[152:155], v[144:147], v[48:51]\n\t"
    "v_exp_f32 v135, v135\n\t"
    "v_mfma_f32_16x16x32_bf16 v[64:67], a[160:163], v[144:147], v[64:67]\n\t"
    "ds_read_b64_tr_b16 a[196:197], v197 offset:24576\n\t"
    "v_cvt_pk_bf16_f32 v155, v134, v135\n\t"
    "v_mfma_f32_16x16x32_bf16 v[64:67], a[164:167], v[148:151], v[64:67]\n\t"
    "v_exp_f32 v136, v136\n\t"
    "v_mfma_f32_16x16x32_bf16 v[80:83], a[172:175], v[148:151], v[80:83]\n\t"
    "ds_read_b64_tr_b16 a[198:199], v197 offset:28672\n\t"
    "ds_read_b64_tr_b16 a[200:201], v198 offset:16384\n\t"
    "v_mfma_f32_16x16x32_bf16 v[80:83], a[168:171], v[144:147], v[80:83]\n\t"
    "v_exp_f32 v137, v137\n\t"
    "v_mfma_f32_16x16x32_bf16 v[96:99], a[176:179], v[144:147], v[96:99]\n\t"
    "ds_read_b64_tr_b16 a[202:203], v198 offset:20480\n\t"
    "v_cvt_pk_bf16_f32 v156, v136, v137\n\t"
    "v_mfma_f32_16x16x32_bf16 v[96:99], a[180:183], v[148:151], v[96:99]\n\t"
    "v_exp_f32 v138, v138\n\t"
    "v_mfma_f32_16x16x32_bf16 v[112:115], a[188:191], v[148:151], v[112:115]\n\t"
    "ds_read_b64_tr_b16 a[204:205], v198 offset:24576\n\t"
    "ds_read_b64_tr_b16 a[206:207], v198 offset:28672\n\t"
    "v_mfma_f32_16x16x32_bf16 v[112:115], a[184:187], v[144:147], v[112:115]\n\t"
    "v_exp_f32 v139, v139\n\t"
    "v_smfmac_f32_16x16x64_bf16 v[176:179], v[192:195], v[144:151], v245\n\t"
    "s_waitcnt lgkmcnt(8)\n\t"
    "v_cvt_pk_bf16_f32 v157, v138, v139\n\t"
    "s_waitcnt vmcnt(0)\n\t"
    "s_cmp_eq_u32 s90, 0\n\t"
    "s_cselect_b32 s46, s46, 0\n\t"
    "s_cselect_b32 s91, 1, s91\n\t"
    "v_add_u32 v252, 1, v255\n\t"
    "buffer_store_dword v252, off, s[44:47], s77 sc1\n\t"
    "v_mfma_f32_16x16x32_bf16 v[128:131], a[64:67], a[0:3], v[160:163]\n\t"
    "ds_read_b64_tr_b16 a[208:209], v197 offset:16640\n\t"
    "ds_read_b64_tr_b16 a[210:211], v197 offset:20736\n\t"
    "v_mfma_f32_16x16x32_bf16 v[128:131], a[68:71], a[4:7], v[128:131]\n\t"
    "v_exp_f32 v140, v140\n\t"
    "v_mfma_f32_16x16x32_bf16 v[128:131], a[72:75], a[8:11], v[128:131]\n\t"
    "ds_read_b64_tr_b16 a[212:213], v197 offset:24832\n\t"
    "ds_read_b64_tr_b16 a[214:215], v197 offset:28928\n\t"
    "v_mfma_f32_16x16x32_bf16 v[128:131], a[76:79], a[12:15], v[128:131]\n\t"
    "v_exp_f32 v141, v141\n\t"
    "v_mfma_f32_16x16x32_bf16 v[132:135], a[92:95], a[12:15], v[160:163]\n\t"
    "ds_read_b64_tr_b16 a[216:217], v198 offset:16640\n\t"
    "v_cvt_pk_bf16_f32 v158, v140, v141\n\t"
    "v_mfma_f32_16x16x32_bf16 v[132:135], a[88:91], a[8:11], v[132:135]\n\t"
    "v_exp_f32 v142, v142\n\t"
    "v_mfma_f32_16x16x32_bf16 v[132:135], a[84:87], a[4:7], v[132:135]\n\t"
    "ds_read_b64_tr_b16 a[218:219], v198 offset:20736\n\t"
    "ds_read_b64_tr_b16 a[220:221], v198 offset:24832\n\t"
    "v_mfma_f32_16x16x32_bf16 v[132:135], a[80:83], a[0:3], v[132:135]\n\t"
    "v_exp_f32 v143, v143\n\t"
    "v_mfma_f32_16x16x32_bf16 v[136:139], a[96:99], a[0:3], v[160:163]\n\t"
    "ds_read_b64_tr_b16 a[222:223], v198 offset:28928\n\t"
    "v_cvt_pk_bf16_f32 v159, v142, v143\n\t"
    "v_mfma_f32_16x16x32_bf16 v[136:139], a[100:103], a[4:7], v[136:139]\n\t"
    "v_exp_f32 v128, v128\n\t"
    "v_mfma_f32_16x16x32_bf16 v[136:139], a[104:107], a[8:11], v[136:139]\n\t"
    "ds_read_b64_tr_b16 a[224:225], v197 offset:16896\n\t"
    "ds_read_b64_tr_b16 a[226:227], v197 offset:20992\n\t"
    "v_mfma_f32_16x16x32_bf16 v[136:139], a[108:111], a[12:15], v[136:139]\n\t"
    "v_exp_f32 v129, v129\n\t"
    "v_mfma_f32_16x16x32_bf16 v[140:143], a[124:127], a[12:15], v[160:163]\n\t"
    "ds_read_b64_tr_b16 a[228:229], v197 offset:25088\n\t"
    "v_cvt_pk_bf16_f32 v144, v128, v129\n\t"
    "v_mfma_f32_16x16x32_bf16 v[140:143], a[120:123], a[8:11], v[140:143]\n\t"
    "v_exp_f32 v130, v130\n\t"
    "v_mfma_f32_16x16x32_bf16 v[140:143], a[116:119], a[4:7], v[140:143]\n\t"
    "ds_read_b64_tr_b16 a[230:231], v197 offset:29184\n\t"
    "ds_read_b64_tr_b16 a[232:233], v198 offset:16896\n\t"
    "v_mfma_f32_16x16x32_bf16 v[140:143], a[112:115], a[0:3], v[140:143]\n\t"
    "v_exp_f32 v131, v131\n\t"
    "v_mfma_f32_16x16x32_bf16 v[4:7], a[128:131], v[152:155], v[4:7]\n\t"
    "ds_read_b64_tr_b16 a[234:235], v198 offset:20992\n\t"
    "v_cvt_pk_bf16_f32 v145, v130, v131\n\t"
    "v_mfma_f32_16x16x32_bf16 v[4:7], a[132:135], v[156:159], v[4:7]\n\t"
    "v_exp_f32 v132, v132\n\t"
    "v_mfma_f32_16x16x32_bf16 v[20:23], a[140:143], v[156:159], v[20:23]\n\t"
    "ds_read_b64_tr_b16 a[236:237], v198 offset:25088\n\t"
    "ds_read_b64_tr_b16 a[238:239], v198 offset:29184\n\t"
    "v_mfma_f32_16x16x32_bf16 v[20:23], a[136:139], v[152:155], v[20:23]\n\t"
    "v_exp_f32 v133, v133\n\t"
    "v_mfma_f32_16x16x32_bf16 v[36:39], a[144:147], v[152:155], v[36:39]\n\t"
    "ds_read_b64_tr_b16 a[240:241], v197 offset:17152\n\t"
    "v_cvt_pk_bf16_f32 v146, v132, v133\n\t"
    "v_mfma_f32_16x16x32_bf16 v[36:39], a[148:151], v[156:159], v[36:39]\n\t"
    "v_exp_f32 v134, v134\n\t"
    "v_mfma_f32_16x16x32_bf16 v[52:55], a[156:159], v[156:159], v[52:55]\n\t"
    "ds_read_b64_tr_b16 a[242:243], v197 offset:21248\n\t"
    "ds_read_b64_tr_b16 a[244:245], v197 offset:25344\n\t"
    "v_mfma_f32_16x16x32_bf16 v[52:55], a[152:155], v[152:155], v[52:55]\n\t"
    "v_exp_f32 v135, v135\n\t"
    "v_mfma_f32_16x16x32_bf16 v[68:71], a[160:163], v[152:155], v[68:71]\n\t"
    "ds_read_b64_tr_b16 a[246:247], v197 offset:29440\n\t"
    "v_cvt_pk_bf16_f32 v147, v134, v135\n\t"
    "v_add_f32 v228, v212, v164\n\t"
    "v_mfma_f32_16x16x32_bf16 v[68:71], a[164:167], v[156:159], v[68:71]\n\t"
    "v_exp_f32 v136, v136\n\t"
    "v_add_f32 v229, v213, v164\n\t"
    "v_mfma_f32_16x16x32_bf16 v[84:87], a[172:175], v[156:159], v[84:87]\n\t"
    "ds_read_b64_tr_b16 a[248:249], v198 offset:17152\n\t"
    "ds_read_b64_tr_b16 a[250:251], v198 offset:21248\n\t"
    "v_add_f32 v230, v214, v164\n\t"
    "v_mfma_f32_16x16x32_bf16 v[84:87], a[168:171], v[152:155], v[84:87]\n\t"
    "v_exp_f32 v137, v137\n\t"
    "v_add_f32 v231, v215, v164\n\t"
    "v_mfma_f32_16x16x32_bf16 v[100:103], a[176:179], v[152:155], v[100:103]\n\t"
    "ds_read_b64_tr_b16 a[252:253], v198 offset:25344\n\t"
    "v_cvt_pk_bf16_f32 v148, v136, v137\n\t"
    "v_add_f32 v232, v216, v164\n\t"
    "v_mfma_f32_16x16x32_bf16 v[100:103], a[180:183], v[156:159], v[100:103]\n\t"
    "s_add_u32 s69, s76, 8192\n\t"
    "v_exp_f32 v138, v138\n\t"
    "v_add_f32 v233, v217, v164\n\t"
    "v_mfma_f32_16x16x32_bf16 v[116:119], a[188:191], v[156:159], v[116:119]\n\t"
    "ds_read_b64_tr_b16 a[254:255], v198 offset:29440\n\t"
    "v_add_f32 v234, v218, v164\n\t"
    "v_mfma_f32_16x16x32_bf16 v[116:119], a[184:187], v[152:155], v[116:119]\n\t"
    "s_add_u32 s70, s76, 4096\n\t"
    "v_exp_f32 v139, v139\n\t"
    "v_add_f32 v235, v219, v164\n\t"
    "v_smfmac_f32_16x16x64_bf16 v[180:183], v[192:195], v[152:159], v245\n\t"
    "buffer_load_dword v249, off, s[48:51], s78 sc1\n\t"
    "s_barrier\n\t"
    "v_cvt_pk_bf16_f32 v149, v138, v139\n\t"
    "v_mfma_f32_16x16x32_bf16 v[128:131], a[64:67], a[16:19], v[164:167]\n\t"
    "ds_read_b128 a[64:67], v196 offset:0\n\t"
    "v_mfma_f32_16x16x32_bf16 v[128:131], a[68:71], a[20:23], v[128:131]\n\t"
    "v_exp_f32 v140, v140\n\t"
    "v_mfma_f32_16x16x32_bf16 v[128:131], a[72:75], a[24:27], v[128:131]\n\t"
    "ds_read_b128 a[68:71], v196 offset:256\n\t"
    "ds_read_b128 a[72:75], v196 offset:512\n\t"
    "v_mfma_f32_16x16x32_bf16 v[128:131], a[76:79], a[28:31], v[128:131]\n\t"
    "v_exp_f32 v141, v141\n\t"
    "v_mfma_f32_16x16x32_bf16 v[132:135], a[92:95], a[28:31], v[164:167]\n\t"
    "ds_read_b128 a[76:79], v196 offset:768\n\t"
    "v_cvt_pk_bf16_f32 v150, v140, v141\n\t"
    "v_mfma_f32_16x16x32_bf16 v[132:135], a[88:91], a[24:27], v[132:135]\n\t"
    "v_exp_f32 v142, v142\n\t"
    "v_mfma_f32_16x16x32_bf16 v[132:135], a[84:87], a[20:23], v[132:135]\n\t"
    "ds_read_b128 a[92:95], v196 offset:4864\n\t"
    "ds_read_b128 a[88:91], v196 offset:4608\n\t"
    "v_mfma_f32_16x16x32_bf16 v[132:135], a[80:83], a[16:19], v[132:135]\n\t"
    "v_exp_f32 v143, v143\n\t"
    "v_mfma_f32_16x16x32_bf16 v[136:139], a[96:99], a[16:19], v[164:167]\n\t"
    "ds_read_b128 a[84:87], v196 offset:4352\n\t"
    "v_cvt_pk_bf16_f32 v151, v142, v143\n\t"
    "v_mfma_f32_16x16x32_bf16 v[136:139], a[100:103], a[20:23], v[136:139]\n\t"
    "v_exp_f32 v128, v128\n\t"
    "v_mfma_f32_16x16x32_bf16 v[136:139], a[104:107], a[24:27], v[136:139]\n\t"
    "ds_read_b128 a[80:83], v196 offset:4096\n\t"
    "ds_read_b64_tr_b16 a[128:129], v197 offset:0\n\t"
    "v_mfma_f32_16x16x32_bf16 v[136:139], a[108:111], a[28:31], v[136:139]\n\t"
    "v_exp_f32 v129, v129\n\t"
    "v_mfma_f32_16x16x32_bf16 v[140:143], a[124:127], a[28:31], v[164:167]\n\t"
    "ds_read_b64_tr_b16 a[130:131], v197 offset:4096\n\t"
    "v_cvt_pk_bf16_f32 v152, v128, v129\n\t"
    "v_mfma_f32_16x16x32_bf16 v[140:143], a[120:123], a[24:27], v[140:143]\n\t"
    "s_waitcnt lgkmcnt(10)\n\t"
    "v_exp_f32 v130, v130\n\t"
    "v_mfma_f32_16x16x32_bf16 v[140:143], a[116:119], a[20:23], v[140:143]\n\t"
    "ds_read_b64_tr_b16 a[136:137], v198 offset:0\n\t"
    "ds_read_b64_tr_b16 a[138:139], v198 offset:4096\n\t"
    "v_mfma_f32_16x16x32_bf16 v[140:143], a[112:115], a[16:19], v[140:143]\n\t"
    "v_exp_f32 v131, v131\n\t"
    "v_mfma_f32_16x16x32_bf16 v[0:3], a[192:195], v[144:147], v[0:3]\n\t"
    "ds_read_b64_tr_b16 a[144:145], v197 offset:256\n\t"
    "v_cvt_pk_bf16_f32 v153, v130, v131\n\t"
    "v_mfma_f32_16x16x32_bf16 v[0:3], a[196:199], v[148:151], v[0:3]\n\t"
    "v_exp_f32 v132, v132\n\t"
    "v_mfma_f32_16x16x32_bf16 v[16:19], a[204:207], v[148:151], v[16:19]\n\t"
    "ds_read_b64_tr_b16 a[146:147], v197 offset:4352\n\t"
    "ds_read_b64_tr_b16 a[152:153], v198 offset:256\n\t"
    "v_mfma_f32_16x16x32_bf16 v[16:19], a[200:203], v[144:147], v[16:19]\n\t"
    "v_exp_f32 v133, v133\n\t"
    "v_mfma_f32_16x16x32_bf16 v[32:35], a[208:211], v[144:147], v[32:35]\n\t"
    "ds_read_b64_tr_b16 a[154:155], v198 offset:4352\n\t"
    "v_cvt_pk_bf16_f32 v154, v132, v133\n\t"
    "v_mfma_f32_16x16x32_bf16 v[32:35], a[212:215], v[148:151], v[32:35]\n\t"
    "v_exp_f32 v134, v134\n\t"
    "v_mfma_f32_16x16x32_bf16 v[48:51], a[220:223], v[148:151], v[48:51]\n\t"
    "ds_read_b64_tr_b16 a[160:161], v197 offset:512\n\t"
    "ds_read_b64_tr_b16 a[162:163], v197 offset:4608\n\t"
    "v_mfma_f32_16x16x32_bf16 v[48:51], a[216:219], v[144:147], v[48:51]\n\t"
    "v_exp_f32 v135, v135\n\t"
    "v_mfma_f32_16x16x32_bf16 v[64:67], a[224:227], v[144:147], v[64:67]\n\t"
    "ds_read_b64_tr_b16 a[168:169], v198 offset:512\n\t"
    "v_cvt_pk_bf16_f32 v155, v134, v135\n\t"
    "v_mfma_f32_16x16x32_bf16 v[64:67], a[228:231], v[148:151], v[64:67]\n\t"
    "v_exp_f32 v136, v136\n\t"
    "v_mfma_f32_16x16x32_bf16 v[80:83], a[236:239], v[148:151], v[80:83]\n\t"
    "ds_read_b64_tr_b16 a[170:171], v198 offset:4608\n\t"
    "ds_read_b64_tr_b16 a[176:177], v197 offset:768\n\t"
    "v_mfma_f32_16x16x32_bf16 v[80:83], a[232:235], v[144:147], v[80:83]\n\t"
    "v_exp_f32 v137, v137\n\t"
    "v_mfma_f32_16x16x32_bf16 v[96:99], a[240:243], v[144:147], v[96:99]\n\t"
    "ds_read_b64_tr_b16 a[178:179], v197 offset:4864\n\t"
    "v_cvt_pk_bf16_f32 v156, v136, v137\n\t"
    "v_mfma_f32_16x16x32_bf16 v[96:99], a[244:247], v[148:151], v[96:99]\n\t"
    "v_exp_f32 v138, v138\n\t"
    "v_mfma_f32_16x16x32_bf16 v[112:115], a[252:255], v[148:151], v[112:115]\n\t"
    "ds_read_b64_tr_b16 a[184:185], v198 offset:768\n\t"
    "ds_read_b64_tr_b16 a[186:187], v198 offset:4864\n\t"
    "v_mfma_f32_16x16x32_bf16 v[112:115], a[248:251], v[144:147], v[112:115]\n\t"
    "v_exp_f32 v139, v139\n\t"
    "v_smfmac_f32_16x16x64_bf16 v[176:179], v[192:195], v[144:151], v245\n\t"
    "s_waitcnt lgkmcnt(0)\n\t"
    "v_cvt_pk_bf16_f32 v157, v138, v139\n\t"
    "v_mfma_f32_16x16x32_bf16 v[128:131], a[64:67], a[0:3], v[220:223]\n\t"
    "v_mfma_f32_16x16x32_bf16 v[128:131], a[68:71], a[4:7], v[128:131]\n\t"
    "v_exp_f32 v140, v140\n\t"
    "v_mfma_f32_16x16x32_bf16 v[128:131], a[72:75], a[8:11], v[128:131]\n\t"
    "v_mfma_f32_16x16x32_bf16 v[128:131], a[76:79], a[12:15], v[128:131]\n\t"
    "v_exp_f32 v141, v141\n\t"
    "v_mfma_f32_16x16x32_bf16 v[132:135], a[80:83], a[0:3], v[224:227]\n\t"
    "v_cvt_pk_bf16_f32 v158, v140, v141\n\t"
    "v_mfma_f32_16x16x32_bf16 v[132:135], a[84:87], a[4:7], v[132:135]\n\t"
    "v_exp_f32 v142, v142\n\t"
    "v_mfma_f32_16x16x32_bf16 v[132:135], a[88:91], a[8:11], v[132:135]\n\t"
    "v_mfma_f32_16x16x32_bf16 v[132:135], a[92:95], a[12:15], v[132:135]\n\t"
    "v_exp_f32 v143, v143\n\t"
    "v_mfma_f32_16x16x32_bf16 v[136:139], a[64:67], a[16:19], v[228:231]\n\t"
    "v_cvt_pk_bf16_f32 v159, v142, v143\n\t"
    "v_mfma_f32_16x16x32_bf16 v[136:139], a[68:71], a[20:23], v[136:139]\n\t"
    "v_exp_f32 v128, v128\n\t"
    "v_mfma_f32_16x16x32_bf16 v[136:139], a[72:75], a[24:27], v[136:139]\n\t"
    "v_mfma_f32_16x16x32_bf16 v[136:139], a[76:79], a[28:31], v[136:139]\n\t"
    "v_exp_f32 v129, v129\n\t"
    "v_mfma_f32_16x16x32_bf16 v[140:143], a[80:83], a[16:19], v[232:235]\n\t"
    "v_cvt_pk_bf16_f32 v144, v128, v129\n\t"
    "v_mfma_f32_16x16x32_bf16 v[140:143], a[84:87], a[20:23], v[140:143]\n\t"
    "v_exp_f32 v130, v130\n\t"
    "v_mfma_f32_16x16x32_bf16 v[140:143], a[88:91], a[24:27], v[140:143]\n\t"
    "v_mfma_f32_16x16x32_bf16 v[140:143], a[92:95], a[28:31], v[140:143]\n\t"
    "v_exp_f32 v131, v131\n\t"
    "v_mfma_f32_16x16x32_bf16 v[4:7], a[192:195], v[152:155], v[4:7]\n\t"
    "v_cvt_pk_bf16_f32 v145, v130, v131\n\t"
    "v_mfma_f32_16x16x32_bf16 v[4:7], a[196:199], v[156:159], v[4:7]\n\t"
    "v_exp_f32 v132, v132\n\t"
    "v_mfma_f32_16x16x32_bf16 v[20:23], a[204:207], v[156:159], v[20:23]\n\t"
    "v_mov_b32 v220, 0x3f003f00\n\t"
    "v_mov_b32 v221, 0x3f003f00\n\t"
    "v_mfma_f32_16x16x32_bf16 v[20:23], a[200:203], v[152:155], v[20:23]\n\t"
    "v_exp_f32 v133, v133\n\t"
    "v_mfma_f32_16x16x32_bf16 v[36:39], a[208:211], v[152:155], v[36:39]\n\t"
    "v_cvt_pk_bf16_f32 v146, v132, v133\n\t"
    "v_mov_b32 v222, 0x3f003f00\n\t"
    "v_mfma_f32_16x16x32_bf16 v[36:39], a[212:215], v[156:159], v[36:39]\n\t"
    "v_exp_f32 v134, v134\n\t"
    "v_mfma_f32_16x16x32_bf16 v[52:55], a[220:223], v[156:159], v[52:55]\n\t"
    "v_mov_b32 v223, 0x3f003f00\n\t"
    "v_mfma_f32_16x16x32_bf16 v[52:55], a[216:219], v[152:155], v[52:55]\n\t"
    "v_exp_f32 v135, v135\n\t"
    "v_mfma_f32_16x16x32_bf16 v[68:71], a[224:227], v[152:155], v[68:71]\n\t"
    "v_cvt_pk_bf16_f32 v147, v134, v135\n\t"
    "s_waitcnt vmcnt(0)\n\t"
    "v_cmp_ne_u32 vcc, v249, v254\n\t"
    "s_mov_b32 s66, 0\n\t"
    "s_cbranch_vccz NEB_%=\n\t"
    "buffer_load_dwordx4 v[236:239], v248, s[48:51], s69 offen sc1\n\t"
    "buffer_load_dwordx4 v[8:11], v248, s[48:51], s76 offen offset:0 sc1\n\t"
    "buffer_load_dwordx4 v[40:43], v248, s[48:51], s76 offen offset:1024 sc1\n\t"
    "buffer_load_dwordx4 v[72:75], v248, s[48:51], s76 offen offset:2048 sc1\n\t"
    "buffer_load_dwordx4 v[104:107], v248, s[48:51], s76 offen offset:3072 sc1\n\t"
    "buffer_load_dwordx4 v[12:15], v248, s[48:51], s70 offen offset:0 sc1\n\t"
    "buffer_load_dwordx4 v[44:47], v248, s[48:51], s70 offen offset:1024 sc1\n\t"
    "buffer_load_dwordx4 v[76:79], v248, s[48:51], s70 offen offset:2048 sc1\n\t"
    "buffer_load_dwordx4 v[108:111], v248, s[48:51], s70 offen offset:3072 sc1\n\t"
    "s_mov_b32 s66, 1\n\t"
    "NEB_%=:\n\t"
    "v_mfma_f32_16x16x32_bf16 v[68:71], a[228:231], v[156:159], v[68:71]\n\t"
    "v_exp_f32 v136, v136\n\t"
    "v_mfma_f32_16x16x32_bf16 v[84:87], a[236:239], v[156:159], v[84:87]\n\t"
    "v_mfma_f32_16x16x32_bf16 v[84:87], a[232:235], v[152:155], v[84:87]\n\t"
    "v_exp_f32 v137, v137\n\t"
    "v_mfma_f32_16x16x32_bf16 v[100:103], a[240:243], v[152:155], v[100:103]\n\t"
    "v_cvt_pk_bf16_f32 v148, v136, v137\n\t"
    "v_mfma_f32_16x16x32_bf16 v[100:103], a[244:247], v[156:159], v[100:103]\n\t"
    "v_exp_f32 v138, v138\n\t"
    "v_mfma_f32_16x16x32_bf16 v[116:119], a[252:255], v[156:159], v[116:119]\n\t"
    "v_mfma_f32_16x16x32_bf16 v[116:119], a[248:251], v[152:155], v[116:119]\n\t"
    "v_exp_f32 v139, v139\n\t"
    "v_smfmac_f32_16x16x64_bf16 v[180:183], v[192:195], v[152:159], v245\n\t"
    "v_cvt_pk_bf16_f32 v149, v138, v139\n\t"
    "v_mfma_f32_16x16x32_bf16 v[176:179], v[220:223], v[144:147], v[176:179]\n\t"
    "v_exp_f32 v140, v140\n\t"
    "v_mfma_f32_16x16x32_bf16 v[0:3], a[128:131], v[144:147], v[0:3]\n\t"
    "v_exp_f32 v141, v141\n\t"
    "v_mfma_f32_16x16x32_bf16 v[16:19], a[136:139], v[144:147], v[16:19]\n\t"
    "v_exp_f32 v142, v142\n\t"
    "v_mfma_f32_16x16x32_bf16 v[32:35], a[144:147], v[144:147], v[32:35]\n\t"
    "v_exp_f32 v143, v143\n\t"
    "v_mfma_f32_16x16x32_bf16 v[48:51], a[152:155], v[144:147], v[48:51]\n\t"
    "v_cvt_pk_bf16_f32 v150, v140, v141\n\t"
    "v_cvt_pk_bf16_f32 v151, v142, v143\n\t"
    "v_mfma_f32_16x16x32_bf16 v[64:67], a[160:163], v[144:147], v[64:67]\n\t"
    "s_and_b32 s68, %1, 1\n\t"
    "v_and_b32 v240, 15, v203\n\t"
    "v_lshrrev_b32 v241, 4, v203\n\t"
    "v_mfma_f32_16x16x32_bf16 v[80:83], a[168:171], v[144:147], v[80:83]\n\t"
    "s_lshl_b32 s68, s68, 14\n\t"
    "v_mul_u32_u24 v196, 272, v240\n\t"
    "v_mul_u32_u24 v246, 272, v241\n\t"
    "v_mfma_f32_16x16x32_bf16 v[96:99], a[176:179], v[144:147], v[96:99]\n\t"
    "s_lshr_b32 s71, %1, 1\n\t"
    "v_lshl_add_u32 v196, v241, 3, v196\n\t"
    "v_lshl_add_u32 v246, v240, 4, v246\n\t"
    "v_mfma_f32_16x16x32_bf16 v[112:115], a[184:187], v[144:147], v[112:115]\n\t"
    "s_lshl_b32 s71, s71, 16\n\t"
    "v_mfma_f32_16x16x32_bf16 v[180:183], v[220:223], v[148:151], v[180:183]\n\t"
    "s_add_u32 s68, s68, s71\n\t"
    "v_mfma_f32_16x16x32_bf16 v[4:7], a[128:131], v[148:151], v[4:7]\n\t"
    "s_add_u32 s68, s68, 0x8000\n\t"
    "v_add_u32 v196, s68, v196\n\t"
    "v_add_u32 v246, s68, v246\n\t"
    "v_mfma_f32_16x16x32_bf16 v[20:23], a[136:139], v[148:151], v[20:23]\n\t"
    "s_lshl_b32 s68, %1, 2\n\t"
    "v_mfma_f32_16x16x32_bf16 v[36:39], a[144:147], v[148:151], v[36:39]\n\t"
    "s_add_u32 s68, s68, 0x20000\n\t"
    "v_mov_b32 v242, s68\n\t"
    "v_mov_b32 v243, 0x20000\n\t"
    "v_mfma_f32_16x16x32_bf16 v[52:55], a[152:155], v[148:151], v[52:55]\n\t"
    "s_add_u32 s71, s89, 4096\n\t"
    "v_add_u32 v252, 2, v255\n\t"
    "v_mfma_f32_16x16x32_bf16 v[68:71], a[160:163], v[148:151], v[68:71]\n\t"
    "v_add_f32 v176, v176, v177\n\t"
    "v_mfma_f32_16x16x32_bf16 v[84:87], a[168:171], v[148:151], v[84:87]\n\t"
    "v_add_f32 v180, v180, v181\n\t"
    "v_add_f32 v240, v176, v180\n\t"
    "v_cmp_ngt_f32 vcc, 0x53800000, v240\n\t"
    "v_mfma_f32_16x16x32_bf16 v[100:103], a[176:179], v[148:151], v[100:103]\n\t"
    "s_cmp_lg_u64 vcc, 0\n\t"
    "s_cselect_b32 s68, 1, s90\n\t"
    "v_mov_b32 v241, s68\n\t"
    "v_mfma_f32_16x16x32_bf16 v[116:119], a[184:187], v[148:151], v[116:119]\n\t"
    "ds_write_b32 v242, v241\n\t"
    "s_waitcnt lgkmcnt(0)\n\t"
    "s_barrier\n\t"
    "ds_read_b128 v[240:243], v243\n\t"
    "s_cmp_lg_u32 s66, 0\n\t"
    "s_cbranch_scc1 GOTB_%=\n\t"
    "s_waitcnt lgkmcnt(0)\n\t"
    "v_or3_b32 v240, v240, v241, v242\n\t"
    "v_or_b32 v240, v240, v243\n\t"
    "s_nop 0\n\t"
    "v_readfirstlane_b32 s68, v240\n\t"
    "s_nop 1\n\t"
    "s_andn2_b32 s68, s68, s67\n\t"
    "s_cbranch_scc1 FB_%=\n\t"
    "s_mov_b32 s68, 0\n\t"
    "PLB_%=:\n\t"
    "buffer_load_dword v249, off, s[48:51], s78 sc1\n\t"
    "s_add_u32 s68, s68, 1\n\t"
    "s_waitcnt vmcnt(0)\n\t"
    "v_cmp_ne_u32 vcc, v249, v254\n\t"
    "s_cmp_gt_u32 s68, 0x400000\n\t"
    "s_cbranch_scc1 PXB_%=\n\t"
    "s_cbranch_vccz PLB_%=\n\t"
    "PXB_%=:\n\t"
    "buffer_load_dwordx4 v[236:239], v248, s[48:51], s69 offen sc1\n\t"
    "buffer_load_dwordx4 v[8:11], v248, s[48:51], s76 offen offset:0 sc1\n\t"
    "buffer_load_dwordx4 v[40:43], v248, s[48:51], s76 offen offset:1024 sc1\n\t"
    "buffer_load_dwordx4 v[72:75], v248, s[48:51], s76 offen offset:2048 sc1\n\t"
    "buffer_load_dwordx4 v[104:107], v248, s[48:51], s76 offen offset:3072 sc1\n\t"
    "buffer_load_dwordx4 v[12:15], v248, s[48:51], s70 offen offset:0 sc1\n\t"
    "buffer_load_dwordx4 v[44:47], v248, s[48:51], s70 offen offset:1024 sc1\n\t"
    "buffer_load_dwordx4 v[76:79], v248, s[48:51], s70 offen offset:2048 sc1\n\t"
    "buffer_load_dwordx4 v[108:111], v248, s[48:51], s70 offen offset:3072 sc1\n\t"
    "GOTB_%=:\n\t"
    "s_waitcnt vmcnt(8)\n\t"
    "v_min_f32 v136, v160, v236\n\t"
    "v_sub_f32 v128, v136, v160\n\t"
    "v_sub_f32 v130, v136, v236\n\t"
    "v_exp_f32 v128, v128\n\t"
    "v_exp_f32 v130, v130\n\t"
    "s_nop 0\n\t"
    "v_mul_f32 v136, v128, v176\n\t"
    "v_fmac_f32 v136, v130, v237\n\t"
    "v_rcp_f32 v136, v136\n\t"
    "s_nop 0\n\t"
    "v_mul_f32 v128, v128, v136\n\t"
    "v_mul_f32 v130, v130, v136\n\t"
    "v_min_f32 v136, v164, v238\n\t"
    "v_sub_f32 v132, v136, v164\n\t"
    "v_sub_f32 v134, v136, v238\n\t"
    "v_exp_f32 v132, v132\n\t"
    "v_exp_f32 v134, v134\n\t"
    "s_nop 0\n\t"
    "v_mul_f32 v136, v132, v180\n\t"
    "v_fmac_f32 v136, v134, v239\n\t"
    "v_rcp_f32 v136, v136\n\t"
    "s_nop 0\n\t"
    "v_mul_f32 v132, v132, v136\n\t"
    "v_mul_f32 v134, v134, v136\n\t"
    "s_waitcnt lgkmcnt(0)\n\t"
    "v_or3_b32 v240, v240, v241, v242\n\t"
    "v_or_b32 v240, v240, v243\n\t"
    "s_nop 0\n\t"
    "v_readfirstlane_b32 s68, v240\n\t"
    "s_nop 1\n\t"
    "s_andn2_b32 s68, s68, s67\n\t"
    "s_cbranch_scc1 FB_%=\n\t"
    "v_pk_mul_f32 v[0:1], v[0:1], v[128:129] op_sel_hi:[1,0]\n\t"
    "v_pk_mul_f32 v[2:3], v[2:3], v[128:129] op_sel_hi:[1,0]\n\t"
    "v_pk_mul_f32 v[16:17], v[16:17], v[128:129] op_sel_hi:[1,0]\n\t"
    "v_pk_mul_f32 v[18:19], v[18:19], v[128:129] op_sel_hi:[1,0]\n\t"
    "s_waitcnt vmcnt(7)\n\t"
    "v_lshlrev_b32 v24, 16, v8\n\t"
    "v_and_b32 v25, 0xffff0000, v8\n\t"
    "v_lshlrev_b32 v26, 16, v9\n\t"
    "v_and_b32 v27, 0xffff0000, v9\n\t"
    "v_pk_fma_f32 v[0:1], v[24:25], v[130:131], v[0:1] op_sel_hi:[1,0,1]\n\t"
    "v_pk_fma_f32 v[2:3], v[26:27], v[130:131], v[2:3] op_sel_hi:[1,0,1]\n\t"
    "v_cvt_pk_bf16_f32 v8, v0, v1\n\t"
    "v_cvt_pk_bf16_f32 v9, v2, v3\n\t"
    "v_lshlrev_b32 v24, 16, v10\n\t"
    "v_and_b32 v25, 0xffff0000, v10\n\t"
    "v_lshlrev_b32 v26, 16, v11\n\t"
    "v_and_b32 v27, 0xffff0000, v11\n\t"
    "v_pk_fma_f32 v[16:17], v[24:25], v[130:131], v[16:17] op_sel_hi:[1,0,1]\n\t"
    "v_pk_fma_f32 v[18:19], v[26:27], v[130:131], v[18:19] op_sel_hi:[1,0,1]\n\t"
    "v_cvt_pk_bf16_f32 v10, v16, v17\n\t"
    "v_cvt_pk_bf16_f32 v11, v18, v19\n\t"
    "ds_write_b64 v196, v[8:9] offset:0\n\t"
    "ds_write_b64 v196, v[10:11] offset:32\n\t"
    "v_pk_mul_f32 v[32:33], v[32:33], v[128:129] op_sel_hi:[1,0]\n\t"
    "v_pk_mul_f32 v[34:35], v[34:35], v[128:129] op_sel_hi:[1,0]\n\t"
    "v_pk_mul_f32 v[48:49], v[48:49], v[128:129] op_sel_hi:[1,0]\n\t"
    "v_pk_mul_f32 v[50:51], v[50:51], v[128:129] op_sel_hi:[1,0]\n\t"
    "s_waitcnt vmcnt(6)\n\t"
    "v_lshlrev_b32 v56, 16, v40\n\t"
    "v_and_b32 v57, 0xffff0000, v40\n\t"
    "v_lshlrev_b32 v58, 16, v41\n\t"
    "v_and_b32 v59, 0xffff0000, v41\n\t"
    "v_pk_fma_f32 v[32:33], v[56:57], v[130:131], v[32:33] op_sel_hi:[1,0,1]\n\t"
    "v_pk_fma_f32 v[34:35], v[58:59], v[130:131], v[34:35] op_sel_hi:[1,0,1]\n\t"
    "v_cvt_pk_bf16_f32 v40, v32, v33\n\t"
    "v_cvt_pk_bf16_f32 v41, v34, v35\n\t"
    "v_lshlrev_b32 v56, 16, v42\n\t"
    "v_and_b32 v57, 0xffff0000, v42\n\t"
    "v_lshlrev_b32 v58, 16, v43\n\t"
    "v_and_b32 v59, 0xffff0000, v43\n\t"
    "v_pk_fma_f32 v[48:49], v[56:57], v[130:131], v[48:49] op_sel_hi:[1,0,1]\n\t"
    "v_pk_fma_f32 v[50:51], v[58:59], v[130:131], v[50:51] op_sel_hi:[1,0,1]\n\t"
    "v_cvt_pk_bf16_f32 v42, v48, v49\n\t"
    "v_cvt_pk_bf16_f32 v43, v50, v51\n\t"
    "ds_write_b64 v196, v[40:41] offset:64\n\t"
    "ds_write_b64 v196, v[42:43] offset:96\n\t"
    "v_pk_mul_f32 v[64:65], v[64:65], v[128:129] op_sel_hi:[1,0]\n\t"
    "v_pk_mul_f32 v[66:67], v[66:67], v[128:129] op_sel_hi:[1,0]\n\t"
    "v_pk_mul_f32 v[80:81], v[80:81], v[128:129] op_sel_hi:[1,0]\n\t"
    "v_pk_mul_f32 v[82:83], v[82:83], v[128:129] op_sel_hi:[1,0]\n\t"
    "s_waitcnt vmcnt(5)\n\t"
    "v_lshlrev_b32 v88, 16, v72\n\t"
    "v_and_b32 v89, 0xffff0000, v72\n\t"
    "v_lshlrev_b32 v90, 16, v73\n\t"
    "v_and_b32 v91, 0xffff0000, v73\n\t"
    "v_pk_fma_f32 v[64:65], v[88:89], v[130:131], v[64:65] op_sel_hi:[1,0,1]\n\t"
    "v_pk_fma_f32 v[66:67], v[90:91], v[130:131], v[66:67] op_sel_hi:[1,0,1]\n\t"
    "v_cvt_pk_bf16_f32 v72, v64, v65\n\t"
    "v_cvt_pk_bf16_f32 v73, v66, v67\n\t"
    "v_lshlrev_b32 v88, 16, v74\n\t"
    "v_and_b32 v89, 0xffff0000, v74\n\t"
    "v_lshlrev_b32 v90, 16, v75\n\t"
    "v_and_b32 v91, 0xffff0000, v75\n\t"
    "v_pk_fma_f32 v[80:81], v[88:89], v[130:131], v[80:81] op_sel_hi:[1,0,1]\n\t"
    "v_pk_fma_f32 v[82:83], v[90:91], v[130:131], v[82:83] op_sel_hi:[1,0,1]\n\t"
    "v_cvt_pk_bf16_f32 v74, v80, v81\n\t"
    "v_cvt_pk_bf16_f32 v75, v82, v83\n\t"
    "ds_write_b64 v196, v[72:73] offset:128\n\t"
    "ds_write_b64 v196, v[74:75] offset:160\n\t"
    "v_pk_mul_f32 v[96:97], v[96:97], v[128:129] op_sel_hi:[1,0]\n\t"
    "v_pk_mul_f32 v[98:99], v[98:99], v[128:129] op_sel_hi:[1,0]\n\t"
    "v_pk_mul_f32 v[112:113], v[112:113], v[128:129] op_sel_hi:[1,0]\n\t"
    "v_pk_mul_f32 v[114:115], v[114:115], v[128:129] op_sel_hi:[1,0]\n\t"
    "s_waitcnt vmcnt(4)\n\t"
    "v_lshlrev_b32 v120, 16, v104\n\t"
    "v_and_b32 v121, 0xffff0000, v104\n\t"
    "v_lshlrev_b32 v122, 16, v105\n\t"
    "v_and_b32 v123, 0xffff0000, v105\n\t"
    "v_pk_fma_f32 v[96:97], v[120:121], v[130:131], v[96:97] op_sel_hi:[1,0,1]\n\t"
    "v_pk_fma_f32 v[98:99], v[122:123], v[130:131], v[98:99] op_sel_hi:[1,0,1]\n\t"
    "v_cvt_pk_bf16_f32 v104, v96, v97\n\t"
    "v_cvt_pk_bf16_f32 v105, v98, v99\n\t"
    "v_lshlrev_b32 v120, 16, v106\n\t"
    "v_and_b32 v121, 0xffff0000, v106\n\t"
    "v_lshlrev_b32 v122, 16, v107\n\t"
    "v_and_b32 v123, 0xffff0000, v107\n\t"
    "v_pk_fma_f32 v[112:113], v[120:121], v[130:131], v[112:113] op_sel_hi:[1,0,1]\n\t"
    "v_pk_fma_f32 v[114:115], v[122:123], v[130:131], v[114:115] op_sel_hi:[1,0,1]\n\t"
    "v_cvt_pk_bf16_f32 v106, v112, v113\n\t"
    "v_cvt_pk_bf16_f32 v107, v114, v115\n\t"
    "ds_write_b64 v196, v[104:105] offset:192\n\t"
    "ds_write_b64 v196, v[106:107] offset:224\n\t"
    "ds_read_b128 v[0:3], v246 offset:0\n\t"
    "ds_read_b128 v[32:35], v246 offset:1088\n\t"
    "ds_read_b128 v[64:67], v246 offset:2176\n\t"
    "ds_read_b128 v[96:99], v246 offset:3264\n\t"
    "v_pk_mul_f32 v[4:5], v[4:5], v[132:133] op_sel_hi:[1,0]\n\t"
    "v_pk_mul_f32 v[6:7], v[6:7], v[132:133] op_sel_hi:[1,0]\n\t"
    "v_pk_mul_f32 v[20:21], v[20:21], v[132:133] op_sel_hi:[1,0]\n\t"
    "v_pk_mul_f32 v[22:23], v[22:23], v[132:133] op_sel_hi:[1,0]\n\t"
    "s_waitcnt vmcnt(3)\n\t"
    "v_lshlrev_b32 v28, 16, v12\n\t"
    "v_and_b32 v29, 0xffff0000, v12\n\t"
    "v_lshlrev_b32 v30, 16, v13\n\t"
    "v_and_b32 v31, 0xffff0000, v13\n\t"
    "v_pk_fma_f32 v[4:5], v[28:29], v[134:135], v[4:5] op_sel_hi:[1,0,1]\n\t"
    "v_pk_fma_f32 v[6:7], v[30:31], v[134:135], v[6:7] op_sel_hi:[1,0,1]\n\t"
    "v_cvt_pk_bf16_f32 v12, v4, v5\n\t"
    "v_cvt_pk_bf16_f32 v13, v6, v7\n\t"
    "v_lshlrev_b32 v28, 16, v14\n\t"
    "v_and_b32 v29, 0xffff0000, v14\n\t"
    "v_lshlrev_b32 v30, 16, v15\n\t"
    "v_and_b32 v31, 0xffff0000, v15\n\t"
    "v_pk_fma_f32 v[20:21], v[28:29], v[134:135], v[20:21] op_sel_hi:[1,0,1]\n\t"
    "v_pk_fma_f32 v[22:23], v[30:31], v[134:135], v[22:23] op_sel_hi:[1,0,1]\n\t"
    "v_cvt_pk_bf16_f32 v14, v20, v21\n\t"
    "v_cvt_pk_bf16_f32 v15, v22, v23\n\t"
    "ds_write_b64 v196, v[12:13] offset:4352\n\t"
    "ds_write_b64 v196, v[14:15] offset:4384\n\t"
    "s_waitcnt lgkmcnt(2)\n\t"
    "buffer_store_dwordx4 v[0:3], v248, s[56:59], s89 offen offset:0 sc1\n\t"
    "buffer_store_dwordx4 v[32:35], v248, s[56:59], s89 offen offset:1024 sc1\n\t"
    "buffer_store_dwordx4 v[64:67], v248, s[56:59], s89 offen offset:2048 sc1\n\t"
    "buffer_store_dwordx4 v[96:99], v248, s[56:59], s89 offen offset:3072 sc1\n\t"
    "v_pk_mul_f32 v[36:37], v[36:37], v[132:133] op_sel_hi:[1,0]\n\t"
    "v_pk_mul_f32 v[38:39], v[38:39], v[132:133] op_sel_hi:[1,0]\n\t"
    "v_pk_mul_f32 v[52:53], v[52:53], v[132:133] op_sel_hi:[1,0]\n\t"
    "v_pk_mul_f32 v[54:55], v[54:55], v[132:133] op_sel_hi:[1,0]\n\t"
    "s_waitcnt vmcnt(6)\n\t"
    "v_lshlrev_b32 v60, 16, v44\n\t"
    "v_and_b32 v61, 0xffff0000, v44\n\t"
    "v_lshlrev_b32 v62, 16, v45\n\t"
    "v_and_b32 v63, 0xffff0000, v45\n\t"
    "v_pk_fma_f32 v[36:37], v[60:61], v[134:135], v[36:37] op_sel_hi:[1,0,1]\n\t"
    "v_pk_fma_f32 v[38:39], v[62:63], v[134:135], v[38:39] op_sel_hi:[1,0,1]\n\t"
    "v_cvt_pk_bf16_f32 v44, v36, v37\n\t"
    "v_cvt_pk_bf16_f32 v45, v38, v39\n\t"
    "v_lshlrev_b32 v60, 16, v46\n\t"
    "v_and_b32 v61, 0xffff0000, v46\n\t"
    "v_lshlrev_b32 v62, 16, v47\n\t"
    "v_and_b32 v63, 0xffff0000, v47\n\t"
    "v_pk_fma_f32 v[52:53], v[60:61], v[134:135], v[52:53] op_sel_hi:[1,0,1]\n\t"
    "v_pk_fma_f32 v[54:55], v[62:63], v[134:135], v[54:55] op_sel_hi:[1,0,1]\n\t"
    "v_cvt_pk_bf16_f32 v46, v52, v53\n\t"
    "v_cvt_pk_bf16_f32 v47, v54, v55\n\t"
    "ds_write_b64 v196, v[44:45] offset:4416\n\t"
    "ds_write_b64 v196, v[46:47] offset:4448\n\t"
    "v_pk_mul_f32 v[68:69], v[68:69], v[132:133] op_sel_hi:[1,0]\n\t"
    "v_pk_mul_f32 v[70:71], v[70:71], v[132:133] op_sel_hi:[1,0]\n\t"
    "v_pk_mul_f32 v[84:85], v[84:85], v[132:133] op_sel_hi:[1,0]\n\t"
    "v_pk_mul_f32 v[86:87], v[86:87], v[132:133] op_sel_hi:[1,0]\n\t"
    "s_waitcnt vmcnt(5)\n\t"
    "v_lshlrev_b32 v92, 16, v76\n\t"
    "v_and_b32 v93, 0xffff0000, v76\n\t"
    "v_lshlrev_b32 v94, 16, v77\n\t"
    "v_and_b32 v95, 0xffff0000, v77\n\t"
    "v_pk_fma_f32 v[68:69], v[92:93], v[134:135], v[68:69] op_sel_hi:[1,0,1]\n\t"
    "v_pk_fma_f32 v[70:71], v[94:95], v[134:135], v[70:71] op_sel_hi:[1,0,1]\n\t"
    "v_cvt_pk_bf16_f32 v76, v68, v69\n\t"
    "v_cvt_pk_bf16_f32 v77, v70, v71\n\t"
    "v_lshlrev_b32 v92, 16, v78\n\t"
    "v_and_b32 v93, 0xffff0000, v78\n\t"
    "v_lshlrev_b32 v94, 16, v79\n\t"
    "v_and_b32 v95, 0xffff0000, v79\n\t"
    "v_pk_fma_f32 v[84:85], v[92:93], v[134:135], v[84:85] op_sel_hi:[1,0,1]\n\t"
    "v_pk_fma_f32 v[86:87], v[94:95], v[134:135], v[86:87] op_sel_hi:[1,0,1]\n\t"
    "v_cvt_pk_bf16_f32 v78, v84, v85\n\t"
    "v_cvt_pk_bf16_f32 v79, v86, v87\n\t"
    "ds_write_b64 v196, v[76:77] offset:4480\n\t"
    "ds_write_b64 v196, v[78:79] offset:4512\n\t"
    "v_pk_mul_f32 v[100:101], v[100:101], v[132:133] op_sel_hi:[1,0]\n\t"
    "v_pk_mul_f32 v[102:103], v[102:103], v[132:133] op_sel_hi:[1,0]\n\t"
    "v_pk_mul_f32 v[116:117], v[116:117], v[132:133] op_sel_hi:[1,0]\n\t"
    "v_pk_mul_f32 v[118:119], v[118:119], v[132:133] op_sel_hi:[1,0]\n\t"
    "s_waitcnt vmcnt(4)\n\t"
    "v_lshlrev_b32 v124, 16, v108\n\t"
    "v_and_b32 v125, 0xffff0000, v108\n\t"
    "v_lshlrev_b32 v126, 16, v109\n\t"
    "v_and_b32 v127, 0xffff0000, v109\n\t"
    "v_pk_fma_f32 v[100:101], v[124:125], v[134:135], v[100:101] op_sel_hi:[1,0,1]\n\t"
    "v_pk_fma_f32 v[102:103], v[126:127], v[134:135], v[102:103] op_sel_hi:[1,0,1]\n\t"
    "v_cvt_pk_bf16_f32 v108, v100, v101\n\t"
    "v_cvt_pk_bf16_f32 v109, v102, v103\n\t"
    "v_lshlrev_b32 v124, 16, v110\n\t"
    "v_and_b32 v125, 0xffff0000, v110\n\t"
    "v_lshlrev_b32 v126, 16, v111\n\t"
    "v_and_b32 v127, 0xffff0000, v111\n\t"
    "v_pk_fma_f32 v[116:117], v[124:125], v[134:135], v[116:117] op_sel_hi:[1,0,1]\n\t"
    "v_pk_fma_f32 v[118:119], v[126:127], v[134:135], v[118:119] op_sel_hi:[1,0,1]\n\t"
    "v_cvt_pk_bf16_f32 v110, v116, v117\n\t"
    "v_cvt_pk_bf16_f32 v111, v118, v119\n\t"
    "ds_write_b64 v196, v[108:109] offset:4544\n\t"
    "ds_write_b64 v196, v[110:111] offset:4576\n\t"
    "ds_read_b128 v[4:7], v246 offset:4352\n\t"
    "ds_read_b128 v[36:39], v246 offset:5440\n\t"
    "ds_read_b128 v[68:71], v246 offset:6528\n\t"
    "ds_read_b128 v[100:103], v246 offset:7616\n\t"
    "s_waitcnt lgkmcnt(3)\n\t"
    "buffer_store_dwordx4 v[4:7], v248, s[56:59], s71 offen offset:0 sc1\n\t"
    "s_waitcnt lgkmcnt(2)\n\t"
    "buffer_store_dwordx4 v[36:39], v248, s[56:59], s71 offen offset:1024 sc1\n\t"
    "s_waitcnt lgkmcnt(1)\n\t"
    "buffer_store_dwordx4 v[68:71], v248, s[56:59], s71 offen offset:2048 sc1\n\t"
    "s_waitcnt lgkmcnt(0)\n\t"
    "buffer_store_dwordx4 v[100:103], v248, s[56:59], s71 offen offset:3072 sc1\n\t"
    "buffer_store_dword v252, off, s[48:51], s77 sc1\n\t"
    "s_endpgm\n\t"
    "FB_%=:\n\t"
    "s_waitcnt vmcnt(0)\n\t"
    "s_mov_b32 s67, 1\n\t"
    "s_mov_b64 s[92:93], 0\n\t"
    "s_lshl_b32 s69, s86, 1\n\t"
    "s_add_u32 s69, s69, s87\n\t"
    "s_mov_b64 s[36:37], s[18:19]\n\t"
    "s_lshl_b32 s68, s69, 18\n\t"
    "s_add_u32 s36, s36, s68\n\t"
    "s_addc_u32 s37, s37, 0\n\t"
    "s_and_b32 s37, s37, 0xffff\n\t"
    "s_mov_b32 s38, 262144\n\t"
    "s_mov_b32 s39, 0x20000\n\t"
    "v_and_b32 v236, 15, v203\n\t"
    "v_lshlrev_b32 v236, 8, v236\n\t"
    "v_lshrrev_b32 v237, 4, v203\n\t"
    "v_lshl_add_u32 v236, v237, 4, v236\n\t"
    "v_mov_b32 v16, 0xff800000\n\t"
    "v_mov_b32 v17, 0xff800000\n\t"
    "v_mov_b32 v18, 0xff800000\n\t"
    "v_mov_b32 v19, 0xff800000\n\t"
    "s_mov_b32 s68, 0\n\t"
    "11:\n\t"
    "buffer_load_dwordx4 v[32:35], v236, s[36:39], s68 offen offset:0\n\t"
    "buffer_load_dwordx4 v[36:39], v236, s[36:39], s68 offen offset:64\n\t"
    "buffer_load_dwordx4 v[40:43], v236, s[36:39], s68 offen offset:128\n\t"
    "buffer_load_dwordx4 v[44:47], v236, s[36:39], s68 offen offset:192\n\t"
    "s_waitcnt vmcnt(0)\n\t"
    "v_mfma_f32_16x16x32_bf16 v[0:3], v[32:35], a[0:3], 0\n\t"
    "v_mfma_f32_16x16x32_bf16 v[0:3], v[36:39], a[4:7], v[0:3]\n\t"
    "v_mfma_f32_16x16x32_bf16 v[0:3], v[40:43], a[8:11], v[0:3]\n\t"
    "v_mfma_f32_16x16x32_bf16 v[0:3], v[44:47], a[12:15], v[0:3]\n\t"
    "v_mfma_f32_16x16x32_bf16 v[4:7], v[32:35], a[16:19], 0\n\t"
    "v_mfma_f32_16x16x32_bf16 v[4:7], v[36:39], a[20:23], v[4:7]\n\t"
    "v_mfma_f32_16x16x32_bf16 v[4:7], v[40:43], a[24:27], v[4:7]\n\t"
    "v_mfma_f32_16x16x32_bf16 v[4:7], v[44:47], a[28:31], v[4:7]\n\t"
    "v_mfma_f32_16x16x32_bf16 v[8:11], v[32:35], a[32:35], 0\n\t"
    "v_mfma_f32_16x16x32_bf16 v[8:11], v[36:39], a[36:39], v[8:11]\n\t"
    "v_mfma_f32_16x16x32_bf16 v[8:11], v[40:43], a[40:43], v[8:11]\n\t"
    "v_mfma_f32_16x16x32_bf16 v[8:11], v[44:47], a[44:47], v[8:11]\n\t"
    "v_mfma_f32_16x16x32_bf16 v[12:15], v[32:35], a[48:51], 0\n\t"
    "v_mfma_f32_16x16x32_bf16 v[12:15], v[36:39], a[52:55], v[12:15]\n\t"
    "v_mfma_f32_16x16x32_bf16 v[12:15], v[40:43], a[56:59], v[12:15]\n\t"
    "v_mfma_f32_16x16x32_bf16 v[12:15], v[44:47], a[60:63], v[12:15]\n\t"
    "s_nop 7\n\t"
    "v_max3_f32 v16, v16, v0, v1\n\t"
    "v_max3_f32 v16, v16, v2, v3\n\t"
    "v_max3_f32 v17, v17, v4, v5\n\t"
    "v_max3_f32 v17, v17, v6, v7\n\t"
    "v_max3_f32 v18, v18, v8, v9\n\t"
    "v_max3_f32 v18, v18, v10, v11\n\t"
    "v_max3_f32 v19, v19, v12, v13\n\t"
    "v_max3_f32 v19, v19, v14, v15\n\t"
    "s_add_u32 s68, s68, 4096\n\t"
    "s_cmp_lt_u32 s68, 0x40000\n\t"
    "s_cbranch_scc1 11b\n\t"
    "s_mov_b64 s[36:37], s[60:61]\n\t"
    "s_mov_b32 s38, 8192\n\t"
    "s_mov_b32 s68, 0\n\t"
    "buffer_load_dwordx4 v[32:35], v236, s[36:39], s68 offen offset:0\n\t"
    "buffer_load_dwordx4 v[36:39], v236, s[36:39], s68 offen offset:64\n\t"
    "buffer_load_dwordx4 v[40:43], v236, s[36:39], s68 offen offset:128\n\t"
    "buffer_load_dwordx4 v[44:47], v236, s[36:39], s68 offen offset:192\n\t"
    "s_waitcnt vmcnt(0)\n\t"
    "v_mfma_f32_16x16x32_bf16 v[0:3], v[32:35], a[0:3], v[204:207]\n\t"
    "v_mfma_f32_16x16x32_bf16 v[0:3], v[36:39], a[4:7], v[0:3]\n\t"
    "v_mfma_f32_16x16x32_bf16 v[0:3], v[40:43], a[8:11], v[0:3]\n\t"
    "v_mfma_f32_16x16x32_bf16 v[0:3], v[44:47], a[12:15], v[0:3]\n\t"
    "v_mfma_f32_16x16x32_bf16 v[4:7], v[32:35], a[16:19], v[212:215]\n\t"
    "v_mfma_f32_16x16x32_bf16 v[4:7], v[36:39], a[20:23], v[4:7]\n\t"
    "v_mfma_f32_16x16x32_bf16 v[4:7], v[40:43], a[24:27], v[4:7]\n\t"
    "v_mfma_f32_16x16x32_bf16 v[4:7], v[44:47], a[28:31], v[4:7]\n\t"
    "v_mfma_f32_16x16x32_bf16 v[8:11], v[32:35], a[32:35], v[204:207]\n\t"
    "v_mfma_f32_16x16x32_bf16 v[8:11], v[36:39], a[36:39], v[8:11]\n\t"
    "v_mfma_f32_16x16x32_bf16 v[8:11], v[40:43], a[40:43], v[8:11]\n\t"
    "v_mfma_f32_16x16x32_bf16 v[8:11], v[44:47], a[44:47], v[8:11]\n\t"
    "v_mfma_f32_16x16x32_bf16 v[12:15], v[32:35], a[48:51], v[212:215]\n\t"
    "v_mfma_f32_16x16x32_bf16 v[12:15], v[36:39], a[52:55], v[12:15]\n\t"
    "v_mfma_f32_16x16x32_bf16 v[12:15], v[40:43], a[56:59], v[12:15]\n\t"
    "v_mfma_f32_16x16x32_bf16 v[12:15], v[44:47], a[60:63], v[12:15]\n\t"
    "s_nop 7\n\t"
    "v_max3_f32 v16, v16, v0, v1\n\t"
    "v_max3_f32 v16, v16, v2, v3\n\t"
    "v_max3_f32 v17, v17, v4, v5\n\t"
    "v_max3_f32 v17, v17, v6, v7\n\t"
    "v_max3_f32 v18, v18, v8, v9\n\t"
    "v_max3_f32 v18, v18, v10, v11\n\t"
    "v_max3_f32 v19, v19, v12, v13\n\t"
    "v_max3_f32 v19, v19, v14, v15\n\t"
    "s_mov_b32 s68, 4096\n\t"
    "buffer_load_dwordx4 v[32:35], v236, s[36:39], s68 offen offset:0\n\t"
    "buffer_load_dwordx4 v[36:39], v236, s[36:39], s68 offen offset:64\n\t"
    "buffer_load_dwordx4 v[40:43], v236, s[36:39], s68 offen offset:128\n\t"
    "buffer_load_dwordx4 v[44:47], v236, s[36:39], s68 offen offset:192\n\t"
    "s_waitcnt vmcnt(0)\n\t"
    "v_mfma_f32_16x16x32_bf16 v[0:3], v[32:35], a[0:3], v[208:211]\n\t"
    "v_mfma_f32_16x16x32_bf16 v[0:3], v[36:39], a[4:7], v[0:3]\n\t"
    "v_mfma_f32_16x16x32_bf16 v[0:3], v[40:43], a[8:11], v[0:3]\n\t"
    "v_mfma_f32_16x16x32_bf16 v[0:3], v[44:47], a[12:15], v[0:3]\n\t"
    "v_mfma_f32_16x16x32_bf16 v[4:7], v[32:35], a[16:19], v[216:219]\n\t"
    "v_mfma_f32_16x16x32_bf16 v[4:7], v[36:39], a[20:23], v[4:7]\n\t"
    "v_mfma_f32_16x16x32_bf16 v[4:7], v[40:43], a[24:27], v[4:7]\n\t"
    "v_mfma_f32_16x16x32_bf16 v[4:7], v[44:47], a[28:31], v[4:7]\n\t"
    "v_mfma_f32_16x16x32_bf16 v[8:11], v[32:35], a[32:35], v[208:211]\n\t"
    "v_mfma_f32_16x16x32_bf16 v[8:11], v[36:39], a[36:39], v[8:11]\n\t"
    "v_mfma_f32_16x16x32_bf16 v[8:11], v[40:43], a[40:43], v[8:11]\n\t"
    "v_mfma_f32_16x16x32_bf16 v[8:11], v[44:47], a[44:47], v[8:11]\n\t"
    "v_mfma_f32_16x16x32_bf16 v[12:15], v[32:35], a[48:51], v[216:219]\n\t"
    "v_mfma_f32_16x16x32_bf16 v[12:15], v[36:39], a[52:55], v[12:15]\n\t"
    "v_mfma_f32_16x16x32_bf16 v[12:15], v[40:43], a[56:59], v[12:15]\n\t"
    "v_mfma_f32_16x16x32_bf16 v[12:15], v[44:47], a[60:63], v[12:15]\n\t"
    "s_nop 7\n\t"
    "v_max3_f32 v16, v16, v0, v1\n\t"
    "v_max3_f32 v16, v16, v2, v3\n\t"
    "v_max3_f32 v17, v17, v4, v5\n\t"
    "v_max3_f32 v17, v17, v6, v7\n\t"
    "v_max3_f32 v18, v18, v8, v9\n\t"
    "v_max3_f32 v18, v18, v10, v11\n\t"
    "v_max3_f32 v19, v19, v12, v13\n\t"
    "v_max3_f32 v19, v19, v14, v15\n\t"
    "v_mov_b32 v20, v16\n\t"
    "s_nop 1\n\t"
    "v_permlane16_swap_b32 v16, v20\n\t"
    "v_max_f32 v16, v16, v20\n\t"
    "v_mov_b32 v20, v16\n\t"
    "s_nop 1\n\t"
    "v_permlane32_swap_b32 v16, v20\n\t"
    "v_max_f32 v16, v16, v20\n\t"
    "v_sub_f32 v160, 0, v16\n\t"
    "v_sub_f32 v161, 0, v16\n\t"
    "v_sub_f32 v162, 0, v16\n\t"
    "v_sub_f32 v163, 0, v16\n\t"
    "v_mov_b32 v20, v17\n\t"
    "s_nop 1\n\t"
    "v_permlane16_swap_b32 v17, v20\n\t"
    "v_max_f32 v17, v17, v20\n\t"
    "v_mov_b32 v20, v17\n\t"
    "s_nop 1\n\t"
    "v_permlane32_swap_b32 v17, v20\n\t"
    "v_max_f32 v17, v17, v20\n\t"
    "v_sub_f32 v164, 0, v17\n\t"
    "v_sub_f32 v165, 0, v17\n\t"
    "v_sub_f32 v166, 0, v17\n\t"
    "v_sub_f32 v167, 0, v17\n\t"
    "v_mov_b32 v20, v18\n\t"
    "s_nop 1\n\t"
    "v_permlane16_swap_b32 v18, v20\n\t"
    "v_max_f32 v18, v18, v20\n\t"
    "v_mov_b32 v20, v18\n\t"
    "s_nop 1\n\t"
    "v_permlane32_swap_b32 v18, v20\n\t"
    "v_max_f32 v18, v18, v20\n\t"
    "v_sub_f32 v168, 0, v18\n\t"
    "v_sub_f32 v169, 0, v18\n\t"
    "v_sub_f32 v170, 0, v18\n\t"
    "v_sub_f32 v171, 0, v18\n\t"
    "v_mov_b32 v20, v19\n\t"
    "s_nop 1\n\t"
    "v_permlane16_swap_b32 v19, v20\n\t"
    "v_max_f32 v19, v19, v20\n\t"
    "v_mov_b32 v20, v19\n\t"
    "s_nop 1\n\t"
    "v_permlane32_swap_b32 v19, v20\n\t"
    "v_max_f32 v19, v19, v20\n\t"
    "v_sub_f32 v172, 0, v19\n\t"
    "v_sub_f32 v173, 0, v19\n\t"
    "v_sub_f32 v174, 0, v19\n\t"
    "v_sub_f32 v175, 0, v19\n\t"
    "s_barrier\n\t"
    "s_branch 1b\n\t"
    "CE_%=:\n\t"
    :: "s"(__builtin_amdgcn_kernarg_segment_ptr()), "s"(wid), "s"(blockIdx.x), "s"((unsigned)(size_t)lds)
    : "v0","v1","v2","v3","v4","v5","v6","v7","v8","v9","v10","v11","v12","v13","v14","v15","v16","v17","v18","v19","v20","v21","v22","v23","v24","v25","v26","v27","v28","v29","v30","v31","v32","v33","v34","v35","v36","v37","v38","v39","v40","v41","v42","v43","v44","v45","v46","v47","v48","v49","v50","v51","v52","v53","v54","v55","v56","v57","v58","v59","v60","v61","v62","v63","v64","v65","v66","v67","v68","v69","v70","v71","v72","v73","v74","v75","v76","v77","v78","v79","v80","v81","v82","v83","v84","v85","v86","v87","v88","v89","v90","v91","v92","v93","v94","v95","v96","v97","v98","v99","v100","v101","v102","v103","v104","v105","v106","v107","v108","v109","v110","v111","v112","v113","v114","v115","v116","v117","v118","v119","v120","v121","v122","v123","v124","v125","v126","v127","v128","v129","v130","v131","v132","v133","v134","v135","v136","v137","v138","v139","v140","v141","v142","v143","v144","v145","v146","v147","v148","v149","v150","v151","v152","v153","v154","v155","v156","v157","v158","v159","v160","v161","v162","v163","v164","v165","v166","v167","v168","v169","v170","v171","v172","v173","v174","v175","v176","v177","v178","v179","v180","v181","v182","v183","v184","v185","v186","v187","v188","v189","v190","v191","v192","v193","v194","v195","v196","v197","v198","v199","v200","v201","v202","v203","v204","v205","v206","v207","v208","v209","v210","v211","v212","v213","v214","v215","v216","v217","v218","v219","v220","v221","v222","v223","v224","v225","v226","v227","v228","v229","v230","v231","v232","v233","v234","v235","v236","v237","v238","v239","v240","v241","v242","v243","v244","v245","v246","v247","v248","v249","v250","v251","v252","v253","v254","v255","a0","a1","a2","a3","a4","a5","a6","a7","a8","a9","a10","a11","a12","a13","a14","a15","a16","a17","a18","a19","a20","a21","a22","a23","a24","a25","a26","a27","a28","a29","a30","a31","a32","a33","a34","a35","a36","a37","a38","a39","a40","a41","a42","a43","a44","a45","a46","a47","a48","a49","a50","a51","a52","a53","a54","a55","a56","a57","a58","a59","a60","a61","a62","a63","a64","a65","a66","a67","a68","a69","a70","a71","a72","a73","a74","a75","a76","a77","a78","a79","a80","a81","a82","a83","a84","a85","a86","a87","a88","a89","a90","a91","a92","a93","a94","a95","a96","a97","a98","a99","a100","a101","a102","a103","a104","a105","a106","a107","a108","a109","a110","a111","a112","a113","a114","a115","a116","a117","a118","a119","a120","a121","a122","a123","a124","a125","a126","a127","a128","a129","a130","a131","a132","a133","a134","a135","a136","a137","a138","a139","a140","a141","a142","a143","a144","a145","a146","a147","a148","a149","a150","a151","a152","a153","a154","a155","a156","a157","a158","a159","a160","a161","a162","a163","a164","a165","a166","a167","a168","a169","a170","a171","a172","a173","a174","a175","a176","a177","a178","a179","a180","a181","a182","a183","a184","a185","a186","a187","a188","a189","a190","a191","a192","a193","a194","a195","a196","a197","a198","a199","a200","a201","a202","a203","a204","a205","a206","a207","a208","a209","a210","a211","a212","a213","a214","a215","a216","a217","a218","a219","a220","a221","a222","a223","a224","a225","a226","a227","a228","a229","a230","a231","a232","a233","a234","a235","a236","a237","a238","a239","a240","a241","a242","a243","a244","a245","a246","a247","a248","a249","a250","a251","a252","a253","a254","a255","s16","s17","s18","s19","s20","s21","s22","s23","s24","s25","s26","s27","s28","s29","s30","s31","s36","s37","s38","s39","s40","s41","s42","s43","s44","s45","s46","s47","s48","s49","s50","s51","s52","s53","s54","s55","s56","s57","s58","s59","s60","s61","s62","s63","s64","s65","s66","s67","s68","s69","s70","s71","s72","s73","s74","s75","s76","s77","s78","s79","s80","s81","s82","s83","s84","s85","s86","s87","s88","s89","s90","s91","s92","s93","s94","s95","s96","s97","s98","s99","vcc","scc","memory");
  asm volatile("" :: "s"(O), "s"(ws) : "memory");
}

extern "C" void kernel_launch(void* const* d_in, const int* in_sizes, int n_in, void* d_out, int out_size, void* d_ws, size_t ws_size, hipStream_t stream) {
  hipLaunchKernelGGL(tree_attn_kernel, dim3(256), dim3(256), 0, stream, (const char*)d_in[0], (const char*)d_in[1], (const char*)d_in[2],
                     (const char*)d_in[3], (const char*)d_in[4], (const char*)d_in[5], (char*)d_out, (char*)d_ws);
}
```
